```python
import torch

def _softmax(x, axis=-1):
    return torch.softmax(x, dim=axis)
def _gelu(x):
    return torch.nn.functional.gelu(x, approximate="tanh")
def _silu(x):
    return torch.nn.functional.silu(x)
def _sigmoid(x):
    return torch.sigmoid(x)
def _layernorm(x, axis=-1, eps=1e-5):
    m = x.mean(dim=axis, keepdim=True)
    v = x.var(dim=axis, keepdim=True, unbiased=False)
    return (x - m) / torch.sqrt(v + eps)
def _scatter_add(tgt, idx, src, axis=0):
    return tgt.clone().index_add_(axis, idx, src)
def _segment_sum(src, seg, n):
    out = torch.zeros((n,) + src.shape[1:], dtype=src.dtype)
    return out.index_add_(0, seg, src)
def _pad(x, pw):
    flat = tuple(v for pair in reversed(pw) for v in pair)
    return torch.nn.functional.pad(x, flat)

kernel_name = 'interleave_layers__ssm_conv_s4d__dense_activation_relu__prompt'

def setup_inputs():

    _w = (lambda a: (lambda t: t.to(torch.bfloat16) if t.is_floating_point() else t)(torch.as_tensor(a)))
    def _A_setup():
        Bsz, L, D, N = 1, 2048, 512, 128
        return {
            "u": _w((lambda *s: torch.randn(*s, dtype=torch.bfloat16) * 0.05)(Bsz, L, D)),
            "log_A_real": _w((lambda *s: torch.randn(*s, dtype=torch.bfloat16) * 3.2)(D, N) * 0.5),
            "A_imag": _w((lambda *s: torch.randn(*s, dtype=torch.bfloat16) * 0.2)(D, N) * 3.14),
            "C_real": _w((lambda *s: torch.randn(*s, dtype=torch.bfloat16) * 0.2)(D, N) / N ** 0.5),
            "C_imag": _w((lambda *s: torch.randn(*s, dtype=torch.bfloat16) * 0.2)(D, N) / N ** 0.5),
            "log_dt": _w((lambda *s: torch.randn(*s, dtype=torch.bfloat16) * 0.2)(D) * 0.5 - 4.0),
            "D_skip": _w((lambda *s: torch.randn(*s, dtype=torch.bfloat16) * 0.2)(D)),
        }
    _A_in = _A_setup()

    def _B_setup():
        B, S, D, Dff = 1, 2048, 512, 1792
        return {
            "x": _w((lambda *s: torch.randn(*s, dtype=torch.bfloat16) * 0.05)(B, S, D)),
            "w1": _w((lambda *s: torch.randn(*s, dtype=torch.bfloat16) * 0.05)(D, Dff)),
            "w2": _w((lambda *s: torch.randn(*s, dtype=torch.bfloat16) * 0.05)(Dff, D)),
        }
    _B_in = _B_setup()

    return {
        "A_u": _A_in["u"],
        "A_log_A_real": _A_in["log_A_real"],
        "A_A_imag": _A_in["A_imag"],
        "A_C_real": _A_in["C_real"],
        "A_C_imag": _A_in["C_imag"],
        "A_log_dt": _A_in["log_dt"],
        "A_D_skip": _A_in["D_skip"],
        "B_x": _B_in["x"],
        "B_w1": _B_in["w1"],
        "B_w2": _B_in["w2"],
    }

def reference(A_u, A_log_A_real, A_A_imag, A_C_real, A_C_imag, A_log_dt, A_D_skip, B_x, B_w1, B_w2):
    def _A_ref(u, log_A_real, A_imag, C_real, C_imag, log_dt, D_skip):
        L = u.shape[1]
        dt = torch.exp(log_dt)
        A = -torch.exp(log_A_real) + 1j * A_imag
        C = (C_real + 1j * C_imag) * dt[:, None]
        pos = (lambda x, dt: x.to(dt))(torch.arange(L), log_dt.dtype)
        P = torch.exp((A * dt[:, None])[:, :, None] * pos[None, None, :])
        K = 2.0 * torch.einsum("dn,dnl->dl", C, P).real
        L2 = 2 * L
        u_t = (lambda x, *d: x.permute(*d))(u, 0, 2, 1)
        Kf = (lambda x, *a, **k: torch.fft.rfft(x.float(), *a, **k))(K, L2)
        Uf = (lambda x, *a, **k: torch.fft.rfft(x.float(), *a, **k))(u_t, L2)
        y = (lambda x, *a, **k: torch.fft.irfft(x, *a, **k).to(torch.bfloat16))(Uf * Kf[None, :, :], L2)[:, :, :L]
        y = (lambda x, *d: x.permute(*d))(y, 0, 2, 1)
        return y + u * D_skip[None, None, :]

    def _B_ref(x, w1, w2):
        h = torch.einsum("bsd,df->bsf", x, w1)
        zero = torch.zeros_like(h)
        h = torch.maximum(h, zero)
        return torch.einsum("bsf,fd->bsd", h, w2)

    x = A_u
    x = x + _A_ref(_layernorm(x, axis=-1), A_log_A_real, A_A_imag, A_C_real, A_C_imag, A_log_dt, A_D_skip)
    x = x + _B_ref(_layernorm(x, axis=-1), B_w1, B_w2)
    x = x + _A_ref(_layernorm(x, axis=-1), A_log_A_real, A_A_imag, A_C_real, A_C_imag, A_log_dt, A_D_skip)
    x = x + _B_ref(_layernorm(x, axis=-1), B_w1, B_w2)
    return x
```

```cpp
#include <hip/hip_runtime.h>
#include <hip/hip_bf16.h>

typedef unsigned short u16;
typedef short s4 __attribute__((ext_vector_type(4)));
typedef short s8 __attribute__((ext_vector_type(8)));
typedef _Float16 h8 __attribute__((ext_vector_type(8)));
typedef _Float16 h2 __attribute__((ext_vector_type(2)));
typedef __bf16 b2 __attribute__((ext_vector_type(2)));
typedef __bf16 b8 __attribute__((ext_vector_type(8)));
typedef float f4 __attribute__((ext_vector_type(4)));
typedef unsigned u4 __attribute__((ext_vector_type(4)));
typedef unsigned u2 __attribute__((ext_vector_type(2)));
typedef __attribute__((address_space(3))) s4* lds_s4p;

constexpr int SL = 2048;
constexpr int DM = 512;
constexpr int NS = 128;
constexpr int FF = 1792;
#define KTERMS 2

constexpr size_t WS_XT0 = 0;
constexpr size_t WS_XT1 = WS_XT0 + (size_t)SL * DM * 2;
constexpr size_t WS_XT2 = WS_XT1 + (size_t)SL * DM * 2;
constexpr size_t WS_XT3 = WS_XT2 + (size_t)SL * DM * 2;
constexpr size_t WS_KH  = WS_XT3 + (size_t)SL * DM * 2;
constexpr size_t WS_KL  = WS_KH + (size_t)SL * DM * 2;
constexpr size_t WS_W1F = WS_KL + (size_t)SL * DM * 2;
constexpr size_t WS_W2F = WS_W1F + (size_t)DM * FF * 2;
constexpr size_t WS_HF  = WS_W2F + (size_t)DM * FF * 2;
constexpr size_t WS_ST0 = WS_HF + (size_t)SL * FF * 2;
constexpr size_t WS_ST2 = WS_ST0 + (size_t)SL * 8;
constexpr size_t WS_C1  = WS_ST2 + (size_t)SL * 8;
constexpr size_t WS_KSC = WS_C1 + (size_t)FF * 4;

__device__ __forceinline__ float bf2f(unsigned h) { return __uint_as_float(h << 16); }
__device__ __forceinline__ float bflo(unsigned w) { return __uint_as_float(w << 16); }
__device__ __forceinline__ float bfhi(unsigned w) { return __uint_as_float(w & 0xffff0000u); }
__device__ __forceinline__ unsigned pack_bf(float a, float b) {
  b2 v = {(__bf16)a, (__bf16)b};
  return __builtin_bit_cast(unsigned, v);
}
__device__ __forceinline__ float rbf(float f) {
  return __uint_as_float(((unsigned)__builtin_bit_cast(u16, (__bf16)f)) << 16);
}
__device__ __forceinline__ unsigned pack_h(float a, float b) {
  h2 v = {(_Float16)a, (_Float16)b};
  return __builtin_bit_cast(unsigned, v);
}
__device__ __forceinline__ float dot2(unsigned a, unsigned b, float c) {
  return __builtin_amdgcn_fdot2_f32_bf16(__builtin_bit_cast(b2, a), __builtin_bit_cast(b2, b), c, false);
}
__device__ __forceinline__ s4 tr_read(const char* p) {
  return __builtin_amdgcn_ds_read_tr16_b64_v4i16((lds_s4p)p);
}
__device__ __forceinline__ s8 cat8(s4 a, s4 b) { return __builtin_shufflevector(a, b, 0, 1, 2, 3, 4, 5, 6, 7); }
__device__ __forceinline__ float shr1(float v) {
  return __builtin_bit_cast(float, __builtin_amdgcn_update_dpp(0, __builtin_bit_cast(int, v), 0x138, 0xf, 0xf, true));
}
constexpr unsigned BF_ONES = 0x3f803f80u;

__device__ __forceinline__ void ln_consts(float s, float ss, float& mb, float& rinv) {
  float mean = s * (1.0f / DM);
  float var = fmaxf(ss * (1.0f / DM) - mean * mean, 0.0f);
  mb = rbf(mean);
  float sd = rbf(sqrtf(rbf(rbf(var) + 1e-5f)));
  rinv = 1.0f / sd;
}

constexpr int PREP_KGEN = 512, PREP_TR = 64, PREP_W1 = 28, PREP_W2 = 32;

__device__ __forceinline__ int alpha_of(int idx) {
  if (idx < 16) return (idx >> 3) * 128 + (idx & 7);
  if (idx < 24) { int j = idx - 16; return (2 + (j >> 2)) * 128 + (j & 3) * 2; }
  if (idx < 32) { int j = idx - 24; return (4 + (j >> 1)) * 128 + (j & 1) * 4; }
  if (idx < 41) return (8 + idx - 32) * 128;
  return 0;
}

__global__ __launch_bounds__(256) void prep_kernel(
    const u16* __restrict__ x, const u16* __restrict__ logA, const u16* __restrict__ Aim,
    const u16* __restrict__ Cre, const u16* __restrict__ Cim, const u16* __restrict__ logdt,
    const u16* __restrict__ w1, const u16* __restrict__ w2, char* __restrict__ ws) {
  __shared__ __attribute__((aligned(16))) char smem[32768];
  const int tid = threadIdx.x, lane = tid & 63, wid = tid >> 6;
  int b = blockIdx.x;
  if (b < PREP_KGEN) {
    const int d = b;
    f4* par = (f4*)smem;
    float2* parc = (float2*)(smem + 2048);
    f4* red = (f4*)(smem + 4096);
    float* kv = (float*)(smem + 4096 + 12288);
    float* mx = (float*)(smem + 4096 + 12288 + 3072);
    if (tid < NS) {
      float dt = rbf(expf(bf2f(logdt[d])));
      float Ar = -rbf(expf(bf2f(logA[d * NS + tid])));
      float ar = Ar * dt;
      float ai = bf2f(Aim[d * NS + tid]) * dt;
      const float I2PH = 0.15915494f;
      const float I2PL = -6.5578136e-10f * 0.0f + (float)(0.15915494309189535 - (double)0.15915494f);
      float fhi = ai * I2PH;
      float flo = fmaf(ai, I2PH, -fhi) + ai * I2PL;
      par[tid] = f4{ar * 1.44269504f, fhi, flo, 0.f};
      parc[tid] = make_float2(2.0f * bf2f(Cre[d * NS + tid]) * dt, 2.0f * bf2f(Cim[d * NS + tid]) * dt);
    }
    __syncthreads();
    const int idx = lane & 15, c = (lane >> 4) & 1, nh = lane >> 5;
    const float pb = (float)(8 * idx);
    const float pa0 = (float)alpha_of(idx), pa1 = (float)alpha_of(16 + idx), pa2 = (float)alpha_of(32 + idx);
    const float ph = c ? 0.25f : 0.0f;
    f4 acc0 = {0, 0, 0, 0}, acc1 = {0, 0, 0, 0}, acc2 = {0, 0, 0, 0};
#pragma unroll 4
    for (int s = 0; s < 16; ++s) {
      int n = wid * 32 + 2 * s + nh;
      f4 p = par[n];
      float2 pc = parc[n];
      auto rev = [&](float pos) {
        float t = pos * p[1];
        float e = fmaf(pos, p[1], -t);
        return __builtin_amdgcn_fractf(t) + fmaf(pos, p[2], e);
      };
      float rb = rev(pb);
      float eb = __builtin_amdgcn_exp2f(p[0] * pb);
      float cs = __builtin_amdgcn_cosf(rb), sn = __builtin_amdgcn_sinf(rb);
      float P = c ? pc.y : pc.x, Q = c ? -pc.x : pc.y;
      float bv = eb * (P * cs - Q * sn);
      float a0 = __builtin_amdgcn_exp2f(p[0] * pa0) * __builtin_amdgcn_cosf(rev(pa0) + ph);
      float a1 = __builtin_amdgcn_exp2f(p[0] * pa1) * __builtin_amdgcn_cosf(rev(pa1) + ph);
      float a2 = __builtin_amdgcn_exp2f(p[0] * pa2) * __builtin_amdgcn_cosf(rev(pa2) + ph);
      acc0 = __builtin_amdgcn_mfma_f32_16x16x4f32(a0, bv, acc0, 0, 0, 0);
      acc1 = __builtin_amdgcn_mfma_f32_16x16x4f32(a1, bv, acc1, 0, 0, 0);
      acc2 = __builtin_amdgcn_mfma_f32_16x16x4f32(a2, bv, acc2, 0, 0, 0);
    }
    red[(wid * 3 + 0) * 64 + lane] = acc0;
    red[(wid * 3 + 1) * 64 + lane] = acc1;
    red[(wid * 3 + 2) * 64 + lane] = acc2;
    __syncthreads();
    float lmax = 0.f;
    if (tid < 192) {
      int tile = wid;
      f4 v = red[(0 * 3 + tile) * 64 + lane] + red[(1 * 3 + tile) * 64 + lane] +
             (red[(2 * 3 + tile) * 64 + lane] + red[(3 * 3 + tile) * 64 + lane]);
#pragma unroll
      for (int r = 0; r < 4; ++r) {
        int ai_ = 16 * tile + 4 * (lane >> 4) + r;
        kv[ai_ * 16 + (lane & 15)] = v[r];
        bool valid = (ai_ < 40) || (ai_ == 40 && (lane & 15) == 0);
        if (valid) lmax = fmaxf(lmax, fabsf(v[r]));
      }
    }
#pragma unroll
    for (int o = 32; o > 0; o >>= 1) lmax = fmaxf(lmax, __shfl_xor(lmax, o));
    if (lane == 0) mx[wid] = lmax;
    __syncthreads();
    float m = fmaxf(fmaxf(mx[0], mx[1]), mx[2]);
    int e = (int)((__float_as_uint(m) >> 23) & 0xff) - 127;
    int se = 14 - e;
    se = se > 126 ? 126 : (se < -126 ? -126 : se);
    float sc = __uint_as_float((unsigned)(127 + se) << 23);
    float isc = __uint_as_float((unsigned)(127 - se) << 23);
    if (tid == 0) ((float*)(ws + WS_KSC))[d] = isc;
    float kk[8];
#pragma unroll
    for (int j = 0; j < 8; ++j) {
      int l = tid * 8 + j;
      int p = (int)rbf((float)l);
      int blk = p >> 7, bb = (p & 127) >> 3, rem = p & 7;
      int ai_ = blk < 2 ? 8 * blk + rem : (blk < 4 ? 16 + 4 * (blk - 2) + (rem >> 1) : (blk < 8 ? 24 + 2 * (blk - 4) + (rem >> 2) : 32 + blk - 8));
      kk[j] = kv[ai_ * 16 + bb] * sc;
    }
    u4 hi, lo;
#pragma unroll
    for (int j = 0; j < 4; ++j) {
      _Float16 h0 = (_Float16)kk[2 * j], h1 = (_Float16)kk[2 * j + 1];
      h2 hv = {h0, h1};
      hi[j] = __builtin_bit_cast(unsigned, hv);
      lo[j] = pack_h(kk[2 * j] - (float)h0, kk[2 * j + 1] - (float)h1);
    }
    *(u4*)(ws + WS_KH + ((size_t)d * SL + tid * 8) * 2) = hi;
    if (KTERMS > 1) *(u4*)(ws + WS_KL + ((size_t)d * SL + tid * 8) * 2) = lo;
    return;
  }
  b -= PREP_KGEN;
  if (b < PREP_TR) {
    const int t0 = b * 32;
    u4 v[8];
#pragma unroll
    for (int i = 0; i < 8; ++i) v[i] = *(const u4*)(x + (size_t)(t0 + wid * 8 + i) * DM + lane * 8);
    float* st = (float*)(ws + WS_ST0);
    float* st2 = (float*)(ws + WS_ST2);
#pragma unroll
    for (int i = 0; i < 8; ++i) {
      int r = wid * 8 + i;
      int sw = (r & 3) | ((r >> 1) & 4);
      *(u4*)(smem + r * 1024 + ((lane ^ (sw << 1)) * 16)) = v[i];
      float s = 0.f, ss = 0.f;
#pragma unroll
      for (int j = 0; j < 4; ++j) { s = dot2(v[i][j], BF_ONES, s); ss = dot2(v[i][j], v[i][j], ss); }
#pragma unroll
      for (int o = 32; o > 0; o >>= 1) { s += __shfl_xor(s, o); ss += __shfl_xor(ss, o); }
      if (lane == 0) {
        *(float2*)(st + (t0 + r) * 2) = make_float2(s, ss);
        *(float2*)(st2 + (t0 + r) * 2) = make_float2(0.f, 0.f);
      }
    }
    __syncthreads();
    const int g = lane >> 4, q = (lane >> 2) & 3, p = lane & 3;
    const int sw = q | ((g & 1) << 2);
    u16* xT = (u16*)(ws + WS_XT0);
#pragma unroll
    for (int i = 0; i < 8; ++i) {
      int dt_ = wid * 8 + i;
      const char* base = smem + (8 * g + q) * 1024 + ((dt_ ^ sw) * 32) + 8 * p;
      s4 a = tr_read(base), bq = tr_read(base + 4096);
      s8 o = cat8(a, bq);
      *(s8*)(xT + (size_t)(dt_ * 16 + (lane & 15)) * SL + t0 + 8 * g) = o;
    }
    return;
  }
  b -= PREP_TR;
  if (b < PREP_W1) {
    const int nt = b * 4 + wid;
    const int n = lane & 15, g = lane >> 4;
    u16* W1F = (u16*)(ws + WS_W1F);
    float cs = 0.f;
#pragma unroll 4
    for (int ks = 0; ks < 16; ++ks) {
      u16 e[8];
#pragma unroll
      for (int j = 0; j < 8; ++j) e[j] = w1[(size_t)(32 * ks + 8 * g + j) * FF + 16 * nt + n];
      u4 o;
#pragma unroll
      for (int j = 0; j < 4; ++j) { o[j] = (unsigned)e[2 * j] | ((unsigned)e[2 * j + 1] << 16); cs = dot2(o[j], BF_ONES, cs); }
      *(u4*)(W1F + ((size_t)(nt * 16 + ks) * 64 + lane) * 8) = o;
    }
    cs += __shfl_xor(cs, 16);
    cs += __shfl_xor(cs, 32);
    if (lane < 16) ((float*)(ws + WS_C1))[16 * nt + n] = cs;
    return;
  }
  b -= PREP_W1;
  {
    const int nt = b;
    const int n = lane & 15, g = lane >> 4;
    u16* W2F = (u16*)(ws + WS_W2F);
#pragma unroll 2
    for (int i = 0; i < 14; ++i) {
      int ks = wid * 14 + i;
      u16 e[8];
#pragma unroll
      for (int j = 0; j < 8; ++j) {
        int f = 32 * ks + (j < 4 ? 4 * g + j : 16 + 4 * g + (j - 4));
        e[j] = w2[(size_t)f * DM + 16 * nt + n];
      }
      u4 o;
#pragma unroll
      for (int j = 0; j < 4; ++j) o[j] = (unsigned)e[2 * j] | ((unsigned)e[2 * j + 1] << 16);
      *(u4*)(W2F + ((size_t)(nt * 56 + ks) * 64 + lane) * 8) = o;
    }
  }
}

constexpr int CV_U = 2064 * 2;
constexpr int CV_K = 2160 * 2;
constexpr int CV_OFF_K = 2 * CV_U;
constexpr int CV_OFF_Y = CV_OFF_K + 2 * KTERMS * CV_K;
constexpr int CV_OFF_C = CV_OFF_Y + 2 * SL * 4;
constexpr int CV_SMEM = CV_OFF_C + 2 * 33 * 16;

__global__ __launch_bounds__(256) void conv_kernel(
    const u16* __restrict__ xT, const float* __restrict__ stats, const u16* __restrict__ Kh,
    const u16* __restrict__ Kl, const float* __restrict__ ksc, const u16* __restrict__ dskip,
    u16* __restrict__ outT) {
  __shared__ __attribute__((aligned(16))) char smem[CV_SMEM];
  const int tid = threadIdx.x, lane = tid & 63, wid = tid >> 6;
  const int d0 = blockIdx.x * 2;
  const int t0 = tid * 8;
  u4 xr[2], kr[2][KTERMS];
  f4 sr[4];
#pragma unroll
  for (int i = 0; i < 4; ++i) sr[i] = *(const f4*)(stats + t0 * 2 + i * 4);
#pragma unroll
  for (int c = 0; c < 2; ++c) xr[c] = *(const u4*)(xT + (size_t)(d0 + c) * SL + t0);
#pragma unroll
  for (int c = 0; c < 2; ++c) {
    kr[c][0] = *(const u4*)(Kh + (size_t)(d0 + c) * SL + t0);
    if (KTERMS > 1) kr[c][KTERMS - 1] = *(const u4*)(Kl + (size_t)(d0 + c) * SL + t0);
  }
  if (tid < 14 * 2 * KTERMS) {
    int which = tid / 14, j = tid % 14;
    *(u4*)(smem + CV_OFF_K + which * CV_K + j * 16) = u4{0, 0, 0, 0};
  } else if (tid >= 64 && tid < 70) {
    int c = (tid - 64) / 3, j = (tid - 64) % 3;
    *(u2*)(smem + c * CV_U + j * 8) = u2{0, 0};
  } else if (tid >= 128 && tid < 130) {
    *(f4*)(smem + CV_OFF_C + (tid - 128) * 33 * 16) = f4{0, 0, 0, 0};
  }
  float mb[8], ri[8];
#pragma unroll
  for (int j = 0; j < 8; ++j) ln_consts(sr[j >> 1][(j & 1) * 2], sr[j >> 1][(j & 1) * 2 + 1], mb[j], ri[j]);
  float uu[2][8];
#pragma unroll
  for (int c = 0; c < 2; ++c) {
#pragma unroll
    for (int j = 0; j < 8; ++j) {
      float xv = (j & 1) ? bfhi(xr[c][j >> 1]) : bflo(xr[c][j >> 1]);
      uu[c][j] = rbf(rbf(xv - mb[j]) * ri[j]);
    }
    u2 a = {pack_h(uu[c][0], uu[c][1]), pack_h(uu[c][2], uu[c][3])};
    u2 bq = {pack_h(uu[c][4], uu[c][5]), pack_h(uu[c][6], uu[c][7])};
    *(u2*)(smem + c * CV_U + 24 + tid * 16) = a;
    *(u2*)(smem + c * CV_U + 32 + tid * 16) = bq;
#pragma unroll
    for (int k = 0; k < KTERMS; ++k) *(u4*)(smem + CV_OFF_K + (c * KTERMS + k) * CV_K + 224 + tid * 16) = kr[c][k];
  }
  __syncthreads();
  const int ch = wid >> 1, par = wid & 1;
  const int g = lane >> 4, q = (lane >> 2) & 3, p = lane & 3;
  const char* ub = smem + ch * CV_U + 8 * (p + q + 8 * g);
  const char* kb = smem + CV_OFF_K + ch * KTERMS * CV_K + 8 * (31 + 16 * par + 4 * p - 8 * g - q) - 32;
  s8 B[16];
#pragma unroll
  for (int n = 0; n < 16; ++n) B[n] = cat8(tr_read(ub + 256 * n), tr_read(ub + 256 * n + 32));
  f4 acc[16];
#pragma unroll
  for (int a = 0; a < 16; ++a) acc[a] = f4{0, 0, 0, 0};
#pragma unroll
  for (int dl = 0; dl < 16; ++dl) {
#pragma unroll
    for (int k = 0; k < KTERMS; ++k) {
      s8 A = cat8(tr_read(kb + k * CV_K + 256 * dl + 32), tr_read(kb + k * CV_K + 256 * dl));
#pragma unroll
      for (int n = 0; n + dl < 16; ++n)
        acc[n + dl] = __builtin_amdgcn_mfma_f32_16x16x32_f16(__builtin_bit_cast(h8, A), __builtin_bit_cast(h8, B[n]), acc[n + dl], 0, 0, 0);
    }
  }
  {
    float* Y = (float*)(smem + CV_OFF_Y) + ch * SL;
    f4* C = (f4*)(smem + CV_OFF_C) + ch * 33;
#pragma unroll
    for (int a = 0; a < 16; ++a) {
      int i = 2 * a + par;
      float w3 = acc[a][3];
      float w2 = acc[a][2] + shr1(w3);
      float w1 = acc[a][1] + shr1(w2);
      float w0 = acc[a][0] + shr1(w1);
      Y[64 * i + lane] = w0;
      if (lane == 63) C[i + 1] = f4{w1, w2, w3, 0.f};
    }
  }
  __syncthreads();
#pragma unroll
  for (int c = 0; c < 2; ++c) {
    const float* Y = (const float*)(smem + CV_OFF_Y) + c * SL;
    f4 y0 = *(const f4*)(Y + t0), y1 = *(const f4*)(Y + t0 + 4);
    if ((tid & 7) == 0) {
      f4 cr = ((const f4*)(smem + CV_OFF_C))[c * 33 + (tid >> 3)];
      y0[0] += cr[0]; y0[1] += cr[1]; y0[2] += cr[2];
    }
    float isc = ksc[d0 + c];
    float ds = bf2f(dskip[d0 + c]);
    float o[8];
#pragma unroll
    for (int j = 0; j < 8; ++j) {
      float y = rbf((j < 4 ? y0[j & 3] : y1[j & 3]) * isc);
      float a = rbf(y + rbf(uu[c][j] * ds));
      float xv = (j & 1) ? bfhi(xr[c][j >> 1]) : bflo(xr[c][j >> 1]);
      o[j] = xv + a;
    }
    u4 ov = {pack_bf(o[0], o[1]), pack_bf(o[2], o[3]), pack_bf(o[4], o[5]), pack_bf(o[6], o[7])};
    *(u4*)(outT + (size_t)(d0 + c) * SL + t0) = ov;
  }
}

constexpr int G1_SMEM = 512 * 256 + 128 * 8;

__global__ __launch_bounds__(256, 1) void gemm1_kernel(
    const u16* __restrict__ xT, const u16* __restrict__ W1F, const float* __restrict__ c1,
    u16* __restrict__ HF) {
  extern __shared__ __attribute__((aligned(16))) char smem[];
  const int tid = threadIdx.x, lane = tid & 63, wid = tid >> 6;
  const int bx = blockIdx.x, xj = bx & 7, r = bx >> 3;
  const int tp = 4 * (xj >> 1) + (r & 3);
  const int fp = 7 * (xj & 1) + (r >> 2);
  const int nt0 = fp * 8 + 2 * wid;
  s8 wf[16][2];
#pragma unroll
  for (int ks = 0; ks < 16; ++ks)
#pragma unroll
    for (int i = 0; i < 2; ++i) wf[ks][i] = *(const s8*)(W1F + ((size_t)((nt0 + i) * 16 + ks) * 64 + lane) * 8);
  u4 xr[4][8];
  const int lr = tid >> 4, lc = tid & 15;
#pragma unroll
  for (int c = 0; c < 4; ++c)
#pragma unroll
    for (int i = 0; i < 8; ++i) xr[c][i] = *(const u4*)(xT + (size_t)(c * 128 + i * 16 + lr) * SL + tp * 128 + lc * 8);
  const int g = lane >> 4, q = (lane >> 2) & 3, p = lane & 3;
  float cf[8];
#pragma unroll
  for (int j = 0; j < 8; ++j) cf[j] = c1[nt0 * 16 + (j < 4 ? 4 * g + j : 16 + 4 * g + j - 4)];
  const int wsw = (lr & 3) | ((lr >> 1) & 4);
  char* wbase = smem + lr * 256 + ((lc ^ (wsw << 1)) * 16);
  const int sw = q | ((g & 1) << 2);
  const char* rb[8];
#pragma unroll
  for (int i = 0; i < 8; ++i) {
    int tt = (i + 2 * wid) & 7;
    rb[i] = smem + (8 * g + q) * 256 + ((tt ^ sw) * 32) + 8 * p;
  }
  f4 acc[8][2];
#pragma unroll
  for (int i = 0; i < 8; ++i) { acc[i][0] = f4{0, 0, 0, 0}; acc[i][1] = f4{0, 0, 0, 0}; }
  float s0 = 0.f, ss0 = 0.f, s1 = 0.f, ss1 = 0.f;
#pragma unroll
  for (int c = 0; c < 4; ++c) {
#pragma unroll
    for (int i = 0; i < 8; ++i) *(u4*)(wbase + (c * 128 + i * 16) * 256) = xr[c][i];
    __syncthreads();
#pragma unroll
    for (int k4 = 0; k4 < 4; ++k4) {
      int ks = c * 4 + k4;
#pragma unroll
      for (int i = 0; i < 8; ++i) {
        s8 xb = cat8(tr_read(rb[i] + ks * 8192), tr_read(rb[i] + ks * 8192 + 1024));
        acc[i][0] = __builtin_amdgcn_mfma_f32_16x16x32_bf16(__builtin_bit_cast(b8, wf[ks][0]), __builtin_bit_cast(b8, xb), acc[i][0], 0, 0, 0);
        acc[i][1] = __builtin_amdgcn_mfma_f32_16x16x32_bf16(__builtin_bit_cast(b8, wf[ks][1]), __builtin_bit_cast(b8, xb), acc[i][1], 0, 0, 0);
        if (i < 2) {
          u4 xv = __builtin_bit_cast(u4, xb);
#pragma unroll
          for (int j = 0; j < 4; ++j) {
            if (i == 0) { s0 = dot2(xv[j], BF_ONES, s0); ss0 = dot2(xv[j], xv[j], ss0); }
            else { s1 = dot2(xv[j], BF_ONES, s1); ss1 = dot2(xv[j], xv[j], ss1); }
          }
        }
      }
    }
  }
  s0 += __shfl_xor(s0, 16); s0 += __shfl_xor(s0, 32);
  ss0 += __shfl_xor(ss0, 16); ss0 += __shfl_xor(ss0, 32);
  s1 += __shfl_xor(s1, 16); s1 += __shfl_xor(s1, 32);
  ss1 += __shfl_xor(ss1, 16); ss1 += __shfl_xor(ss1, 32);
  float2* st = (float2*)(smem + 512 * 256);
  if (lane < 32) {
    float mb, ri;
    ln_consts(lane < 16 ? s0 : s1, lane < 16 ? ss0 : ss1, mb, ri);
    st[wid * 32 + lane] = make_float2(mb, ri);
  }
  __syncthreads();
  const int ksf = fp * 4 + wid;
#pragma unroll
  for (int i = 0; i < 8; ++i) {
    int tt = (i + 2 * wid) & 7;
    float2 mr = st[tt * 16 + (lane & 15)];
    float o[8];
#pragma unroll
    for (int j = 0; j < 8; ++j) o[j] = fmaxf((acc[i][j >> 2][j & 3] - mr.x * cf[j]) * mr.y, 0.f);
    u4 ov = {pack_bf(o[0], o[1]), pack_bf(o[2], o[3]), pack_bf(o[4], o[5]), pack_bf(o[6], o[7])};
    *(u4*)(HF + ((size_t)((tp * 8 + tt) * 56 + ksf) * 64 + lane) * 8) = ov;
  }
}

template <bool FINAL>
__global__ __launch_bounds__(256, 1) void gemm2_kernel(
    const u16* __restrict__ HF, const u16* __restrict__ W2F, const u16* __restrict__ xT,
    u16* __restrict__ out, float* __restrict__ stats) {
  __shared__ __attribute__((aligned(16))) f4 red[4 * 3 * 4 * 64];
  const int tid = threadIdx.x, lane = tid & 63, wid = tid >> 6;
  const int bx = blockIdx.x, xj = bx & 7, r = bx >> 3;
  const int tb = 4 * xj + (r & 3), db = r >> 2;
  const int g = lane >> 4, n = lane & 15;
  constexpr int NK = 14, PD = 6;
  const u16* ap[4];
  const u16* bp[4];
#pragma unroll
  for (int i = 0; i < 4; ++i) {
    int mt = (i + wid) & 3;
    ap[i] = HF + ((size_t)((tb * 4 + mt) * 56 + wid * NK) * 64 + lane) * 8;
    bp[i] = W2F + ((size_t)((db * 4 + i) * 56 + wid * NK) * 64 + lane) * 8;
  }
  s8 a[NK][4], b[NK][4];
#pragma unroll
  for (int ks = 0; ks < PD; ++ks)
#pragma unroll
    for (int i = 0; i < 4; ++i) { a[ks][i] = *(const s8*)(ap[i] + ks * 512); b[ks][i] = *(const s8*)(bp[i] + ks * 512); }
  u2 res[4];
  if (!FINAL) {
#pragma unroll
    for (int j = 0; j < 4; ++j) res[j] = *(const u2*)(xT + (size_t)(db * 64 + j * 16 + n) * SL + tb * 64 + wid * 16 + 4 * g);
  } else {
#pragma unroll
    for (int j = 0; j < 4; ++j) {
      const u16* px = xT + (size_t)(db * 64 + j * 16 + 4 * g) * SL + tb * 64 + wid * 16 + n;
      res[j] = u2{(unsigned)px[0] | ((unsigned)px[SL] << 16), (unsigned)px[2 * SL] | ((unsigned)px[3 * SL] << 16)};
    }
  }
  f4 acc[4][4];
#pragma unroll
  for (int i = 0; i < 4; ++i)
#pragma unroll
    for (int j = 0; j < 4; ++j) acc[i][j] = f4{0, 0, 0, 0};
#pragma unroll
  for (int ks = 0; ks < NK; ++ks) {
    if (ks + PD < NK) {
#pragma unroll
      for (int i = 0; i < 4; ++i) { a[ks + PD][i] = *(const s8*)(ap[i] + (ks + PD) * 512); b[ks + PD][i] = *(const s8*)(bp[i] + (ks + PD) * 512); }
    }
#pragma unroll
    for (int i = 0; i < 4; ++i)
#pragma unroll
      for (int j = 0; j < 4; ++j) {
        if (!FINAL) acc[i][j] = __builtin_amdgcn_mfma_f32_16x16x32_bf16(__builtin_bit_cast(b8, a[ks][i]), __builtin_bit_cast(b8, b[ks][j]), acc[i][j], 0, 0, 0);
        else acc[i][j] = __builtin_amdgcn_mfma_f32_16x16x32_bf16(__builtin_bit_cast(b8, b[ks][j]), __builtin_bit_cast(b8, a[ks][i]), acc[i][j], 0, 0, 0);
      }
  }
#pragma unroll
  for (int i = 1; i < 4; ++i) {
    int mt = (i + wid) & 3;
#pragma unroll
    for (int j = 0; j < 4; ++j) red[((mt * 3 + i - 1) * 4 + j) * 64 + lane] = acc[i][j];
  }
  __syncthreads();
#pragma unroll
  for (int j = 0; j < 4; ++j) {
    acc[0][j] += red[((wid * 3 + 0) * 4 + j) * 64 + lane];
    acc[0][j] += red[((wid * 3 + 1) * 4 + j) * 64 + lane] + red[((wid * 3 + 2) * 4 + j) * 64 + lane];
  }
  float s[4] = {0, 0, 0, 0}, ss[4] = {0, 0, 0, 0};
#pragma unroll
  for (int j = 0; j < 4; ++j) {
    float o[4];
    o[0] = rbf(bflo(res[j][0]) + rbf(acc[0][j][0]));
    o[1] = rbf(bfhi(res[j][0]) + rbf(acc[0][j][1]));
    o[2] = rbf(bflo(res[j][1]) + rbf(acc[0][j][2]));
    o[3] = rbf(bfhi(res[j][1]) + rbf(acc[0][j][3]));
    u2 ov = {pack_bf(o[0], o[1]), pack_bf(o[2], o[3])};
    if (!FINAL) {
      *(u2*)(out + (size_t)(db * 64 + j * 16 + n) * SL + tb * 64 + wid * 16 + 4 * g) = ov;
#pragma unroll
      for (int e = 0; e < 4; ++e) { s[e] += o[e]; ss[e] += o[e] * o[e]; }
    } else {
      *(u2*)(out + (size_t)(tb * 64 + wid * 16 + n) * DM + db * 64 + j * 16 + 4 * g) = ov;
    }
  }
  if (!FINAL) {
#pragma unroll
    for (int e = 0; e < 4; ++e) {
#pragma unroll
      for (int o = 1; o < 16; o <<= 1) { s[e] += __shfl_xor(s[e], o); ss[e] += __shfl_xor(ss[e], o); }
    }
    if (n < 8) {
      int e = n >> 1;
      float v = (n & 1) ? (e == 0 ? ss[0] : e == 1 ? ss[1] : e == 2 ? ss[2] : ss[3]) : (e == 0 ? s[0] : e == 1 ? s[1] : e == 2 ? s[2] : s[3]);
      atomicAdd(stats + (tb * 64 + wid * 16 + 4 * g) * 2 + n, v);
    }
  }
}

extern "C" void kernel_launch(void* const* d_in, const int* in_sizes, int n_in,
                              void* d_out, int out_size, void* d_ws, size_t ws_size,
                              hipStream_t stream) {
  const u16* x = (const u16*)d_in[0];
  const u16* logA = (const u16*)d_in[1];
  const u16* Aim = (const u16*)d_in[2];
  const u16* Cre = (const u16*)d_in[3];
  const u16* Cim = (const u16*)d_in[4];
  const u16* logdt = (const u16*)d_in[5];
  const u16* dskip = (const u16*)d_in[6];
  const u16* w1 = (const u16*)d_in[8];
  const u16* w2 = (const u16*)d_in[9];
  char* ws = (char*)d_ws;
  u16* out = (u16*)d_out;
  hipFuncSetAttribute((const void*)gemm1_kernel, hipFuncAttributeMaxDynamicSharedMemorySize, G1_SMEM);

  prep_kernel<<<PREP_KGEN + PREP_TR + PREP_W1 + PREP_W2, 256, 0, stream>>>(x, logA, Aim, Cre, Cim, logdt, w1, w2, ws);
  conv_kernel<<<256, 256, 0, stream>>>((const u16*)(ws + WS_XT0), (const float*)(ws + WS_ST0), (const u16*)(ws + WS_KH),
                                       (const u16*)(ws + WS_KL), (const float*)(ws + WS_KSC), dskip, (u16*)(ws + WS_XT1));
  gemm1_kernel<<<224, 256, G1_SMEM, stream>>>((const u16*)(ws + WS_XT1), (const u16*)(ws + WS_W1F), (const float*)(ws + WS_C1), (u16*)(ws + WS_HF));
  gemm2_kernel<false><<<256, 256, 0, stream>>>((const u16*)(ws + WS_HF), (const u16*)(ws + WS_W2F), (const u16*)(ws + WS_XT1),
                                               (u16*)(ws + WS_XT2), (float*)(ws + WS_ST2));
  conv_kernel<<<256, 256, 0, stream>>>((const u16*)(ws + WS_XT2), (const float*)(ws + WS_ST2), (const u16*)(ws + WS_KH),
                                       (const u16*)(ws + WS_KL), (const float*)(ws + WS_KSC), dskip, (u16*)(ws + WS_XT3));
  gemm1_kernel<<<224, 256, G1_SMEM, stream>>>((const u16*)(ws + WS_XT3), (const u16*)(ws + WS_W1F), (const float*)(ws + WS_C1), (u16*)(ws + WS_HF));
  gemm2_kernel<true><<<256, 256, 0, stream>>>((const u16*)(ws + WS_HF), (const u16*)(ws + WS_W2F), (const u16*)(ws + WS_XT3),
                                              out, nullptr);
}
```

```cpp
#include <hip/hip_runtime.h>
#include <hip/hip_bf16.h>

typedef unsigned short u16;
typedef short s4 __attribute__((ext_vector_type(4)));
typedef short s2v __attribute__((ext_vector_type(2)));
typedef short s8 __attribute__((ext_vector_type(8)));
typedef _Float16 h8 __attribute__((ext_vector_type(8)));
typedef _Float16 h2 __attribute__((ext_vector_type(2)));
typedef __bf16 b2 __attribute__((ext_vector_type(2)));
typedef __bf16 b8 __attribute__((ext_vector_type(8)));
typedef float f4 __attribute__((ext_vector_type(4)));
typedef float f2 __attribute__((ext_vector_type(2)));
typedef __bf16 b4 __attribute__((ext_vector_type(4)));
typedef float f16v __attribute__((ext_vector_type(16)));
typedef unsigned u4 __attribute__((ext_vector_type(4)));
typedef unsigned u2 __attribute__((ext_vector_type(2)));
typedef __attribute__((address_space(3))) s4* lds_s4p;

#define STAMP_DECL
#define STAMP(i)
#define STAMP_WAITV
#define STAMP_OUT(k)
#define SCHED __builtin_amdgcn_sched_barrier(0)
template <int... Is> struct iseq {};
template <int N, int... Is> struct mk_iseq : mk_iseq<N - 1, N - 1, Is...> {};
template <int... Is> struct mk_iseq<0, Is...> { typedef iseq<Is...> type; };
template <int N> using make_iseq = typename mk_iseq<N>::type;
template <int I> struct ic { static constexpr int value = I; };
template <class F, int... Is> __device__ __forceinline__ void sfor_impl(F&& f, iseq<Is...>) { (f(ic<Is>{}), ...); }
template <int N, class F> __device__ __forceinline__ void sfor(F&& f) { sfor_impl(f, make_iseq<N>{}); }
#define AINL __attribute__((always_inline))

constexpr int SL = 2048;
constexpr int DM = 512;
constexpr int NS = 128;
constexpr int FF = 1792;
#define KTERMS 2

constexpr size_t WS_XT0 = 0;
constexpr size_t WS_XT1 = WS_XT0 + (size_t)SL * DM * 2;
constexpr size_t WS_XT2 = WS_XT1 + (size_t)SL * DM * 2;
constexpr size_t WS_XT3 = WS_XT2 + (size_t)SL * DM * 2;
constexpr size_t WS_KH  = WS_XT3 + (size_t)SL * DM * 2;
constexpr size_t WS_KL  = WS_KH + (size_t)SL * DM * 2;
constexpr size_t WS_W1F = WS_KL + (size_t)SL * DM * 2;
constexpr size_t WS_W2F = WS_W1F + (size_t)DM * FF * 2;
constexpr size_t WS_HF  = WS_W2F + (size_t)DM * FF * 2;
constexpr size_t WS_HF2 = WS_HF + (size_t)SL * FF * 2;
constexpr size_t WS_ST0 = WS_HF2 + (size_t)SL * FF * 2;
constexpr size_t WS_ST2 = WS_ST0 + (size_t)SL * 8;
constexpr size_t WS_C1  = WS_ST2 + (size_t)SL * 8;
constexpr size_t WS_KSC = WS_C1 + (size_t)FF * 4;
constexpr size_t WS_PST = WS_KSC + (size_t)DM * 4;
constexpr size_t WS_PST2 = WS_PST + (size_t)SL * 32;

__host__ __device__ __forceinline__ int xoff(int d, int t) { return (t >> 5) * 16384 + d * 32 + (t & 31); }
__device__ __forceinline__ float bf2f(unsigned h) { return __uint_as_float(h << 16); }
__device__ __forceinline__ float bflo(unsigned w) { return __uint_as_float(w << 16); }
__device__ __forceinline__ float bfhi(unsigned w) { return __uint_as_float(w & 0xffff0000u); }
__device__ __forceinline__ unsigned pack_bf(float a, float b) {
  b2 v = {(__bf16)a, (__bf16)b};
  return __builtin_bit_cast(unsigned, v);
}
__device__ __forceinline__ float rbf(float f) {
  return __uint_as_float(((unsigned)__builtin_bit_cast(u16, (__bf16)f)) << 16);
}
__device__ __forceinline__ unsigned pack_h(float a, float b) {
  h2 v = {(_Float16)a, (_Float16)b};
  return __builtin_bit_cast(unsigned, v);
}
__device__ __forceinline__ float dot2(unsigned a, unsigned b, float c) {
  return __builtin_amdgcn_fdot2_f32_bf16(__builtin_bit_cast(b2, a), __builtin_bit_cast(b2, b), c, false);
}
__device__ __forceinline__ s4 tr_read(const char* p) {
  return __builtin_amdgcn_ds_read_tr16_b64_v4i16((lds_s4p)p);
}
__device__ __forceinline__ s8 cat8(s4 a, s4 b) { return __builtin_shufflevector(a, b, 0, 1, 2, 3, 4, 5, 6, 7); }
template <int CTRL>
__device__ __forceinline__ float dppf(float v) {
  return __builtin_bit_cast(float, __builtin_amdgcn_update_dpp(0, __builtin_bit_cast(int, v), CTRL, 0xf, 0xf, true));
}
__device__ __forceinline__ float shr1(float v) { return dppf<0x138>(v); }
__device__ __forceinline__ float row_sum16(float v) {
  v += dppf<0x128>(v);
  v += dppf<0x124>(v);
  v += dppf<0x122>(v);
  v += dppf<0x121>(v);
  return v;
}
constexpr unsigned BF_ONES = 0x3f803f80u;
__device__ __forceinline__ void st16(void* p, u4 v) { asm volatile("global_store_dwordx4 %0, %1, off sc1\n\ts_nop 1" :: "v"(p), "v"(v) : "memory"); }
__device__ __forceinline__ void st8(void* p, u2 v) { asm volatile("global_store_dwordx2 %0, %1, off sc1\n\ts_nop 0" :: "v"(p), "v"(v) : "memory"); }

__device__ __forceinline__ void ln_consts(float s, float ss, float& mb, float& rinv) {
  float mean = s * (1.0f / DM);
  float var = fmaxf(ss * (1.0f / DM) - mean * mean, 0.0f);
  mb = rbf(mean);
  float sd = rbf(__builtin_amdgcn_sqrtf(rbf(rbf(var) + 1e-5f)));
  rinv = __builtin_amdgcn_rcpf(sd);
}

constexpr int PREP_W1 = 56, PREP_W2 = 56, PREP_TR = 64;
constexpr int PK_PAR = 0, PK_PARC = 2048, PK_RED = 3072, PK_KV = PK_RED + 12288, PK_MX = PK_KV + 3072, PK_SZ = PK_MX + 128;
constexpr int PREP_SMEM = 32768 + 1024 + 2 * PK_SZ;

__device__ __forceinline__ int alpha_of(int idx) {
  if (idx < 16) return (idx >> 3) * 128 + (idx & 7);
  if (idx < 24) { int j = idx - 16; return (2 + (j >> 2)) * 128 + (j & 3) * 2; }
  if (idx < 32) { int j = idx - 24; return (4 + (j >> 1)) * 128 + (j & 1) * 4; }
  if (idx < 41) return (8 + idx - 32) * 128;
  return 0;
}
__device__ __forceinline__ float exp_acc(float x) {
  const float L2EH = 1.44269504f, L2EL = (float)(1.4426950408889634 - (double)1.44269504f);
  float t = x * L2EH;
  float e = fmaf(x, L2EH, -t) + x * L2EL;
  float r = __builtin_amdgcn_exp2f(t);
  return fmaf(r, e * 0.69314718f, r);
}
__device__ __forceinline__ float row_max16(float v) {
  v = fmaxf(v, dppf<0x128>(v));
  v = fmaxf(v, dppf<0x124>(v));
  v = fmaxf(v, dppf<0x122>(v));
  v = fmaxf(v, dppf<0x121>(v));
  return v;
}
__device__ __forceinline__ void split_h(float a, float b, unsigned& hi, unsigned& lo) {
  float ah = __uint_as_float(__float_as_uint(a) & 0xffffe000u), bh = __uint_as_float(__float_as_uint(b) & 0xffffe000u);
  hi = pack_h(ah, bh);
  lo = pack_h(a - ah, b - bh);
}

__global__ __launch_bounds__(512, 2) void prep_kernel(
    const u16* __restrict__ x, const u16* __restrict__ logA, const u16* __restrict__ Aim,
    const u16* __restrict__ Cre, const u16* __restrict__ Cim, const u16* __restrict__ logdt,
    const u16* __restrict__ w1, const u16* __restrict__ w2, char* __restrict__ ws) {
  __shared__ __attribute__((aligned(16))) char smem[PREP_SMEM];
  const int tid = threadIdx.x, lane = tid & 63;
  const int wid = __builtin_amdgcn_readfirstlane(tid >> 6);
  STAMP_DECL STAMP(0);
  const int b = blockIdx.x;
  const int job = b < PREP_W1 ? 0 : (b < PREP_W1 + PREP_W2 ? 1 : (b < PREP_W1 + PREP_W2 + PREP_TR ? 2 : 3));
  const int jb = job == 0 ? b : (job == 1 ? b - PREP_W1 : b - PREP_W1 - PREP_W2);
  const int ch = wid >> 2, w4 = wid & 3, t8 = tid & 255;
  const int d = b * 2 + ch;
  const int idx = lane & 15, g = lane >> 4;
  const int pn = d * NS + (t8 & (NS - 1));
  const u16 pl0 = logdt[d], pl1 = logA[pn], pl2 = Aim[pn], pl3 = Cre[pn], pl4 = Cim[pn];
  u4 v[4];
  int lofs;
  int ls1, ls2;
  {
    const int jx = job == 3 ? 0 : jb;
    const u16* src;
    int gs1, gs2;
    if (job == 0) {
      src = w1 + (size_t)(tid >> 2) * FF + jx * 32 + (tid & 3) * 8;
      gs1 = 128 * FF; gs2 = 256 * FF; lofs = tid * 16; ls1 = 8192; ls2 = 16384;
    } else if (job == 1) {
      src = w2 + (size_t)((jx >> 2) * 128 + (tid >> 4)) * DM + (jx & 3) * 128 + (tid & 15) * 8;
      gs1 = 32 * DM; gs2 = 64 * DM; lofs = (tid >> 4) * 256 + (((tid & 15) ^ (((tid >> 4) & 7) << 1)) * 16); ls1 = 8192; ls2 = 16384;
    } else {
      const int r0 = (wid >> 2) * 8 + (wid & 3);
      src = x + (size_t)(jx * 32 + r0) * DM + lane * 8;
      gs1 = 4 * DM; gs2 = 16 * DM; lofs = r0 * 1024 + ((lane ^ (wid << 1)) * 16); ls1 = 4096; ls2 = 16384;
    }
    v[0] = *(const u4*)(src); v[1] = *(const u4*)(src + gs1); v[2] = *(const u4*)(src + gs2); v[3] = *(const u4*)(src + gs1 + gs2);
  }
  int kidx[8];
#pragma unroll
  for (int j = 0; j < 8; ++j) {
    float pf = rbf((float)(t8 * 8 + j));
    int p = (int)pf;
    int e = (int)(__float_as_uint(pf) >> 23) - (127 + 7);
    e = e < 0 ? 0 : (e > 3 ? 3 : e);
    int ai_ = ((((p >> 7) << 3) + (p & 7)) >> e) + 8 * e;
    kidx[j] = ai_ * 16 + ((p & 127) >> 3);
  }
  char* ks = smem + 32768 + 1024 + ch * PK_SZ;
  f4* par = (f4*)(ks + PK_PAR);
  f2* parc = (f2*)(ks + PK_PARC);
  f4* red = (f4*)(ks + PK_RED);
  float* kv = (float*)(ks + PK_KV);
  float* mc = (float*)(ks + PK_MX);
  {
    float dt = rbf(exp_acc(bf2f(pl0)));
    float Ar = -rbf(exp_acc(bf2f(pl1)));
    float ar = Ar * dt;
    float ai = bf2f(pl2) * dt;
    const float I2PH = 0.15915494f;
    const float I2PL = (float)(0.15915494309189535 - (double)0.15915494f);
    float fh = ai * I2PH;
    float fl = fmaf(ai, I2PH, -fh) + ai * I2PL;
    fh -= rintf(fh);
    float fhi = __uint_as_float(__float_as_uint(fh) & 0xfffff000u);
    float flo = (fh - fhi) + fl;
    float crv = 2.0f * bf2f(pl3) * dt;
    float civ = 2.0f * bf2f(pl4) * dt;
    float cm = row_sum16(fabsf(crv) + fabsf(civ));
    if (t8 < NS) {
      par[t8] = f4{ar * 1.44269504f, fhi, flo, 0.f};
      parc[t8] = f2{crv, civ};
      if (idx == 0) mc[t8 >> 4] = cm;
    }
  }
  *(u4*)(smem + lofs) = v[0];
  *(u4*)(smem + lofs + ls1) = v[1];
  *(u4*)(smem + lofs + ls2) = v[2];
  *(u4*)(smem + lofs + ls1 + ls2) = v[3];
  __syncthreads();
  STAMP(1);
  const int q = (lane >> 2) & 3, p = lane & 3;
  float* csr = (float*)(smem + 32768);
  if (job == 0) {
    const int h = lane >> 5, blk = (lane >> 4) & 1;
    const char* rb = smem + (wid * 64 + 8 * h + q) * 64 + blk * 32 + p * 8;
    u16* W1F = (u16*)(ws + WS_W1F);
    float cs = 0.f;
#pragma unroll
    for (int i = 0; i < 4; ++i) {
      s8 o = cat8(tr_read(rb + i * 1024), tr_read(rb + i * 1024 + 256));
      u4 ou = __builtin_bit_cast(u4, o);
#pragma unroll
      for (int j = 0; j < 4; ++j) cs = dot2(ou[j], BF_ONES, cs);
      st16(W1F + ((size_t)(jb * 32 + wid * 4 + i) * 64 + lane) * 8, ou);
    }
    cs += __shfl_xor(cs, 32);
    if (lane < 32) csr[wid * 32 + lane] = cs;
  } else if (job == 1) {
    const int rl = 16 * (g >> 1) + 4 * (g & 1) + q;
    const int sw = rl & 7;
    u16* W2F = (u16*)(ws + WS_W2F);
#pragma unroll
    for (int i = 0; i < 4; ++i) {
      const int nt = (wid & 1) * 4 + i;
      const char* rb = smem + ((wid >> 1) * 32 + rl) * 256 + ((nt ^ sw) * 32) + p * 8;
      s8 o = cat8(tr_read(rb), tr_read(rb + 2048));
      st16(W2F + ((size_t)(((jb & 3) * 8 + nt) * 56 + (jb >> 2) * 4 + (wid >> 1)) * 64 + lane) * 8, __builtin_bit_cast(u4, o));
    }
  } else if (job == 2) {
    const int sw = q | ((g & 1) << 2);
    u16* xT = (u16*)(ws + WS_XT0);
#pragma unroll
    for (int i = 0; i < 4; ++i) {
      int dt_ = wid * 4 + i;
      const char* base = smem + (8 * g + q) * 1024 + ((dt_ ^ sw) * 32) + 8 * p;
      s8 o = cat8(tr_read(base), tr_read(base + 4096));
      *(s8*)(xT + xoff(dt_ * 16 + idx, jb * 32 + 8 * g)) = o;
    }
    float sv[4], ssv[4];
#pragma unroll
    for (int i = 0; i < 4; ++i) {
      float s = 0.f, ss = 0.f;
#pragma unroll
      for (int j = 0; j < 4; ++j) { s = dot2(v[i][j], BF_ONES, s); ss = dot2(v[i][j], v[i][j], ss); }
      sv[i] = row_sum16(s); ssv[i] = row_sum16(ss);
    }
    float val = sv[0];
#pragma unroll
    for (int i = 1; i < 4; ++i) val = (idx == 2 * i) ? sv[i] : val;
#pragma unroll
    for (int i = 0; i < 4; ++i) val = (idx == 2 * i + 1) ? ssv[i] : val;
    val += __shfl_xor(val, 16);
    val += __shfl_xor(val, 32);
    if (lane < 8) {
      const int i = lane >> 1;
      const int r = jb * 32 + (i >> 1) * 16 + (wid >> 2) * 8 + (i & 1) * 4 + (wid & 3);
      ((float*)(ws + WS_ST0))[r * 2 + (lane & 1)] = val;
      ((float*)(ws + WS_ST2))[r * 2 + (lane & 1)] = 0.f;
    }
  }
  STAMP(2);
  int ce;
  {
    f4 m0 = *(const f4*)(mc), m1 = *(const f4*)(mc + 4);
    float m = ((m0[0] + m0[1]) + (m0[2] + m0[3])) + ((m1[0] + m1[1]) + (m1[2] + m1[3]));
    ce = (int)((__float_as_uint(m) >> 23) & 0xff) - 127;
    ce = ce < -100 ? -100 : (ce > 100 ? 100 : ce);
  }
  {
    const f2 pos01 = {(float)(8 * idx), (float)alpha_of(idx)};
    const f2 pos23 = {(float)alpha_of(16 + idx), (float)alpha_of(32 + idx)};
    const f2 eo01 = {(float)(12 - ce), 13.f}, eo23 = {13.f, 13.f};
    f4 acc0 = {0, 0, 0, 0}, acc1 = {0, 0, 0, 0}, acc2 = {0, 0, 0, 0};
#pragma unroll
    for (int k2 = 0; k2 < 2; ++k2) {
      u4 hi[4], lo[4];
#pragma unroll
      for (int e = 0; e < 4; ++e) {
        const int n = (w4 * 2 + k2) * 16 + 4 * g + e;
        const f4 pp = par[n];
        const f2 pc = parc[n];
        f2 t01 = pos01 * pp[1], t23 = pos23 * pp[1];
        f2 r01 = {__builtin_amdgcn_fractf(t01[0]), __builtin_amdgcn_fractf(t01[1])};
        f2 r23 = {__builtin_amdgcn_fractf(t23[0]), __builtin_amdgcn_fractf(t23[1])};
        r01 = __builtin_elementwise_fma(pos01, f2{pp[2], pp[2]}, r01);
        r23 = __builtin_elementwise_fma(pos23, f2{pp[2], pp[2]}, r23);
        f2 a01 = __builtin_elementwise_fma(pos01, f2{pp[0], pp[0]}, eo01);
        f2 a23 = __builtin_elementwise_fma(pos23, f2{pp[0], pp[0]}, eo23);
        float rv[4] = {r01[0], r01[1], r23[0], r23[1]};
        float av[4] = {a01[0], a01[1], a23[0], a23[1]};
#pragma unroll
        for (int qq = 0; qq < 4; ++qq) {
          float ex = __builtin_amdgcn_exp2f(av[qq]);
          float cs = __builtin_amdgcn_cosf(rv[qq]), sn = __builtin_amdgcn_sinf(rv[qq]);
          f2 z;
          if (qq == 0) {
            f2 t = f2{cs, sn} * pc[0];
            t = __builtin_elementwise_fma(f2{-sn, cs}, f2{pc[1], pc[1]}, t);
            z = t * ex;
          } else {
            z = f2{cs, -sn} * ex;
          }
          unsigned hh, ll;
          split_h(z[0], z[1], hh, ll);
          hi[qq][e] = hh; lo[qq][e] = ll;
        }
      }
      const h8 bh = __builtin_bit_cast(h8, hi[0]), bl = __builtin_bit_cast(h8, lo[0]);
      acc0 = __builtin_amdgcn_mfma_f32_16x16x32_f16(__builtin_bit_cast(h8, lo[1]), bh, acc0, 0, 0, 0);
      acc1 = __builtin_amdgcn_mfma_f32_16x16x32_f16(__builtin_bit_cast(h8, lo[2]), bh, acc1, 0, 0, 0);
      acc2 = __builtin_amdgcn_mfma_f32_16x16x32_f16(__builtin_bit_cast(h8, lo[3]), bh, acc2, 0, 0, 0);
      acc0 = __builtin_amdgcn_mfma_f32_16x16x32_f16(__builtin_bit_cast(h8, hi[1]), bl, acc0, 0, 0, 0);
      acc1 = __builtin_amdgcn_mfma_f32_16x16x32_f16(__builtin_bit_cast(h8, hi[2]), bl, acc1, 0, 0, 0);
      acc2 = __builtin_amdgcn_mfma_f32_16x16x32_f16(__builtin_bit_cast(h8, hi[3]), bl, acc2, 0, 0, 0);
      acc0 = __builtin_amdgcn_mfma_f32_16x16x32_f16(__builtin_bit_cast(h8, hi[1]), bh, acc0, 0, 0, 0);
      acc1 = __builtin_amdgcn_mfma_f32_16x16x32_f16(__builtin_bit_cast(h8, hi[2]), bh, acc1, 0, 0, 0);
      acc2 = __builtin_amdgcn_mfma_f32_16x16x32_f16(__builtin_bit_cast(h8, hi[3]), bh, acc2, 0, 0, 0);
    }
    red[(w4 * 3 + 0) * 64 + lane] = acc0;
    red[(w4 * 3 + 1) * 64 + lane] = acc1;
    red[(w4 * 3 + 2) * 64 + lane] = acc2;
  }
  STAMP(3);
  __syncthreads();
  STAMP(4);
  if (w4 < 3) {
    f4 vv = red[(0 * 3 + w4) * 64 + lane] + red[(1 * 3 + w4) * 64 + lane] +
            (red[(2 * 3 + w4) * 64 + lane] + red[(3 * 3 + w4) * 64 + lane]);
#pragma unroll
    for (int r = 0; r < 4; ++r) kv[(16 * w4 + 4 * g + r) * 16 + idx] = vv[r] * (1.0f / 2048.0f);
  } else if (job == 0 && tid < 256 && lane < 32) {
    float cs = 0.f;
#pragma unroll
    for (int i = 0; i < 8; ++i) cs += csr[i * 32 + lane];
    ((float*)(ws + WS_C1))[jb * 32 + lane] = cs;
  }
  if (t8 == 0) ((float*)(ws + WS_KSC))[d] = __uint_as_float((unsigned)(127 + ce - 14) << 23);
  __syncthreads();
  u4 hi, lo;
#pragma unroll
  for (int j = 0; j < 4; ++j) {
    if (KTERMS > 1) {
      unsigned hh, ll;
      split_h(kv[kidx[2 * j]], kv[kidx[2 * j + 1]], hh, ll);
      hi[j] = hh; lo[j] = ll;
    } else {
      hi[j] = pack_h(kv[kidx[2 * j]], kv[kidx[2 * j + 1]]);
    }
  }
  st16(ws + WS_KH + ((size_t)d * SL + t8 * 8) * 2, hi);
  if (KTERMS > 1) st16(ws + WS_KL + ((size_t)d * SL + t8 * 8) * 2, lo);
  STAMP(5); STAMP_WAITV STAMP(6); STAMP_OUT(0);
}

constexpr int CV_U = 2064 * 2;
constexpr int CV_K = 2160 * 2;
constexpr int CV_OFF_K = 2 * CV_U;
constexpr int CV_OFF_Y = CV_OFF_K + 2 * KTERMS * CV_K;
constexpr int CV_Q = (SL + 4) * 4;
constexpr int CV_SMEM = CV_OFF_Y + 2 * 4 * CV_Q;

constexpr int CV_RPF = 2 + 2 * KTERMS;
constexpr int CV_R0 = (KTERMS == 2 ? 6 : 10) * CV_RPF;
constexpr int CV_NM = 68 * KTERMS;
struct CvStep { int a, n, k, first, op, arg, arg2; };
template <int SUB>
struct CvSched {
  CvStep s[CV_NM];
  int wt, wc, slack;
  constexpr CvSched() : s{}, wt(-1), wc(4), slack(1000) {
    const int AMAX = SUB == 0 ? 15 : 14;
    const int NR = (AMAX + 1) * CV_RPF;
    int nr = CV_R0, mi = 0;
    int endat[16] = {};
    for (int a = 0; a < 16; ++a) endat[a] = 1 << 20;
    for (int a = 0; a <= AMAX; ++a) {
      if ((((a + 1) >> 1) & 1) != SUB) continue;
      for (int nn = 0; nn <= a; ++nn) {
        const int n = (a == 0) ? 0 : (nn + 1 < a ? nn + 1 : (nn + 1 == a ? 0 : a));
        for (int k = 0; k < KTERMS; ++k) {
          CvStep st{a, n, k, (nn == 0 && k == 0) ? 1 : 0, 0, 0, 0};
          { const int need = ((n > a - n ? n : a - n) + 1) * CV_RPF; if (nr < NR && nr - need < slack) slack = nr - need; }
          ++mi;
          if (wc == 4) {
            for (int t = wt + 1; t < 16; ++t)
              if (endat[t] < (1 << 20)) { if (endat[t] + 8 <= mi) { wt = t; wc = 0; } break; }
          }
          if (wc < 4) { st.op = 2; st.arg = wt; st.arg2 = wc; ++wc; }
          else if (nr < NR) { st.op = 1; st.arg = nr; ++nr; }
          s[mi - 1] = st;
        }
      }
      endat[a] = mi;
    }
  }
};
template <int R>
__device__ __forceinline__ void cv_rd(s4 (&A)[16][KTERMS][2], s4 (&B)[16][2], const char* ub, const char* kb) {
  constexpr int i = R / CV_RPF, part = R % CV_RPF;
  if constexpr (part < 2) B[i][part] = tr_read(ub + 256 * i + 32 * part);
  else A[i][(part - 2) >> 1][part & 1] = tr_read(kb + ((part - 2) >> 1) * CV_K + 256 * i + 32 * (1 - (part & 1)));
}
template <int SUB, int I>
__device__ __forceinline__ void cv_step(s4 (&A)[16][KTERMS][2], s4 (&B)[16][2], f4 (&acc)[16], const char* ub, const char* kb, char* qb) {
  constexpr CvSched<SUB> S{};
  constexpr CvStep st = S.s[I];
  const s8 av = cat8(A[st.a - st.n][st.k][0], A[st.a - st.n][st.k][1]), bv = cat8(B[st.n][0], B[st.n][1]);
  if constexpr (st.first) asm volatile("v_mfma_f32_16x16x32_f16 %0, %1, %2, 0" : "=&v"(acc[st.a]) : "v"(av), "v"(bv));
  else asm volatile("v_mfma_f32_16x16x32_f16 %0, %1, %2, %0" : "+v"(acc[st.a]) : "v"(av), "v"(bv));
  if constexpr (st.op == 2) *(float*)(qb + st.arg2 * (CV_Q + 4) + st.arg * 512) = acc[st.arg][st.arg2];
  else if constexpr (st.op == 1) cv_rd<st.arg>(A, B, ub, kb);
  SCHED;
}
template <int SUB, int... Rs, int... Is>
__device__ __forceinline__ void conv_mfma_impl(const char* ub, const char* kb, char* qb, iseq<Rs...>, iseq<Is...>) {
  s4 A[16][KTERMS][2];
  s4 B[16][2];
  f4 acc[16];
  (cv_rd<Rs>(A, B, ub, kb), ...);
  SCHED;
  (cv_step<SUB, Is>(A, B, acc, ub, kb, qb), ...);
  asm volatile("s_nop 7");
  SCHED;
  constexpr CvSched<SUB> S{};
  static_assert(S.slack >= 4, "fragment read scheduled too late");
#pragma unroll
  for (int t = 0; t < 16; ++t) {
    if ((((t + 1) >> 1) & 1) != SUB) continue;
#pragma unroll
    for (int r = 0; r < 4; ++r)
      if (t > S.wt || (t == S.wt && r >= S.wc)) *(float*)(qb + r * (CV_Q + 4) + t * 512) = acc[t][r];
  }
}
template <int SUB>
__device__ __forceinline__ void conv_mfma(const char* ub, const char* kb, char* qb) {
  conv_mfma_impl<SUB>(ub, kb, qb, make_iseq<CV_R0>{}, make_iseq<CV_NM>{});
}

__global__ __launch_bounds__(512, 2) void conv_kernel(
    const u16* __restrict__ xT, const float* __restrict__ stats, const u16* __restrict__ Kh,
    const u16* __restrict__ Kl, const float* __restrict__ ksc, const u16* __restrict__ dskip,
    u16* __restrict__ outT) {
  extern __shared__ __attribute__((aligned(16))) char smem[];
  const int tid = threadIdx.x, lane = tid & 63;
  const int wid = __builtin_amdgcn_readfirstlane(tid >> 6);
  STAMP_DECL STAMP(0);
  const int d0 = blockIdx.x * 2;
  const int t0 = tid * 4;
  u2 xr[2];
  u4 kr[KTERMS];
  f4 sr[2];
#pragma unroll
  for (int i = 0; i < 2; ++i) sr[i] = *(const f4*)(stats + t0 * 2 + i * 4);
#pragma unroll
  for (int c = 0; c < 2; ++c) xr[c] = *(const u2*)(xT + xoff(d0 + c, t0));
  kr[0] = *(const u4*)(Kh + (size_t)d0 * SL + tid * 8);
  if (KTERMS > 1) kr[KTERMS - 1] = *(const u4*)(Kl + (size_t)d0 * SL + tid * 8);
  const float2 isc2 = *(const float2*)(ksc + d0);
  const unsigned ds2 = *(const unsigned*)(dskip + d0);
  if (tid < 14 * 2 * KTERMS) {
    int which = tid / 14, j = tid % 14;
    *(u4*)(smem + CV_OFF_K + which * CV_K + j * 16) = u4{0, 0, 0, 0};
  } else if (tid >= 64 && tid < 70) {
    int c = (tid - 64) / 3, j = (tid - 64) % 3;
    *(u2*)(smem + c * CV_U + j * 8) = u2{0, 0};
  } else if (tid >= 128 && tid < 136) {
    *(f4*)(smem + CV_OFF_Y + (tid - 128) * CV_Q) = f4{0, 0, 0, 0};
  }
  STAMP_WAITV STAMP(1);
  float mb[4], ri[4];
#pragma unroll
  for (int j = 0; j < 4; ++j) ln_consts(sr[j >> 1][(j & 1) * 2], sr[j >> 1][(j & 1) * 2 + 1], mb[j], ri[j]);
  u2 ub16[2];
#pragma unroll
  for (int c = 0; c < 2; ++c) {
    unsigned d01 = pack_bf(bflo(xr[c][0]) - mb[0], bfhi(xr[c][0]) - mb[1]);
    unsigned d23 = pack_bf(bflo(xr[c][1]) - mb[2], bfhi(xr[c][1]) - mb[3]);
    ub16[c][0] = pack_bf(bflo(d01) * ri[0], bfhi(d01) * ri[1]);
    ub16[c][1] = pack_bf(bflo(d23) * ri[2], bfhi(d23) * ri[3]);
    u2 a = {pack_h(bflo(ub16[c][0]), bfhi(ub16[c][0])), pack_h(bflo(ub16[c][1]), bfhi(ub16[c][1]))};
    *(u2*)(smem + c * CV_U + 24 + tid * 8) = a;
  }
#pragma unroll
  for (int k = 0; k < KTERMS; ++k)
    *(u4*)(smem + CV_OFF_K + ((tid >> 8) * KTERMS + k) * CV_K + 224 + (tid & 255) * 16) = kr[k];
  __syncthreads();
  STAMP(2);
  {
    const int ch = wid >> 2, par = (wid >> 1) & 1;
    const int g = lane >> 4, q = (lane >> 2) & 3, p = lane & 3;
    const char* ub = smem + ch * CV_U + 8 * (p + q + 8 * g);
    const char* kb = smem + CV_OFF_K + ch * KTERMS * CV_K + 8 * (31 + 16 * par + 4 * p - 8 * g - q) - 32;
    char* qb = smem + CV_OFF_Y + ch * 4 * CV_Q + par * 256 + lane * 4;
    if (wid & 1) conv_mfma<1>(ub, kb, qb);
    else conv_mfma<0>(ub, kb, qb);
  }
  STAMP(3);
  __syncthreads();
  STAMP(4);
#pragma unroll
  for (int c = 0; c < 2; ++c) {
    const char* Q = smem + CV_OFF_Y + c * 4 * CV_Q + t0 * 4;
    f4 y0 = (*(const f4*)(Q) + *(const f4*)(Q + CV_Q)) + (*(const f4*)(Q + 2 * CV_Q) + *(const f4*)(Q + 3 * CV_Q));
    float isc = c ? isc2.y : isc2.x;
    float ds = c ? bfhi(ds2) : bflo(ds2);
    unsigned y01 = pack_bf(y0[0] * isc, y0[1] * isc), y23 = pack_bf(y0[2] * isc, y0[3] * isc);
    unsigned s01 = pack_bf(bflo(ub16[c][0]) * ds, bfhi(ub16[c][0]) * ds), s23 = pack_bf(bflo(ub16[c][1]) * ds, bfhi(ub16[c][1]) * ds);
    unsigned a01 = pack_bf(bflo(y01) + bflo(s01), bfhi(y01) + bfhi(s01)), a23 = pack_bf(bflo(y23) + bflo(s23), bfhi(y23) + bfhi(s23));
    u2 ov = {pack_bf(bflo(xr[c][0]) + bflo(a01), bfhi(xr[c][0]) + bfhi(a01)), pack_bf(bflo(xr[c][1]) + bflo(a23), bfhi(xr[c][1]) + bfhi(a23))};
    st8(outT + xoff(d0 + c, t0), ov);
  }
  STAMP(5); STAMP_WAITV STAMP(6); STAMP_OUT(1);
}

constexpr int G1_SMEM = 512 * 256 + 512 * 8;

__device__ __forceinline__ void glds16(const void* gsrc, unsigned lds_dst) {
  unsigned keep;
  asm volatile("s_mov_b32 %0, m0\n\ts_mov_b32 m0, %2\n\ts_nop 0\n\tglobal_load_lds_dwordx4 %1, off\n\ts_mov_b32 m0, %0"
               : "=&s"(keep) : "v"(gsrc), "s"(lds_dst) : "memory");
}
template <int OFF>
__device__ __forceinline__ void gload16(u4& dst, const void* p) {
  asm volatile("global_load_dwordx4 %0, %1, off offset:%2" : "=v"(dst) : "v"(p), "i"(OFF) : "memory");
}
template <int N>
__device__ __forceinline__ void wait_w(u4& a, u4& b, u4& c, u4& d) {
  asm volatile("s_waitcnt vmcnt(%4)\n\ts_barrier" : "+v"(a), "+v"(b), "+v"(c), "+v"(d) : "i"(N) : "memory");
}

template <int N>
__device__ __forceinline__ void wait_only(u4& a) {
  asm volatile("s_waitcnt vmcnt(%1)" : "+v"(a) : "i"(N) : "memory");
}
template <int N>
__device__ __forceinline__ void wait_vm() { asm volatile("s_waitcnt vmcnt(%0)" :: "i"(N) : "memory"); }
__device__ __forceinline__ void add1(float& a, float b) { asm("v_add_f32 %0, %0, %1" : "+v"(a) : "v"(b)); }
__device__ __forceinline__ void sqa1(float& a, float b) { asm("v_fmac_f32 %0, %1, %1" : "+v"(a) : "v"(b)); }
__device__ __forceinline__ void sth(void* p, u4 v) {
  st16(p, v);
}
__device__ __forceinline__ void bar() { asm volatile("s_waitcnt lgkmcnt(0)\n\ts_barrier" ::: "memory"); }
constexpr int G1_ST = 4 * 32768;
#define X_EPI 1
#define G1_XPF 3
#define G1_WPF 8

__global__ __launch_bounds__(512, 2) void gemm1_kernel(
    const u16* __restrict__ xT, const u16* __restrict__ W1F, const float* __restrict__ c1,
    u16* __restrict__ HF, float* __restrict__ pst) {
  extern __shared__ __attribute__((aligned(16))) char smem[];
  const int tid = threadIdx.x, lane = tid & 63;
  const int wid = __builtin_amdgcn_readfirstlane(tid >> 6);
  STAMP_DECL STAMP(0);
  const int bx = blockIdx.x, xj = bx & 7, r = bx >> 3;
  const int tp = 4 * (xj >> 1) + (r & 3);
  const int fp = 7 * (xj & 1) + (r >> 2);
  const int h = lane >> 5, blk = (lane >> 4) & 1, q = (lane >> 2) & 3, p = lane & 3;
  const char* rbase = smem + (8 * h + q) * 64 + blk * 32 + 8 * p;
  float* st = (float*)(smem + G1_ST);
  if (wid >= 4) {
    const int hq = wid - 4;
    const char* xsrc = (const char*)xT + (size_t)tp * 131072 + hq * 1024 + lane * 16;
    const unsigned ldst = (unsigned)(size_t)smem + hq * 1024;
    auto issue_c = [&](int c) AINL {
#pragma unroll
      for (int i = 0; i < 3; ++i) glds16(xsrc + c * 4096 + i * 32768, ldst + c * 4096 + i * 32768);
    };
#pragma unroll
    for (int c = 0; c < G1_XPF; ++c) issue_c(c);
#pragma unroll 1
    for (int c = 0; c < 8 - G1_XPF; ++c) {
      wait_vm<3 * (G1_XPF - 1)>(); bar();
      issue_c(c + G1_XPF);
    }
    sfor<G1_XPF>([&](auto I) AINL {
      constexpr int i = decltype(I)::value;
      if constexpr (i == 0) wait_vm<3 * (G1_XPF - 1)>(); else wait_vm<3 * (G1_XPF - 1 - i) + 8>();
      bar();
      if constexpr (i == 0) {
#pragma unroll
        for (int k = 0; k < 8; ++k) glds16(xsrc + 3 * 32768 + k * 4096, ldst + 3 * 32768 + k * 4096);
      }
    });
    wait_vm<0>();
    bar();
#pragma unroll 1
    for (int it = 0; it < 2; ++it) {
      const int u = it == 0 ? 12 + hq : (fp - 1) * 4 + hq;
      if (it == 1 && (fp < 1 || fp > 3)) break;
      const char* rq = rbase + u * 8192;
      s4 fr[8][2];
      sfor<8>([&](auto I) AINL {
        constexpr int i = decltype(I)::value;
        fr[i][0] = tr_read(rq + i * 1024);
        fr[i][1] = tr_read(rq + i * 1024 + 256);
      });
      float sa = 0.f, sb = 0.f, qa = 0.f, qb = 0.f;
      sfor<8>([&](auto I) AINL {
        constexpr int i = decltype(I)::value;
        u4 xv = __builtin_bit_cast(u4, cat8(fr[i][0], fr[i][1]));
#pragma unroll
        for (int j = 0; j < 4; ++j) {
          float lo = bflo(xv[j]), hi = bfhi(xv[j]);
          add1(sa, lo); add1(sb, hi); sqa1(qa, lo); sqa1(qb, hi);
        }
      });
      float s0 = sa + sb, ss0 = qa + qb;
      s0 += __shfl_xor(s0, 32);
      ss0 += __shfl_xor(ss0, 32);
      if (it == 0) {
        if (lane < 32) st[128 + hq * 32 + lane] = s0;
        bar();
      }
      if (lane < 32 && (it == 1 || fp == 0)) *(float2*)(pst + ((tp * 128 + (u >> 2) * 32 + lane) * 4 + (u & 3)) * 2) = make_float2(s0, ss0);
    }
    return;
  }
  const int ft = fp * 4 + wid;
  u4 wf[32];
  u4 cfu[4];
  const char* wsrc = (const char*)W1F + ((size_t)ft * 32 * 64 + lane) * 16;
  auto issue_w = [&](auto S) AINL {
    constexpr int s = decltype(S)::value;
    gload16<(s & 3) * 1024>(wf[s], wsrc + (s >> 2) * 4096);
  };
  sfor<G1_WPF>(issue_w);
  f16v acc[4], asum;
  s4 xa[3][3][2];
  constexpr int XD = 4, XR = XD + 1;
  s4 xc[XR][2];
  u4 ov;
  float nm[4];
  const int w3 = wid == 3 ? 0 : wid;
  int tto[3];
#pragma unroll
  for (int i = 0; i < 3; ++i) { int tt = i + w3; tto[i] = tt >= 3 ? tt - 3 : tt; }
  const char* rb[3];
#pragma unroll
  for (int i = 0; i < 3; ++i) rb[i] = rbase + tto[i] * 32768;
  u16* dst = HF + ((size_t)((tp * 8 + blk) * 56 + ft) * 64 + (lane & 15) + 16 * h) * 8;
  auto epi_pair = [&](const f16v& a, float m, int e) AINL -> unsigned {
    f4 cf = __builtin_bit_cast(f4, cfu[e >> 2]);
    unsigned pu = pack_bf(fmaf(cf[e & 3], m, a[e]), fmaf(cf[(e & 3) + 1], m, a[e + 1]));
    asm("" : "+v"(pu));
    return __builtin_bit_cast(unsigned, __builtin_elementwise_max(__builtin_bit_cast(s2v, pu), s2v{0, 0}));
  };
  f16v z;
#pragma unroll
  for (int j = 0; j < 16; ++j) z[j] = 0.f;
  const s8 ones8 = {0x3f80, 0x3f80, 0x3f80, 0x3f80, 0x3f80, 0x3f80, 0x3f80, 0x3f80};
  SCHED;
  asm volatile("s_barrier" ::: "memory");
  STAMP(1);
#pragma unroll
  for (int k = 0; k < 2; ++k)
#pragma unroll
    for (int i = 0; i < 3; ++i) { xa[k][i][0] = tr_read(rb[i] + k * 1024); xa[k][i][1] = tr_read(rb[i] + k * 1024 + 256); }
  SCHED;
  sfor<32>([&](auto S) AINL {
    constexpr int s = decltype(S)::value;
    constexpr int issued = (G1_WPF + s <= 32) ? G1_WPF + s : 36;
    asum = __builtin_amdgcn_mfma_f32_32x32x16_bf16(__builtin_bit_cast(b8, ones8), __builtin_bit_cast(b8, cat8(xa[s % 3][0][0], xa[s % 3][0][1])), s == 0 ? z : asum, 0, 0, 0);
    wait_only<issued - s - 1>(wf[s]);
    if constexpr ((s & 3) == 2 && s < 30) asm volatile("s_barrier" ::: "memory");
    SCHED;
    sfor<3>([&](auto I) AINL {
      constexpr int i = decltype(I)::value;
      const b8 xv = __builtin_bit_cast(b8, cat8(xa[s % 3][i][0], xa[s % 3][i][1]));
      acc[i] = __builtin_amdgcn_mfma_f32_32x32x16_bf16(__builtin_bit_cast(b8, wf[s]), xv, s == 0 ? z : acc[i], 0, 0, 0);
      if constexpr (s + 2 < 32) {
        xa[(s + 2) % 3][i][0] = tr_read(rb[i] + (s + 2) * 1024);
        xa[(s + 2) % 3][i][1] = tr_read(rb[i] + (s + 2) * 1024 + 256);
      }
      if constexpr (i == 0) {
        if constexpr (G1_WPF + s < 32) issue_w(ic<(G1_WPF + s < 32) ? G1_WPF + s : 0>{});
        if constexpr (G1_WPF + s == 32) {
          gload16<0>(cfu[0], c1 + ft * 32 + 4 * h);
          gload16<32>(cfu[1], c1 + ft * 32 + 4 * h);
          gload16<64>(cfu[2], c1 + ft * 32 + 4 * h);
          gload16<96>(cfu[3], c1 + ft * 32 + 4 * h);
        }
      }
      SCHED;
    });
  });
  STAMP(2);
  if (lane < 32) st[wid * 32 + lane] = -rbf(asum[0] * (1.0f / DM));
  asm volatile("s_waitcnt vmcnt(0) lgkmcnt(0)\n\ts_barrier" : "+v"(cfu[0]), "+v"(cfu[1]), "+v"(cfu[2]), "+v"(cfu[3]) :: "memory");
  STAMP(3);
  {
    unsigned rbo = (unsigned)(rbase - smem) + 3 * 32768;
    asm volatile("" : "+v"(rbo));
    const char* rbb = smem + rbo;
    sfor<XD>([&](auto I) AINL {
      constexpr int i = decltype(I)::value;
      xc[i][0] = tr_read(rbb + i * 1024); xc[i][1] = tr_read(rbb + i * 1024 + 256);
    });
#pragma unroll
    for (int i = 0; i < 3; ++i) nm[i] = st[tto[i] * 32 + (lane & 31)];
    SCHED;
    sfor<32>([&](auto S) AINL {
      constexpr int s = decltype(S)::value;
      const b8 xv = __builtin_bit_cast(b8, cat8(xc[s % XR][0], xc[s % XR][1]));
      acc[3] = __builtin_amdgcn_mfma_f32_32x32x16_bf16(__builtin_bit_cast(b8, wf[s]), xv, s == 0 ? z : acc[3], 0, 0, 0);
      if constexpr (s + XD < 32) {
        xc[(s + XD) % XR][0] = tr_read(rbb + (s + XD) * 1024);
        xc[(s + XD) % XR][1] = tr_read(rbb + (s + XD) * 1024 + 256);
      }
      if constexpr (s < 24) {
        constexpr int i = s >> 3, pr = s & 7;
        ov[pr & 3] = epi_pair(acc[i], nm[i], 2 * pr);
        if constexpr ((pr & 3) == 3) sth(dst + tto[i] * (2 * 56 * 512) + (pr >> 2) * 256, ov);
      }
      SCHED;
    });
  }
  STAMP(4);
  asm volatile("s_barrier" ::: "memory");
  STAMP(5);
  {
    const float* sp = st + 128 + (lane & 31);
    nm[3] = -rbf(((sp[0] + sp[32]) + (sp[64] + sp[96])) * (1.0f / DM));
  }
#pragma unroll
  for (int c = 0; c < 2; ++c) {
#pragma unroll
    for (int j = 0; j < 4; ++j) ov[j] = epi_pair(acc[3], nm[3], 8 * c + 2 * j);
    sth(dst + 3 * (2 * 56 * 512) + c * 256, ov);
  }
  STAMP(6); STAMP_WAITV STAMP(7); STAMP_OUT(2);
}

template <bool FINAL>
__global__ __launch_bounds__(256, 1) void gemm2_kernel(
    const u16* __restrict__ HF, const u16* __restrict__ W2F, const u16* __restrict__ xT,
    u16* __restrict__ out, float* __restrict__ stats, const float* __restrict__ pst) {
  __shared__ __attribute__((aligned(16))) f4 red[4 * 3 * 4 * 64];
  const int tid = threadIdx.x, lane = tid & 63;
  const int wid = __builtin_amdgcn_readfirstlane(tid >> 6);
  const int bx = blockIdx.x, xj = bx & 7, r = bx >> 3;
  STAMP_DECL STAMP(0);
  const int tb = 8 * (xj >> 1) + (r & 7), db = 4 * (xj & 1) + (r >> 3);
  const int g = lane >> 4, n = lane & 15;
  constexpr int NK = 14, PD = 5;
  const u16* ap[4];
  const u16* bp[4];
#pragma unroll
  for (int i = 0; i < 4; ++i) {
    int mt = (i + wid) & 3;
    ap[i] = HF + (size_t)((tb * 4 + mt) * 56 + wid * NK) * 512 + lane * 8;
    bp[i] = W2F + (size_t)((db * 4 + i) * 56 + wid * NK) * 512 + lane * 8;
  }
  s8 a[NK][4], b[NK][4];
#pragma unroll
  for (int ks = 0; ks < PD; ++ks)
#pragma unroll
    for (int i = 0; i < 4; ++i) { a[ks][i] = *(const s8*)(ap[i] + ks * 512); b[ks][i] = *(const s8*)(bp[i] + ks * 512); }
  f4 acc[4][4];
#pragma unroll
  for (int i = 0; i < 4; ++i)
#pragma unroll
    for (int j = 0; j < 4; ++j) acc[i][j] = f4{0, 0, 0, 0};
  u2 res[4];
  u16 resf[4][4];
  const float2 ps = *(const float2*)(pst + ((tb * 64 + wid * 16 + n) * 4 + g) * 2);
  SCHED;
#pragma unroll
  for (int ks = 0; ks < NK; ++ks) {
    if (ks + PD < NK) {
#pragma unroll
      for (int i = 0; i < 4; ++i) { a[ks + PD][i] = *(const s8*)(ap[i] + (ks + PD) * 512); b[ks + PD][i] = *(const s8*)(bp[i] + (ks + PD) * 512); }
    }
    if (ks + PD == NK) {
      if (!FINAL) {
#pragma unroll
        for (int j = 0; j < 4; ++j) res[j] = *(const u2*)(xT + xoff(db * 64 + j * 16 + n, tb * 64 + wid * 16 + 4 * g));
      } else {
#pragma unroll
        for (int j = 0; j < 4; ++j) {
#pragma unroll
          for (int e = 0; e < 4; ++e) resf[j][e] = xT[xoff(db * 64 + j * 16 + 4 * g + e, tb * 64 + wid * 16 + n)];
        }
      }
    }
    SCHED;
#pragma unroll
    for (int i = 0; i < 4; ++i)
#pragma unroll
      for (int j = 0; j < 4; ++j) {
        if (!FINAL) acc[i][j] = __builtin_amdgcn_mfma_f32_16x16x32_bf16(__builtin_bit_cast(b8, a[ks][i]), __builtin_bit_cast(b8, b[ks][j]), acc[i][j], 0, 0, 0);
        else acc[i][j] = __builtin_amdgcn_mfma_f32_16x16x32_bf16(__builtin_bit_cast(b8, b[ks][j]), __builtin_bit_cast(b8, a[ks][i]), acc[i][j], 0, 0, 0);
      }
    SCHED;
  }
  STAMP(2);
  f4 rr;
  {
    float sx = ps.x, sy = ps.y;
    sx += __shfl_xor(sx, 16); sy += __shfl_xor(sy, 16);
    sx += __shfl_xor(sx, 32); sy += __shfl_xor(sy, 32);
    float mb, ri;
    ln_consts(sx, sy, mb, ri);
    if (FINAL) rr = f4{ri, ri, ri, ri};
    else {
#pragma unroll
      for (int e = 0; e < 4; ++e) rr[e] = __shfl(ri, 4 * g + e);
    }
  }
#pragma unroll
  for (int i = 1; i < 4; ++i) {
    int mt = (i + wid) & 3;
#pragma unroll
    for (int j = 0; j < 4; ++j) red[((mt * 3 + i - 1) * 4 + j) * 64 + lane] = acc[i][j];
  }
  __syncthreads();
  STAMP(3);
#pragma unroll
  for (int j = 0; j < 4; ++j) {
    acc[0][j] += red[((wid * 3 + 0) * 4 + j) * 64 + lane];
    acc[0][j] += red[((wid * 3 + 1) * 4 + j) * 64 + lane] + red[((wid * 3 + 2) * 4 + j) * 64 + lane];
  }
  float s[4] = {0, 0, 0, 0}, ss[4] = {0, 0, 0, 0};
#pragma unroll
  for (int j = 0; j < 4; ++j) {
    acc[0][j] *= rr;
    unsigned m01 = pack_bf(acc[0][j][0], acc[0][j][1]), m23 = pack_bf(acc[0][j][2], acc[0][j][3]);
    u2 ov;
    if (!FINAL) {
      ov[0] = pack_bf(bflo(res[j][0]) + bflo(m01), bfhi(res[j][0]) + bfhi(m01));
      ov[1] = pack_bf(bflo(res[j][1]) + bflo(m23), bfhi(res[j][1]) + bfhi(m23));
      *(u2*)(out + xoff(db * 64 + j * 16 + n, tb * 64 + wid * 16 + 4 * g)) = ov;
      float o[4] = {bflo(ov[0]), bfhi(ov[0]), bflo(ov[1]), bfhi(ov[1])};
#pragma unroll
      for (int e = 0; e < 4; ++e) { s[e] += o[e]; ss[e] = fmaf(o[e], o[e], ss[e]); }
    } else {
      ov[0] = pack_bf(bf2f(resf[j][0]) + bflo(m01), bf2f(resf[j][1]) + bfhi(m01));
      ov[1] = pack_bf(bf2f(resf[j][2]) + bflo(m23), bf2f(resf[j][3]) + bfhi(m23));
      *(u2*)(out + (size_t)(tb * 64 + wid * 16 + n) * DM + db * 64 + j * 16 + 4 * g) = ov;
    }
  }
  if (!FINAL) {
#pragma unroll
    for (int e = 0; e < 4; ++e) { s[e] = row_sum16(s[e]); ss[e] = row_sum16(ss[e]); }
    if (n < 8) {
      int e = n >> 1;
      float v = (n & 1) ? (e == 0 ? ss[0] : e == 1 ? ss[1] : e == 2 ? ss[2] : ss[3]) : (e == 0 ? s[0] : e == 1 ? s[1] : e == 2 ? s[2] : s[3]);
      atomicAdd(stats + (tb * 64 + wid * 16 + 4 * g) * 2 + n, v);
    }
  }
  STAMP(4); STAMP_WAITV STAMP(5); STAMP_OUT(FINAL ? 4 : 3);
}

extern "C" void kernel_launch(void* const* d_in, const int* in_sizes, int n_in,
                              void* d_out, int out_size, void* d_ws, size_t ws_size,
                              hipStream_t stream) {
  const u16* x = (const u16*)d_in[0];
  const u16* logA = (const u16*)d_in[1];
  const u16* Aim = (const u16*)d_in[2];
  const u16* Cre = (const u16*)d_in[3];
  const u16* Cim = (const u16*)d_in[4];
  const u16* logdt = (const u16*)d_in[5];
  const u16* dskip = (const u16*)d_in[6];
  const u16* w1 = (const u16*)d_in[8];
  const u16* w2 = (const u16*)d_in[9];
  char* ws = (char*)d_ws;
  u16* out = (u16*)d_out;
  (void)hipFuncSetAttribute((const void*)gemm1_kernel, hipFuncAttributeMaxDynamicSharedMemorySize, G1_SMEM);
  (void)hipFuncSetAttribute((const void*)conv_kernel, hipFuncAttributeMaxDynamicSharedMemorySize, CV_SMEM);

  prep_kernel<<<256, 512, 0, stream>>>(x, logA, Aim, Cre, Cim, logdt, w1, w2, ws);
  conv_kernel<<<256, 512, CV_SMEM, stream>>>((const u16*)(ws + WS_XT0), (const float*)(ws + WS_ST0), (const u16*)(ws + WS_KH),
                                       (const u16*)(ws + WS_KL), (const float*)(ws + WS_KSC), dskip, (u16*)(ws + WS_XT1));
  gemm1_kernel<<<224, 512, G1_SMEM, stream>>>((const u16*)(ws + WS_XT1), (const u16*)(ws + WS_W1F), (const float*)(ws + WS_C1), (u16*)(ws + WS_HF), (float*)(ws + WS_PST));
  gemm2_kernel<false><<<256, 256, 0, stream>>>((const u16*)(ws + WS_HF), (const u16*)(ws + WS_W2F), (const u16*)(ws + WS_XT1),
                                               (u16*)(ws + WS_XT2), (float*)(ws + WS_ST2), (const float*)(ws + WS_PST));
  conv_kernel<<<256, 512, CV_SMEM, stream>>>((const u16*)(ws + WS_XT2), (const float*)(ws + WS_ST2), (const u16*)(ws + WS_KH),
                                       (const u16*)(ws + WS_KL), (const float*)(ws + WS_KSC), dskip, (u16*)(ws + WS_XT3));
  gemm1_kernel<<<224, 512, G1_SMEM, stream>>>((const u16*)(ws + WS_XT3), (const u16*)(ws + WS_W1F), (const float*)(ws + WS_C1), (u16*)(ws + WS_HF2), (float*)(ws + WS_PST2));
  gemm2_kernel<true><<<256, 256, 0, stream>>>((const u16*)(ws + WS_HF2), (const u16*)(ws + WS_W2F), (const u16*)(ws + WS_XT3),
                                              out, nullptr, (const float*)(ws + WS_PST2));
}
```

```cpp
#include <hip/hip_runtime.h>
#include <hip/hip_bf16.h>

typedef unsigned short u16;
typedef short s4 __attribute__((ext_vector_type(4)));
typedef short s2v __attribute__((ext_vector_type(2)));
typedef short s8 __attribute__((ext_vector_type(8)));
typedef _Float16 h8 __attribute__((ext_vector_type(8)));
typedef _Float16 h2 __attribute__((ext_vector_type(2)));
typedef __bf16 b2 __attribute__((ext_vector_type(2)));
typedef __bf16 b8 __attribute__((ext_vector_type(8)));
typedef float f4 __attribute__((ext_vector_type(4)));
typedef float f2 __attribute__((ext_vector_type(2)));
typedef __bf16 b4 __attribute__((ext_vector_type(4)));
typedef float f16v __attribute__((ext_vector_type(16)));
typedef unsigned u4 __attribute__((ext_vector_type(4)));
typedef unsigned u2 __attribute__((ext_vector_type(2)));
typedef __attribute__((address_space(3))) s4* lds_s4p;

#define STAMP_DECL
#define STAMP(i)
#define STAMP_WAITV
#define STAMP_OUT(k)
#define SCHED __builtin_amdgcn_sched_barrier(0)
template <int... Is> struct iseq {};
template <int N, int... Is> struct mk_iseq : mk_iseq<N - 1, N - 1, Is...> {};
template <int... Is> struct mk_iseq<0, Is...> { typedef iseq<Is...> type; };
template <int N> using make_iseq = typename mk_iseq<N>::type;
template <int I> struct ic { static constexpr int value = I; };
template <class F, int... Is> __device__ __forceinline__ void sfor_impl(F&& f, iseq<Is...>) { (f(ic<Is>{}), ...); }
template <int N, class F> __device__ __forceinline__ void sfor(F&& f) { sfor_impl(f, make_iseq<N>{}); }
#define AINL __attribute__((always_inline))

constexpr int SL = 2048;
constexpr int DM = 512;
constexpr int NS = 128;
constexpr int FF = 1792;
#define KTERMS 1

constexpr size_t WS_XT0 = 0;
constexpr size_t WS_XT1 = WS_XT0 + (size_t)SL * DM * 2;
constexpr size_t WS_XT2 = WS_XT1 + (size_t)SL * DM * 2;
constexpr size_t WS_XT3 = WS_XT2 + (size_t)SL * DM * 2;
constexpr size_t WS_KH  = WS_XT3 + (size_t)SL * DM * 2;
constexpr size_t WS_KL  = WS_KH + (size_t)SL * DM * 2;
constexpr size_t WS_W1F = WS_KL + (size_t)SL * DM * 2;
constexpr size_t WS_W2F = WS_W1F + (size_t)DM * FF * 2;
constexpr size_t WS_HF  = WS_W2F + (size_t)DM * FF * 2;
constexpr size_t WS_HF2 = WS_HF + (size_t)SL * FF * 2;
constexpr size_t WS_ST0 = WS_HF2 + (size_t)SL * FF * 2;
constexpr size_t WS_ST2 = WS_ST0 + (size_t)SL * 8;
constexpr size_t WS_C1  = WS_ST2 + (size_t)SL * 8;
constexpr size_t WS_KSC = WS_C1 + (size_t)FF * 4;
constexpr size_t WS_PST = WS_KSC + (size_t)DM * 4;
constexpr size_t WS_PST2 = WS_PST + (size_t)SL * 32;
constexpr size_t WS_CNT = WS_PST2 + (size_t)SL * 32;

__host__ __device__ __forceinline__ int xoff(int d, int t) { return (t >> 5) * 16384 + d * 32 + (t & 31); }
__device__ __forceinline__ float bf2f(unsigned h) { return __uint_as_float(h << 16); }
__device__ __forceinline__ float bflo(unsigned w) { return __uint_as_float(w << 16); }
__device__ __forceinline__ float bfhi(unsigned w) { return __uint_as_float(w & 0xffff0000u); }
__device__ __forceinline__ unsigned pack_bf(float a, float b) {
  b2 v = {(__bf16)a, (__bf16)b};
  return __builtin_bit_cast(unsigned, v);
}
__device__ __forceinline__ float rbf(float f) {
  return __uint_as_float(((unsigned)__builtin_bit_cast(u16, (__bf16)f)) << 16);
}
__device__ __forceinline__ unsigned pack_h(float a, float b) {
  h2 v = {(_Float16)a, (_Float16)b};
  return __builtin_bit_cast(unsigned, v);
}
__device__ __forceinline__ float dot2(unsigned a, unsigned b, float c) {
  return __builtin_amdgcn_fdot2_f32_bf16(__builtin_bit_cast(b2, a), __builtin_bit_cast(b2, b), c, false);
}
__device__ __forceinline__ s4 tr_read(const char* p) {
  return __builtin_amdgcn_ds_read_tr16_b64_v4i16((lds_s4p)p);
}
__device__ __forceinline__ s8 cat8(s4 a, s4 b) { return __builtin_shufflevector(a, b, 0, 1, 2, 3, 4, 5, 6, 7); }
template <int CTRL>
__device__ __forceinline__ float dppf(float v) {
  return __builtin_bit_cast(float, __builtin_amdgcn_update_dpp(0, __builtin_bit_cast(int, v), CTRL, 0xf, 0xf, true));
}
__device__ __forceinline__ float shr1(float v) { return dppf<0x138>(v); }
__device__ __forceinline__ float row_sum16(float v) {
  v += dppf<0x128>(v);
  v += dppf<0x124>(v);
  v += dppf<0x122>(v);
  v += dppf<0x121>(v);
  return v;
}
constexpr unsigned BF_ONES = 0x3f803f80u;
__device__ __forceinline__ void st16(void* p, u4 v) { asm volatile("global_store_dwordx4 %0, %1, off sc1\n\ts_nop 1" :: "v"(p), "v"(v) : "memory"); }
__device__ __forceinline__ void st8(void* p, u2 v) { asm volatile("global_store_dwordx2 %0, %1, off sc1\n\ts_nop 0" :: "v"(p), "v"(v) : "memory"); }

__device__ __forceinline__ void ln_consts(float s, float ss, float& mb, float& rinv) {
  float mean = s * (1.0f / DM);
  float var = fmaxf(ss * (1.0f / DM) - mean * mean, 0.0f);
  mb = rbf(mean);
  float sd = rbf(__builtin_amdgcn_sqrtf(rbf(rbf(var) + 1e-5f)));
  rinv = __builtin_amdgcn_rcpf(sd);
}

constexpr int PREP_W1 = 56, PREP_W2 = 56, PREP_TR = 64;
constexpr int PK_PAR = 0, PK_PARC = 2048, PK_RED = 3072, PK_KV = PK_RED + 12288, PK_MX = PK_KV + 3072, PK_SZ = PK_MX + 128;
constexpr int PREP_SMEM = 32768 + 1024 + 2 * PK_SZ;

__device__ __forceinline__ int alpha_of(int idx) {
  if (idx < 16) return (idx >> 3) * 128 + (idx & 7);
  if (idx < 24) { int j = idx - 16; return (2 + (j >> 2)) * 128 + (j & 3) * 2; }
  if (idx < 32) { int j = idx - 24; return (4 + (j >> 1)) * 128 + (j & 1) * 4; }
  if (idx < 41) return (8 + idx - 32) * 128;
  return 0;
}
__device__ __forceinline__ float exp_acc(float x) {
  const float L2EH = 1.44269504f, L2EL = (float)(1.4426950408889634 - (double)1.44269504f);
  float t = x * L2EH;
  float e = fmaf(x, L2EH, -t) + x * L2EL;
  float r = __builtin_amdgcn_exp2f(t);
  return fmaf(r, e * 0.69314718f, r);
}
__device__ __forceinline__ float row_max16(float v) {
  v = fmaxf(v, dppf<0x128>(v));
  v = fmaxf(v, dppf<0x124>(v));
  v = fmaxf(v, dppf<0x122>(v));
  v = fmaxf(v, dppf<0x121>(v));
  return v;
}
__device__ __forceinline__ void split_h(float a, float b, unsigned& hi, unsigned& lo) {
  float ah = __uint_as_float(__float_as_uint(a) & 0xffffe000u), bh = __uint_as_float(__float_as_uint(b) & 0xffffe000u);
  hi = pack_h(ah, bh);
  lo = pack_h(a - ah, b - bh);
}

__global__ __launch_bounds__(512, 2) void prep_kernel(
    const u16* __restrict__ x, const u16* __restrict__ logA, const u16* __restrict__ Aim,
    const u16* __restrict__ Cre, const u16* __restrict__ Cim, const u16* __restrict__ logdt,
    const u16* __restrict__ w1, const u16* __restrict__ w2, char* __restrict__ ws) {
  __shared__ __attribute__((aligned(16))) char smem[PREP_SMEM];
  const int tid = threadIdx.x, lane = tid & 63;
  const int wid = __builtin_amdgcn_readfirstlane(tid >> 6);
  STAMP_DECL STAMP(0);
  const int b = blockIdx.x;
  const int job = b < PREP_W1 ? 0 : (b < PREP_W1 + PREP_W2 ? 1 : (b < PREP_W1 + PREP_W2 + PREP_TR ? 2 : 3));
  const int jb = job == 0 ? b : (job == 1 ? b - PREP_W1 : b - PREP_W1 - PREP_W2);
  const int ch = wid >> 2, w4 = wid & 3, t8 = tid & 255;
  const int d = b * 2 + ch;
  const int idx = lane & 15, g = lane >> 4;
  const int pn = d * NS + (t8 & (NS - 1));
  const u16 pl0 = logdt[d], pl1 = logA[pn], pl2 = Aim[pn], pl3 = Cre[pn], pl4 = Cim[pn];
  u4 v[4];
  int lofs;
  int ls1, ls2;
  {
    const int jx = job == 3 ? 0 : jb;
    const u16* src;
    int gs1, gs2;
    if (job == 0) {
      src = w1 + (size_t)(tid >> 2) * FF + jx * 32 + (tid & 3) * 8;
      gs1 = 128 * FF; gs2 = 256 * FF; lofs = tid * 16; ls1 = 8192; ls2 = 16384;
    } else if (job == 1) {
      src = w2 + (size_t)((jx >> 2) * 128 + (tid >> 4)) * DM + (jx & 3) * 128 + (tid & 15) * 8;
      gs1 = 32 * DM; gs2 = 64 * DM; lofs = (tid >> 4) * 256 + (((tid & 15) ^ (((tid >> 4) & 7) << 1)) * 16); ls1 = 8192; ls2 = 16384;
    } else {
      const int r0 = (wid >> 2) * 8 + (wid & 3);
      src = x + (size_t)(jx * 32 + r0) * DM + lane * 8;
      gs1 = 4 * DM; gs2 = 16 * DM; lofs = r0 * 1024 + ((lane ^ (wid << 1)) * 16); ls1 = 4096; ls2 = 16384;
    }
    v[0] = *(const u4*)(src); v[1] = *(const u4*)(src + gs1); v[2] = *(const u4*)(src + gs2); v[3] = *(const u4*)(src + gs1 + gs2);
  }
  int kidx[8];
#pragma unroll
  for (int j = 0; j < 8; ++j) {
    float pf = rbf((float)(t8 * 8 + j));
    int p = (int)pf;
    int e = (int)(__float_as_uint(pf) >> 23) - (127 + 7);
    e = e < 0 ? 0 : (e > 3 ? 3 : e);
    int ai_ = ((((p >> 7) << 3) + (p & 7)) >> e) + 8 * e;
    kidx[j] = ai_ * 16 + ((p & 127) >> 3);
  }
  char* ks = smem + 32768 + 1024 + ch * PK_SZ;
  f4* par = (f4*)(ks + PK_PAR);
  f2* parc = (f2*)(ks + PK_PARC);
  f4* red = (f4*)(ks + PK_RED);
  float* kv = (float*)(ks + PK_KV);
  float* mc = (float*)(ks + PK_MX);
  {
    float dt = rbf(exp_acc(bf2f(pl0)));
    float Ar = -rbf(exp_acc(bf2f(pl1)));
    float ar = Ar * dt;
    float ai = bf2f(pl2) * dt;
    const float I2PH = 0.15915494f;
    const float I2PL = (float)(0.15915494309189535 - (double)0.15915494f);
    float fh = ai * I2PH;
    float fl = fmaf(ai, I2PH, -fh) + ai * I2PL;
    fh -= rintf(fh);
    float fhi = __uint_as_float(__float_as_uint(fh) & 0xfffff000u);
    float flo = (fh - fhi) + fl;
    float crv = 2.0f * bf2f(pl3) * dt;
    float civ = 2.0f * bf2f(pl4) * dt;
    float cm = row_sum16(fabsf(crv) + fabsf(civ));
    if (t8 < NS) {
      par[t8] = f4{ar * 1.44269504f, fhi, flo, 0.f};
      parc[t8] = f2{crv, civ};
      if (idx == 0) mc[t8 >> 4] = cm;
    }
  }
  *(u4*)(smem + lofs) = v[0];
  *(u4*)(smem + lofs + ls1) = v[1];
  *(u4*)(smem + lofs + ls2) = v[2];
  *(u4*)(smem + lofs + ls1 + ls2) = v[3];
  __syncthreads();
  STAMP(1);
  const int q = (lane >> 2) & 3, p = lane & 3;
  float* csr = (float*)(smem + 32768);
  if (job == 0) {
    const int h = lane >> 5, blk = (lane >> 4) & 1;
    const char* rb = smem + (wid * 64 + 8 * h + q) * 64 + blk * 32 + p * 8;
    u16* W1F = (u16*)(ws + WS_W1F);
    float cs = 0.f;
#pragma unroll
    for (int i = 0; i < 4; ++i) {
      s8 o = cat8(tr_read(rb + i * 1024), tr_read(rb + i * 1024 + 256));
      u4 ou = __builtin_bit_cast(u4, o);
#pragma unroll
      for (int j = 0; j < 4; ++j) cs = dot2(ou[j], BF_ONES, cs);
      st16(W1F + ((size_t)(jb * 32 + wid * 4 + i) * 64 + lane) * 8, ou);
    }
    cs += __shfl_xor(cs, 32);
    if (lane < 32) csr[wid * 32 + lane] = cs;
  } else if (job == 1) {
    const int rl = 16 * (g >> 1) + 4 * (g & 1) + q;
    const int sw = rl & 7;
    u16* W2F = (u16*)(ws + WS_W2F);
#pragma unroll
    for (int i = 0; i < 4; ++i) {
      const int nt = (wid & 1) * 4 + i;
      const char* rb = smem + ((wid >> 1) * 32 + rl) * 256 + ((nt ^ sw) * 32) + p * 8;
      s8 o = cat8(tr_read(rb), tr_read(rb + 2048));
      st16(W2F + ((size_t)(((jb & 3) * 8 + nt) * 56 + (jb >> 2) * 4 + (wid >> 1)) * 64 + lane) * 8, __builtin_bit_cast(u4, o));
    }
  } else if (job == 2) {
    const int sw = q | ((g & 1) << 2);
    u16* xT = (u16*)(ws + WS_XT0);
#pragma unroll
    for (int i = 0; i < 4; ++i) {
      int dt_ = wid * 4 + i;
      const char* base = smem + (8 * g + q) * 1024 + ((dt_ ^ sw) * 32) + 8 * p;
      s8 o = cat8(tr_read(base), tr_read(base + 4096));
      *(s8*)(xT + xoff(dt_ * 16 + idx, jb * 32 + 8 * g)) = o;
    }
    float sv[4], ssv[4];
#pragma unroll
    for (int i = 0; i < 4; ++i) {
      float s = 0.f, ss = 0.f;
#pragma unroll
      for (int j = 0; j < 4; ++j) { s = dot2(v[i][j], BF_ONES, s); ss = dot2(v[i][j], v[i][j], ss); }
      sv[i] = row_sum16(s); ssv[i] = row_sum16(ss);
    }
    float val = sv[0];
#pragma unroll
    for (int i = 1; i < 4; ++i) val = (idx == 2 * i) ? sv[i] : val;
#pragma unroll
    for (int i = 0; i < 4; ++i) val = (idx == 2 * i + 1) ? ssv[i] : val;
    val += __shfl_xor(val, 16);
    val += __shfl_xor(val, 32);
    if (lane < 8) {
      const int i = lane >> 1;
      const int r = jb * 32 + (i >> 1) * 16 + (wid >> 2) * 8 + (i & 1) * 4 + (wid & 3);
      ((float*)(ws + WS_ST0))[r * 2 + (lane & 1)] = val;
      ((float*)(ws + WS_ST2))[r * 2 + (lane & 1)] = 0.f;
    }
  }
  STAMP(2);
  int ce;
  {
    f4 m0 = *(const f4*)(mc), m1 = *(const f4*)(mc + 4);
    float m = ((m0[0] + m0[1]) + (m0[2] + m0[3])) + ((m1[0] + m1[1]) + (m1[2] + m1[3]));
    ce = (int)((__float_as_uint(m) >> 23) & 0xff) - 127;
    ce = ce < -100 ? -100 : (ce > 100 ? 100 : ce);
  }
  {
    const f2 pos01 = {(float)(8 * idx), (float)alpha_of(idx)};
    const f2 pos23 = {(float)alpha_of(16 + idx), (float)alpha_of(32 + idx)};
    const f2 eo01 = {(float)(12 - ce), 13.f}, eo23 = {13.f, 13.f};
    f4 acc0 = {0, 0, 0, 0}, acc1 = {0, 0, 0, 0}, acc2 = {0, 0, 0, 0};
#pragma unroll
    for (int k2 = 0; k2 < 2; ++k2) {
      u4 hi[4], lo[4];
#pragma unroll
      for (int e = 0; e < 4; ++e) {
        const int n = (w4 * 2 + k2) * 16 + 4 * g + e;
        const f4 pp = par[n];
        const f2 pc = parc[n];
        f2 t01 = pos01 * pp[1], t23 = pos23 * pp[1];
        f2 r01 = {__builtin_amdgcn_fractf(t01[0]), __builtin_amdgcn_fractf(t01[1])};
        f2 r23 = {__builtin_amdgcn_fractf(t23[0]), __builtin_amdgcn_fractf(t23[1])};
        r01 = __builtin_elementwise_fma(pos01, f2{pp[2], pp[2]}, r01);
        r23 = __builtin_elementwise_fma(pos23, f2{pp[2], pp[2]}, r23);
        f2 a01 = __builtin_elementwise_fma(pos01, f2{pp[0], pp[0]}, eo01);
        f2 a23 = __builtin_elementwise_fma(pos23, f2{pp[0], pp[0]}, eo23);
        float rv[4] = {r01[0], r01[1], r23[0], r23[1]};
        float av[4] = {a01[0], a01[1], a23[0], a23[1]};
#pragma unroll
        for (int qq = 0; qq < 4; ++qq) {
          float ex = __builtin_amdgcn_exp2f(av[qq]);
          float cs = __builtin_amdgcn_cosf(rv[qq]), sn = __builtin_amdgcn_sinf(rv[qq]);
          f2 z;
          if (qq == 0) {
            f2 t = f2{cs, sn} * pc[0];
            t = __builtin_elementwise_fma(f2{-sn, cs}, f2{pc[1], pc[1]}, t);
            z = t * ex;
          } else {
            z = f2{cs, -sn} * ex;
          }
          unsigned hh, ll;
          split_h(z[0], z[1], hh, ll);
          hi[qq][e] = hh; lo[qq][e] = ll;
        }
      }
      const h8 bh = __builtin_bit_cast(h8, hi[0]), bl = __builtin_bit_cast(h8, lo[0]);
      acc0 = __builtin_amdgcn_mfma_f32_16x16x32_f16(__builtin_bit_cast(h8, lo[1]), bh, acc0, 0, 0, 0);
      acc1 = __builtin_amdgcn_mfma_f32_16x16x32_f16(__builtin_bit_cast(h8, lo[2]), bh, acc1, 0, 0, 0);
      acc2 = __builtin_amdgcn_mfma_f32_16x16x32_f16(__builtin_bit_cast(h8, lo[3]), bh, acc2, 0, 0, 0);
      acc0 = __builtin_amdgcn_mfma_f32_16x16x32_f16(__builtin_bit_cast(h8, hi[1]), bl, acc0, 0, 0, 0);
      acc1 = __builtin_amdgcn_mfma_f32_16x16x32_f16(__builtin_bit_cast(h8, hi[2]), bl, acc1, 0, 0, 0);
      acc2 = __builtin_amdgcn_mfma_f32_16x16x32_f16(__builtin_bit_cast(h8, hi[3]), bl, acc2, 0, 0, 0);
      acc0 = __builtin_amdgcn_mfma_f32_16x16x32_f16(__builtin_bit_cast(h8, hi[1]), bh, acc0, 0, 0, 0);
      acc1 = __builtin_amdgcn_mfma_f32_16x16x32_f16(__builtin_bit_cast(h8, hi[2]), bh, acc1, 0, 0, 0);
      acc2 = __builtin_amdgcn_mfma_f32_16x16x32_f16(__builtin_bit_cast(h8, hi[3]), bh, acc2, 0, 0, 0);
    }
    red[(w4 * 3 + 0) * 64 + lane] = acc0;
    red[(w4 * 3 + 1) * 64 + lane] = acc1;
    red[(w4 * 3 + 2) * 64 + lane] = acc2;
  }
  STAMP(3);
  __syncthreads();
  STAMP(4);
  if (w4 < 3) {
    f4 vv = red[(0 * 3 + w4) * 64 + lane] + red[(1 * 3 + w4) * 64 + lane] +
            (red[(2 * 3 + w4) * 64 + lane] + red[(3 * 3 + w4) * 64 + lane]);
#pragma unroll
    for (int r = 0; r < 4; ++r) kv[(16 * w4 + 4 * g + r) * 16 + idx] = vv[r] * (1.0f / 2048.0f);
  } else if (job == 0 && tid < 256 && lane < 32) {
    float cs = 0.f;
#pragma unroll
    for (int i = 0; i < 8; ++i) cs += csr[i * 32 + lane];
    ((float*)(ws + WS_C1))[jb * 32 + lane] = cs;
  }
  if (t8 == 0) ((float*)(ws + WS_KSC))[d] = __uint_as_float((unsigned)(127 + ce - 14) << 23);
  if (b == 255 && tid < 32) ((unsigned*)(ws + WS_CNT))[tid * 32] = 0u;
  __syncthreads();
  u4 hi, lo;
#pragma unroll
  for (int j = 0; j < 4; ++j) {
    if (KTERMS > 1) {
      unsigned hh, ll;
      split_h(kv[kidx[2 * j]], kv[kidx[2 * j + 1]], hh, ll);
      hi[j] = hh; lo[j] = ll;
    } else {
      hi[j] = pack_h(kv[kidx[2 * j]], kv[kidx[2 * j + 1]]);
    }
  }
  st16(ws + WS_KH + ((size_t)d * SL + t8 * 8) * 2, hi);
  if (KTERMS > 1) st16(ws + WS_KL + ((size_t)d * SL + t8 * 8) * 2, lo);
  STAMP(5); STAMP_WAITV STAMP(6); STAMP_OUT(0);
}

constexpr int CV_U = 2064 * 2;
constexpr int CV_K = 2160 * 2;
constexpr int CV_OFF_K = 2 * CV_U;
constexpr int CV_OFF_Y = CV_OFF_K + 2 * KTERMS * CV_K;
constexpr int CV_Q = (SL + 4) * 4;
constexpr int CV_SMEM = CV_OFF_Y + 2 * 4 * CV_Q;

constexpr int CV_RPF = 2 + 2 * KTERMS;
constexpr int CV_R0 = (KTERMS == 2 ? 6 : 10) * CV_RPF;
constexpr int CV_NM = 68 * KTERMS;
struct CvStep { int a, n, k, first, op, arg, arg2; };
template <int SUB>
struct CvSched {
  CvStep s[CV_NM];
  int wt, wc, slack;
  constexpr CvSched() : s{}, wt(-1), wc(4), slack(1000) {
    const int AMAX = SUB == 0 ? 15 : 14;
    const int NR = (AMAX + 1) * CV_RPF;
    int nr = CV_R0, mi = 0;
    int endat[16] = {};
    for (int a = 0; a < 16; ++a) endat[a] = 1 << 20;
    for (int a = 0; a <= AMAX; ++a) {
      if ((((a + 1) >> 1) & 1) != SUB) continue;
      for (int nn = 0; nn <= a; ++nn) {
        const int n = (a == 0) ? 0 : (nn + 1 < a ? nn + 1 : (nn + 1 == a ? 0 : a));
        for (int k = 0; k < KTERMS; ++k) {
          CvStep st{a, n, k, (nn == 0 && k == 0) ? 1 : 0, 0, 0, 0};
          { const int need = ((n > a - n ? n : a - n) + 1) * CV_RPF; if (nr < NR && nr - need < slack) slack = nr - need; }
          ++mi;
          if (wc == 4) {
            for (int t = wt + 1; t < 16; ++t)
              if (endat[t] < (1 << 20)) { if (endat[t] + 8 <= mi) { wt = t; wc = 0; } break; }
          }
          if (wc < 4) { st.op = 2; st.arg = wt; st.arg2 = wc; ++wc; }
          else if (nr < NR) { st.op = 1; st.arg = nr; ++nr; }
          s[mi - 1] = st;
        }
      }
      endat[a] = mi;
    }
  }
};
template <int R>
__device__ __forceinline__ void cv_rd(s4 (&A)[16][KTERMS][2], s4 (&B)[16][2], const char* ub, const char* kb) {
  constexpr int i = R / CV_RPF, part = R % CV_RPF;
  if constexpr (part < 2) B[i][part] = tr_read(ub + 256 * i + 32 * part);
  else A[i][(part - 2) >> 1][part & 1] = tr_read(kb + ((part - 2) >> 1) * CV_K + 256 * i + 32 * (1 - (part & 1)));
}
template <int SUB, int I>
__device__ __forceinline__ void cv_step(s4 (&A)[16][KTERMS][2], s4 (&B)[16][2], f4 (&acc)[16], const char* ub, const char* kb, char* qb) {
  constexpr CvSched<SUB> S{};
  constexpr CvStep st = S.s[I];
  const s8 av = cat8(A[st.a - st.n][st.k][0], A[st.a - st.n][st.k][1]), bv = cat8(B[st.n][0], B[st.n][1]);
  if constexpr (st.first) asm volatile("v_mfma_f32_16x16x32_f16 %0, %1, %2, 0" : "=&v"(acc[st.a]) : "v"(av), "v"(bv));
  else asm volatile("v_mfma_f32_16x16x32_f16 %0, %1, %2, %0" : "+v"(acc[st.a]) : "v"(av), "v"(bv));
  if constexpr (st.op == 2) *(float*)(qb + st.arg2 * (CV_Q + 4) + st.arg * 512) = acc[st.arg][st.arg2];
  else if constexpr (st.op == 1) cv_rd<st.arg>(A, B, ub, kb);
  SCHED;
}
template <int SUB, int... Rs, int... Is>
__device__ __forceinline__ void conv_mfma_impl(const char* ub, const char* kb, char* qb, iseq<Rs...>, iseq<Is...>) {
  s4 A[16][KTERMS][2];
  s4 B[16][2];
  f4 acc[16];
  (cv_rd<Rs>(A, B, ub, kb), ...);
  SCHED;
  (cv_step<SUB, Is>(A, B, acc, ub, kb, qb), ...);
  asm volatile("s_nop 7");
  SCHED;
  constexpr CvSched<SUB> S{};
  static_assert(S.slack >= 4, "fragment read scheduled too late");
#pragma unroll
  for (int t = 0; t < 16; ++t) {
    if ((((t + 1) >> 1) & 1) != SUB) continue;
#pragma unroll
    for (int r = 0; r < 4; ++r)
      if (t > S.wt || (t == S.wt && r >= S.wc)) *(float*)(qb + r * (CV_Q + 4) + t * 512) = acc[t][r];
  }
}
template <int SUB>
__device__ __forceinline__ void conv_mfma(const char* ub, const char* kb, char* qb) {
  conv_mfma_impl<SUB>(ub, kb, qb, make_iseq<CV_R0>{}, make_iseq<CV_NM>{});
}

__global__ __launch_bounds__(512, 2) void conv_kernel(
    const u16* __restrict__ xT, const float* __restrict__ stats, const u16* __restrict__ Kh,
    const u16* __restrict__ Kl, const float* __restrict__ ksc, const u16* __restrict__ dskip,
    u16* __restrict__ outT) {
  extern __shared__ __attribute__((aligned(16))) char smem[];
  const int tid = threadIdx.x, lane = tid & 63;
  const int wid = __builtin_amdgcn_readfirstlane(tid >> 6);
  STAMP_DECL STAMP(0);
  const int d0 = blockIdx.x * 2;
  const int t0 = tid * 4;
  u2 xr[2];
  u4 kr[KTERMS];
  f4 sr[2];
#pragma unroll
  for (int i = 0; i < 2; ++i) sr[i] = *(const f4*)(stats + t0 * 2 + i * 4);
#pragma unroll
  for (int c = 0; c < 2; ++c) xr[c] = *(const u2*)(xT + xoff(d0 + c, t0));
  kr[0] = *(const u4*)(Kh + (size_t)d0 * SL + tid * 8);
  if (KTERMS > 1) kr[KTERMS - 1] = *(const u4*)(Kl + (size_t)d0 * SL + tid * 8);
  const float2 isc2 = *(const float2*)(ksc + d0);
  const unsigned ds2 = *(const unsigned*)(dskip + d0);
  if (tid < 14 * 2 * KTERMS) {
    int which = tid / 14, j = tid % 14;
    *(u4*)(smem + CV_OFF_K + which * CV_K + j * 16) = u4{0, 0, 0, 0};
  } else if (tid >= 64 && tid < 70) {
    int c = (tid - 64) / 3, j = (tid - 64) % 3;
    *(u2*)(smem + c * CV_U + j * 8) = u2{0, 0};
  } else if (tid >= 128 && tid < 136) {
    *(f4*)(smem + CV_OFF_Y + (tid - 128) * CV_Q) = f4{0, 0, 0, 0};
  }
  STAMP_WAITV STAMP(1);
  float mb[4], ri[4];
#pragma unroll
  for (int j = 0; j < 4; ++j) ln_consts(sr[j >> 1][(j & 1) * 2], sr[j >> 1][(j & 1) * 2 + 1], mb[j], ri[j]);
  u2 ub16[2];
#pragma unroll
  for (int c = 0; c < 2; ++c) {
    unsigned d01 = pack_bf(bflo(xr[c][0]) - mb[0], bfhi(xr[c][0]) - mb[1]);
    unsigned d23 = pack_bf(bflo(xr[c][1]) - mb[2], bfhi(xr[c][1]) - mb[3]);
    ub16[c][0] = pack_bf(bflo(d01) * ri[0], bfhi(d01) * ri[1]);
    ub16[c][1] = pack_bf(bflo(d23) * ri[2], bfhi(d23) * ri[3]);
    u2 a = {pack_h(bflo(ub16[c][0]), bfhi(ub16[c][0])), pack_h(bflo(ub16[c][1]), bfhi(ub16[c][1]))};
    *(u2*)(smem + c * CV_U + 24 + tid * 8) = a;
  }
#pragma unroll
  for (int k = 0; k < KTERMS; ++k)
    *(u4*)(smem + CV_OFF_K + ((tid >> 8) * KTERMS + k) * CV_K + 224 + (tid & 255) * 16) = kr[k];
  __syncthreads();
  STAMP(2);
  {
    const int ch = wid >> 2, par = (wid >> 1) & 1;
    const int g = lane >> 4, q = (lane >> 2) & 3, p = lane & 3;
    const char* ub = smem + ch * CV_U + 8 * (p + q + 8 * g);
    const char* kb = smem + CV_OFF_K + ch * KTERMS * CV_K + 8 * (31 + 16 * par + 4 * p - 8 * g - q) - 32;
    char* qb = smem + CV_OFF_Y + ch * 4 * CV_Q + par * 256 + lane * 4;
    if (wid & 1) conv_mfma<1>(ub, kb, qb);
    else conv_mfma<0>(ub, kb, qb);
  }
  STAMP(3);
  __syncthreads();
  STAMP(4);
#pragma unroll
  for (int c = 0; c < 2; ++c) {
    const char* Q = smem + CV_OFF_Y + c * 4 * CV_Q + t0 * 4;
    f4 y0 = (*(const f4*)(Q) + *(const f4*)(Q + CV_Q)) + (*(const f4*)(Q + 2 * CV_Q) + *(const f4*)(Q + 3 * CV_Q));
    float isc = c ? isc2.y : isc2.x;
    float ds = c ? bfhi(ds2) : bflo(ds2);
    unsigned y01 = pack_bf(y0[0] * isc, y0[1] * isc), y23 = pack_bf(y0[2] * isc, y0[3] * isc);
    unsigned s01 = pack_bf(bflo(ub16[c][0]) * ds, bfhi(ub16[c][0]) * ds), s23 = pack_bf(bflo(ub16[c][1]) * ds, bfhi(ub16[c][1]) * ds);
    unsigned a01 = pack_bf(bflo(y01) + bflo(s01), bfhi(y01) + bfhi(s01)), a23 = pack_bf(bflo(y23) + bflo(s23), bfhi(y23) + bfhi(s23));
    u2 ov = {pack_bf(bflo(xr[c][0]) + bflo(a01), bfhi(xr[c][0]) + bfhi(a01)), pack_bf(bflo(xr[c][1]) + bflo(a23), bfhi(xr[c][1]) + bfhi(a23))};
    st8(outT + xoff(d0 + c, t0), ov);
  }
  STAMP(5); STAMP_WAITV STAMP(6); STAMP_OUT(1);
}

constexpr int G1_SMEM = 512 * 256 + 512 * 8;

__device__ __forceinline__ void glds16(const void* gsrc, unsigned lds_dst) {
  unsigned keep;
  asm volatile("s_mov_b32 %0, m0\n\ts_mov_b32 m0, %2\n\ts_nop 0\n\tglobal_load_lds_dwordx4 %1, off\n\ts_mov_b32 m0, %0"
               : "=&s"(keep) : "v"(gsrc), "s"(lds_dst) : "memory");
}
template <int OFF>
__device__ __forceinline__ void gload16(u4& dst, const void* p) {
  asm volatile("global_load_dwordx4 %0, %1, off offset:%2" : "=v"(dst) : "v"(p), "i"(OFF) : "memory");
}
template <int N>
__device__ __forceinline__ void wait_w(u4& a, u4& b, u4& c, u4& d) {
  asm volatile("s_waitcnt vmcnt(%4)\n\ts_barrier" : "+v"(a), "+v"(b), "+v"(c), "+v"(d) : "i"(N) : "memory");
}

template <int N>
__device__ __forceinline__ void wait_only(u4& a) {
  asm volatile("s_waitcnt vmcnt(%1)" : "+v"(a) : "i"(N) : "memory");
}
template <int N>
__device__ __forceinline__ void wait_vm() { asm volatile("s_waitcnt vmcnt(%0)" :: "i"(N) : "memory"); }
__device__ __forceinline__ void add1(float& a, float b) { asm("v_add_f32 %0, %0, %1" : "+v"(a) : "v"(b)); }
__device__ __forceinline__ void sqa1(float& a, float b) { asm("v_fmac_f32 %0, %1, %1" : "+v"(a) : "v"(b)); }
__device__ __forceinline__ void sth(void* p, u4 v) {
  st16(p, v);
}
__device__ __forceinline__ void bar() { asm volatile("s_waitcnt lgkmcnt(0)\n\ts_barrier" ::: "memory"); }
constexpr int G1_ST = 4 * 32768;
constexpr int G1_ARR = G1_ST + 2048;
#define X_EPI 1
#define F_BPRE 5
#define G1_XPF 3
#define G1_WPF 8

template <int OFF>
__device__ __forceinline__ void gload16_sc1(u4& dst, const void* p) {
  asm volatile("global_load_dwordx4 %0, %1, off offset:%2 sc1" : "=v"(dst) : "v"(p), "i"(OFF) : "memory");
}
template <int N>
__device__ __forceinline__ void wait4(u4 (&a)[4]) {
  asm volatile("s_waitcnt vmcnt(%4)" : "+v"(a[0]), "+v"(a[1]), "+v"(a[2]), "+v"(a[3]) : "i"(N) : "memory");
}

template <bool FINAL>
__global__ __launch_bounds__(512, 2) void ffn_kernel(
    const u16* __restrict__ xT, const u16* __restrict__ W1F, const float* __restrict__ c1,
    u16* HF, float* pst, const u16* __restrict__ W2F, unsigned* cnt,
    u16* __restrict__ out, float* __restrict__ stats) {
  extern __shared__ __attribute__((aligned(16))) char smem[];
  const int tid = threadIdx.x, lane = tid & 63;
  const int wid = __builtin_amdgcn_readfirstlane(tid >> 6);
  STAMP_DECL STAMP(0);
  const int bx = blockIdx.x, xj = bx & 7, r = bx >> 3;
  const int tp = 4 * (xj >> 1) + (r & 3);
  const int fp = 7 * (xj & 1) + (r >> 2);
  if (r < 28) {
  const int h = lane >> 5, blk = (lane >> 4) & 1, q = (lane >> 2) & 3, p = lane & 3;
  const char* rbase = smem + (8 * h + q) * 64 + blk * 32 + 8 * p;
  float* st = (float*)(smem + G1_ST);
  if (wid >= 4) {
    const int hq = wid - 4;
    if (tid == 256) *(unsigned*)(smem + G1_ARR) = 0u;
    const char* xsrc = (const char*)xT + (size_t)tp * 131072 + hq * 1024 + lane * 16;
    const unsigned ldst = (unsigned)(size_t)smem + hq * 1024;
    auto issue_c = [&](int c) AINL {
#pragma unroll
      for (int i = 0; i < 3; ++i) glds16(xsrc + c * 4096 + i * 32768, ldst + c * 4096 + i * 32768);
    };
#pragma unroll
    for (int c = 0; c < G1_XPF; ++c) issue_c(c);
#pragma unroll 1
    for (int c = 0; c < 8 - G1_XPF; ++c) {
      wait_vm<3 * (G1_XPF - 1)>(); bar();
      issue_c(c + G1_XPF);
    }
    sfor<G1_XPF>([&](auto I) AINL {
      constexpr int i = decltype(I)::value;
      if constexpr (i == 0) wait_vm<3 * (G1_XPF - 1)>(); else wait_vm<3 * (G1_XPF - 1 - i) + 8>();
      bar();
      if constexpr (i == 0) {
#pragma unroll
        for (int k = 0; k < 8; ++k) glds16(xsrc + 3 * 32768 + k * 4096, ldst + 3 * 32768 + k * 4096);
      }
    });
    wait_vm<0>();
    bar();
#pragma unroll 1
    for (int it = 0; it < 2; ++it) {
      const int u = it == 0 ? 12 + hq : (fp - 1) * 4 + hq;
      if (it == 1 && (fp < 1 || fp > 3)) break;
      const char* rq = rbase + u * 8192;
      s4 fr[8][2];
      sfor<8>([&](auto I) AINL {
        constexpr int i = decltype(I)::value;
        fr[i][0] = tr_read(rq + i * 1024);
        fr[i][1] = tr_read(rq + i * 1024 + 256);
      });
      float sa = 0.f, sb = 0.f, qa = 0.f, qb = 0.f;
      sfor<8>([&](auto I) AINL {
        constexpr int i = decltype(I)::value;
        u4 xv = __builtin_bit_cast(u4, cat8(fr[i][0], fr[i][1]));
#pragma unroll
        for (int j = 0; j < 4; ++j) {
          float lo = bflo(xv[j]), hi = bfhi(xv[j]);
          add1(sa, lo); add1(sb, hi); sqa1(qa, lo); sqa1(qb, hi);
        }
      });
      float s0 = sa + sb, ss0 = qa + qb;
      s0 += __shfl_xor(s0, 32);
      ss0 += __shfl_xor(ss0, 32);
      if (it == 0) {
        if (lane < 32) st[128 + hq * 32 + lane] = s0;
        bar();
      }
      if (lane < 32 && (it == 1 || fp == 0))
        st8(pst + ((tp * 128 + (u >> 2) * 32 + lane) * 4 + (u & 3)) * 2, u2{__float_as_uint(s0), __float_as_uint(ss0)});
    }
  } else {
  const int ft = fp * 4 + wid;
  u4 wf[32];
  u4 cfu[4];
  const char* wsrc = (const char*)W1F + ((size_t)ft * 32 * 64 + lane) * 16;
  auto issue_w = [&](auto S) AINL {
    constexpr int s = decltype(S)::value;
    gload16<(s & 3) * 1024>(wf[s], wsrc + (s >> 2) * 4096);
  };
  sfor<G1_WPF>(issue_w);
  f16v acc[4], asum;
  s4 xa[3][3][2];
  constexpr int XD = 4, XR = XD + 1;
  s4 xc[XR][2];
  u4 ov;
  float nm[4];
  const int w3 = wid == 3 ? 0 : wid;
  int tto[3];
#pragma unroll
  for (int i = 0; i < 3; ++i) { int tt = i + w3; tto[i] = tt >= 3 ? tt - 3 : tt; }
  const char* rb[3];
#pragma unroll
  for (int i = 0; i < 3; ++i) rb[i] = rbase + tto[i] * 32768;
  u16* dst = HF + ((size_t)((tp * 8 + blk) * 56 + ft) * 64 + (lane & 15) + 16 * h) * 8;
  auto epi_pair = [&](const f16v& a, float m, int e) AINL -> unsigned {
    f4 cf = __builtin_bit_cast(f4, cfu[e >> 2]);
    unsigned pu = pack_bf(fmaf(cf[e & 3], m, a[e]), fmaf(cf[(e & 3) + 1], m, a[e + 1]));
    asm("" : "+v"(pu));
    return __builtin_bit_cast(unsigned, __builtin_elementwise_max(__builtin_bit_cast(s2v, pu), s2v{0, 0}));
  };
  f16v z;
#pragma unroll
  for (int j = 0; j < 16; ++j) z[j] = 0.f;
  const s8 ones8 = {0x3f80, 0x3f80, 0x3f80, 0x3f80, 0x3f80, 0x3f80, 0x3f80, 0x3f80};
  SCHED;
  asm volatile("s_barrier" ::: "memory");
#pragma unroll
  for (int k = 0; k < 2; ++k)
#pragma unroll
    for (int i = 0; i < 3; ++i) { xa[k][i][0] = tr_read(rb[i] + k * 1024); xa[k][i][1] = tr_read(rb[i] + k * 1024 + 256); }
  SCHED;
  sfor<32>([&](auto S) AINL {
    constexpr int s = decltype(S)::value;
    constexpr int issued = (G1_WPF + s <= 32) ? G1_WPF + s : 36;
    asum = __builtin_amdgcn_mfma_f32_32x32x16_bf16(__builtin_bit_cast(b8, ones8), __builtin_bit_cast(b8, cat8(xa[s % 3][0][0], xa[s % 3][0][1])), s == 0 ? z : asum, 0, 0, 0);
    wait_only<issued - s - 1>(wf[s]);
    if constexpr ((s & 3) == 2 && s < 30) asm volatile("s_barrier" ::: "memory");
    SCHED;
    sfor<3>([&](auto I) AINL {
      constexpr int i = decltype(I)::value;
      const b8 xv = __builtin_bit_cast(b8, cat8(xa[s % 3][i][0], xa[s % 3][i][1]));
      acc[i] = __builtin_amdgcn_mfma_f32_32x32x16_bf16(__builtin_bit_cast(b8, wf[s]), xv, s == 0 ? z : acc[i], 0, 0, 0);
      if constexpr (s + 2 < 32) {
        xa[(s + 2) % 3][i][0] = tr_read(rb[i] + (s + 2) * 1024);
        xa[(s + 2) % 3][i][1] = tr_read(rb[i] + (s + 2) * 1024 + 256);
      }
      if constexpr (i == 0) {
        if constexpr (G1_WPF + s < 32) issue_w(ic<(G1_WPF + s < 32) ? G1_WPF + s : 0>{});
        if constexpr (G1_WPF + s == 32) {
          gload16<0>(cfu[0], c1 + ft * 32 + 4 * h);
          gload16<32>(cfu[1], c1 + ft * 32 + 4 * h);
          gload16<64>(cfu[2], c1 + ft * 32 + 4 * h);
          gload16<96>(cfu[3], c1 + ft * 32 + 4 * h);
        }
      }
      SCHED;
    });
  });
  STAMP(2);
  if (lane < 32) st[wid * 32 + lane] = -rbf(asum[0] * (1.0f / DM));
  asm volatile("s_waitcnt vmcnt(0) lgkmcnt(0)\n\ts_barrier" : "+v"(cfu[0]), "+v"(cfu[1]), "+v"(cfu[2]), "+v"(cfu[3]) :: "memory");
  {
    unsigned rbo = (unsigned)(rbase - smem) + 3 * 32768;
    asm volatile("" : "+v"(rbo));
    const char* rbb = smem + rbo;
    sfor<XD>([&](auto I) AINL {
      constexpr int i = decltype(I)::value;
      xc[i][0] = tr_read(rbb + i * 1024); xc[i][1] = tr_read(rbb + i * 1024 + 256);
    });
#pragma unroll
    for (int i = 0; i < 3; ++i) nm[i] = st[tto[i] * 32 + (lane & 31)];
    SCHED;
    sfor<32>([&](auto S) AINL {
      constexpr int s = decltype(S)::value;
      const b8 xv = __builtin_bit_cast(b8, cat8(xc[s % XR][0], xc[s % XR][1]));
      acc[3] = __builtin_amdgcn_mfma_f32_32x32x16_bf16(__builtin_bit_cast(b8, wf[s]), xv, s == 0 ? z : acc[3], 0, 0, 0);
      if constexpr (s + XD < 32) {
        xc[(s + XD) % XR][0] = tr_read(rbb + (s + XD) * 1024);
        xc[(s + XD) % XR][1] = tr_read(rbb + (s + XD) * 1024 + 256);
      }
      if constexpr (s < 24) {
        constexpr int i = s >> 3, pr = s & 7;
        ov[pr & 3] = epi_pair(acc[i], nm[i], 2 * pr);
        if constexpr ((pr & 3) == 3) sth(dst + tto[i] * (2 * 56 * 512) + (pr >> 2) * 256, ov);
      }
      SCHED;
    });
  }
  asm volatile("s_barrier" ::: "memory");
  {
    const float* sp = st + 128 + (lane & 31);
    nm[3] = -rbf(((sp[0] + sp[32]) + (sp[64] + sp[96])) * (1.0f / DM));
  }
#pragma unroll
  for (int c = 0; c < 2; ++c) {
#pragma unroll
    for (int j = 0; j < 4; ++j) ov[j] = epi_pair(acc[3], nm[3], 8 * c + 2 * j);
    sth(dst + 3 * (2 * 56 * 512) + c * 256, ov);
  }
  }
  }
  STAMP(3);
  const int tb = 8 * (xj >> 1) + (r & 7), db = 4 * (xj & 1) + (r >> 3);
  const int g = lane >> 4, n = lane & 15;
  const int mo = wid >> 1, no = 2 * (wid & 1);
  u4 bf[7][4], af[7][4];
  {
    const char* bp[4];
#pragma unroll
    for (int j = 0; j < 4; ++j) bp[j] = (const char*)W2F + ((size_t)((db * 4 + ((j + no) & 3)) * 56 + wid * 7) * 64 + lane) * 16;
    auto issue_b = [&](auto KS) AINL {
      constexpr int ks = decltype(KS)::value;
      sfor<4>([&](auto J) AINL {
        constexpr int j = decltype(J)::value;
        gload16<(ks & 3) * 1024>(bf[ks][j], bp[j] + (ks >> 2) * 4096);
      });
    };
    wait_vm<0>();
    bar();
    STAMP(1);
    if (r < 28 && tid == 0) __hip_atomic_fetch_add(cnt + tp * 32, 1u, __ATOMIC_RELAXED, __HIP_MEMORY_SCOPE_AGENT);
    if (wid == 7) {
      const unsigned* cp = cnt + (tb >> 1) * 32;
      for (int spin = 0; spin < (1 << 22); ++spin) {
        if (__hip_atomic_load(cp, __ATOMIC_RELAXED, __HIP_MEMORY_SCOPE_AGENT) >= 14u) break;
        __builtin_amdgcn_s_sleep(2);
      }
    }
    sfor<F_BPRE>(issue_b);
    bar();
    STAMP(4);
    sfor<7 - F_BPRE>([&](auto I) AINL { issue_b(ic<F_BPRE + decltype(I)::value>{}); });
  }
  u2 res[2];
  u16 resf[2][4];
  if (!FINAL) {
#pragma unroll
    for (int j = 0; j < 2; ++j) res[j] = *(const u2*)(xT + xoff(db * 64 + (no + j) * 16 + n, tb * 64 + mo * 16 + 4 * g));
  } else {
#pragma unroll
    for (int j = 0; j < 2; ++j)
#pragma unroll
      for (int e = 0; e < 4; ++e) resf[j][e] = xT[xoff(db * 64 + (no + j) * 16 + 4 * g + e, tb * 64 + mo * 16 + n)];
  }
  const unsigned long long psu = __hip_atomic_load((const unsigned long long*)(pst + ((tb * 64 + mo * 16 + n) * 4 + g) * 2), __ATOMIC_RELAXED, __HIP_MEMORY_SCOPE_AGENT);
  const char* ap[4];
#pragma unroll
  for (int i = 0; i < 4; ++i) ap[i] = (const char*)HF + ((size_t)((tb * 4 + ((i + mo) & 3)) * 56 + wid * 7) * 64 + lane) * 16;
  auto issue_a = [&](auto KS) AINL {
    constexpr int ks = decltype(KS)::value;
    sfor<4>([&](auto I) AINL {
      constexpr int i = decltype(I)::value;
      gload16_sc1<(ks & 3) * 1024>(af[ks][i], ap[i] + (ks >> 2) * 4096);
    });
  };
  sfor<4>(issue_a);
  f4 acc[4][4];
#pragma unroll
  for (int i = 0; i < 4; ++i)
#pragma unroll
    for (int j = 0; j < 4; ++j) acc[i][j] = f4{0, 0, 0, 0};
  SCHED;
  sfor<7>([&](auto KS) AINL {
    constexpr int ks = decltype(KS)::value;
    wait4<(ks < 4 ? 12 : 4 * (6 - ks))>(af[ks]);
    wait4<63>(bf[ks]);
    SCHED;
#pragma unroll
    for (int i = 0; i < 4; ++i)
#pragma unroll
      for (int j = 0; j < 4; ++j) {
        if (!FINAL) acc[i][j] = __builtin_amdgcn_mfma_f32_16x16x32_bf16(__builtin_bit_cast(b8, af[ks][i]), __builtin_bit_cast(b8, bf[ks][j]), acc[i][j], 0, 0, 0);
        else acc[i][j] = __builtin_amdgcn_mfma_f32_16x16x32_bf16(__builtin_bit_cast(b8, bf[ks][j]), __builtin_bit_cast(b8, af[ks][i]), acc[i][j], 0, 0, 0);
      }
    SCHED;
    if constexpr (ks + 4 < 7) issue_a(ic<(ks + 4 < 7) ? ks + 4 : 0>{});
    SCHED;
  });
  STAMP(5);
  f4* red = (f4*)smem;
#pragma unroll
  for (int i = 0; i < 4; ++i)
#pragma unroll
    for (int jp = 0; jp < 2; ++jp) {
      if (i == 0 && jp == 0) continue;
      const int owner = ((i + mo) & 3) * 2 + ((jp + wid) & 1);
      red[((owner * 7 + i * 2 + jp - 1) * 2 + 0) * 64 + lane] = acc[i][2 * jp];
      red[((owner * 7 + i * 2 + jp - 1) * 2 + 1) * 64 + lane] = acc[i][2 * jp + 1];
    }
  f4 rr;
  {
    float sx = __uint_as_float((unsigned)psu), sy = __uint_as_float((unsigned)(psu >> 32));
    sx += __shfl_xor(sx, 16); sy += __shfl_xor(sy, 16);
    sx += __shfl_xor(sx, 32); sy += __shfl_xor(sy, 32);
    float mb, ri;
    ln_consts(sx, sy, mb, ri);
    if (FINAL) rr = f4{ri, ri, ri, ri};
    else {
#pragma unroll
      for (int e = 0; e < 4; ++e) rr[e] = __shfl(ri, 4 * g + e);
    }
  }
  bar();
  STAMP(6);
  float s[4] = {0, 0, 0, 0}, ss[4] = {0, 0, 0, 0};
#pragma unroll
  for (int j = 0; j < 2; ++j) {
    const f4* rp = red + (wid * 14 + j) * 64 + lane;
    f4 v = ((acc[0][j] + rp[0]) + (rp[128] + rp[256])) + ((rp[384] + rp[512]) + (rp[640] + rp[768]));
    v *= rr;
    unsigned m01 = pack_bf(v[0], v[1]), m23 = pack_bf(v[2], v[3]);
    u2 ov;
    if (!FINAL) {
      ov[0] = pack_bf(bflo(res[j][0]) + bflo(m01), bfhi(res[j][0]) + bfhi(m01));
      ov[1] = pack_bf(bflo(res[j][1]) + bflo(m23), bfhi(res[j][1]) + bfhi(m23));
      *(u2*)(out + xoff(db * 64 + (no + j) * 16 + n, tb * 64 + mo * 16 + 4 * g)) = ov;
      float o[4] = {bflo(ov[0]), bfhi(ov[0]), bflo(ov[1]), bfhi(ov[1])};
#pragma unroll
      for (int e = 0; e < 4; ++e) { s[e] += o[e]; ss[e] = fmaf(o[e], o[e], ss[e]); }
    } else {
      ov[0] = pack_bf(bf2f(resf[j][0]) + bflo(m01), bf2f(resf[j][1]) + bfhi(m01));
      ov[1] = pack_bf(bf2f(resf[j][2]) + bflo(m23), bf2f(resf[j][3]) + bfhi(m23));
      *(u2*)(out + (size_t)(tb * 64 + mo * 16 + n) * DM + db * 64 + (no + j) * 16 + 4 * g) = ov;
    }
  }
  if (!FINAL) {
#pragma unroll
    for (int e = 0; e < 4; ++e) { s[e] = row_sum16(s[e]); ss[e] = row_sum16(ss[e]); }
    if (n < 8) {
      int e = n >> 1;
      float v = (n & 1) ? (e == 0 ? ss[0] : e == 1 ? ss[1] : e == 2 ? ss[2] : ss[3]) : (e == 0 ? s[0] : e == 1 ? s[1] : e == 2 ? s[2] : s[3]);
      atomicAdd(stats + (tb * 64 + mo * 16 + 4 * g) * 2 + n, v);
    }
  }
  STAMP_WAITV STAMP(7); STAMP_OUT(FINAL ? 3 : 2);
}

extern "C" void kernel_launch(void* const* d_in, const int* in_sizes, int n_in,
                              void* d_out, int out_size, void* d_ws, size_t ws_size,
                              hipStream_t stream) {
  const u16* x = (const u16*)d_in[0];
  const u16* logA = (const u16*)d_in[1];
  const u16* Aim = (const u16*)d_in[2];
  const u16* Cre = (const u16*)d_in[3];
  const u16* Cim = (const u16*)d_in[4];
  const u16* logdt = (const u16*)d_in[5];
  const u16* dskip = (const u16*)d_in[6];
  const u16* w1 = (const u16*)d_in[8];
  const u16* w2 = (const u16*)d_in[9];
  char* ws = (char*)d_ws;
  u16* out = (u16*)d_out;
  (void)hipFuncSetAttribute((const void*)ffn_kernel<false>, hipFuncAttributeMaxDynamicSharedMemorySize, G1_SMEM);
  (void)hipFuncSetAttribute((const void*)ffn_kernel<true>, hipFuncAttributeMaxDynamicSharedMemorySize, G1_SMEM);
  (void)hipFuncSetAttribute((const void*)conv_kernel, hipFuncAttributeMaxDynamicSharedMemorySize, CV_SMEM);

  prep_kernel<<<256, 512, 0, stream>>>(x, logA, Aim, Cre, Cim, logdt, w1, w2, ws);
  conv_kernel<<<256, 512, CV_SMEM, stream>>>((const u16*)(ws + WS_XT0), (const float*)(ws + WS_ST0), (const u16*)(ws + WS_KH),
                                             (const u16*)(ws + WS_KL), (const float*)(ws + WS_KSC), dskip, (u16*)(ws + WS_XT1));
  ffn_kernel<false><<<256, 512, G1_SMEM, stream>>>((const u16*)(ws + WS_XT1), (const u16*)(ws + WS_W1F), (const float*)(ws + WS_C1),
                                                   (u16*)(ws + WS_HF), (float*)(ws + WS_PST), (const u16*)(ws + WS_W2F),
                                                   (unsigned*)(ws + WS_CNT), (u16*)(ws + WS_XT2), (float*)(ws + WS_ST2));
  conv_kernel<<<256, 512, CV_SMEM, stream>>>((const u16*)(ws + WS_XT2), (const float*)(ws + WS_ST2), (const u16*)(ws + WS_KH),
                                             (const u16*)(ws + WS_KL), (const float*)(ws + WS_KSC), dskip, (u16*)(ws + WS_XT3));
  ffn_kernel<true><<<256, 512, G1_SMEM, stream>>>((const u16*)(ws + WS_XT3), (const u16*)(ws + WS_W1F), (const float*)(ws + WS_C1),
                                                  (u16*)(ws + WS_HF2), (float*)(ws + WS_PST2), (const u16*)(ws + WS_W2F),
                                                  (unsigned*)(ws + WS_CNT) + 512, out, nullptr);
}
```

```cpp
#include <hip/hip_runtime.h>
#include <hip/hip_bf16.h>

typedef unsigned short u16;
typedef short s4 __attribute__((ext_vector_type(4)));
typedef short s2v __attribute__((ext_vector_type(2)));
typedef short s8 __attribute__((ext_vector_type(8)));
typedef _Float16 h8 __attribute__((ext_vector_type(8)));
typedef _Float16 h2 __attribute__((ext_vector_type(2)));
typedef __bf16 b2 __attribute__((ext_vector_type(2)));
typedef __bf16 b8 __attribute__((ext_vector_type(8)));
typedef float f4 __attribute__((ext_vector_type(4)));
typedef float f2 __attribute__((ext_vector_type(2)));
typedef __bf16 b4 __attribute__((ext_vector_type(4)));
typedef float f16v __attribute__((ext_vector_type(16)));
typedef unsigned u4 __attribute__((ext_vector_type(4)));
typedef unsigned u2 __attribute__((ext_vector_type(2)));
typedef __attribute__((address_space(3))) s4* lds_s4p;

#define STAMP_DECL
#define STAMP(i)
#define STAMP_WAITV
#define STAMP_OUT(k)
#define SCHED __builtin_amdgcn_sched_barrier(0)
template <int... Is> struct iseq {};
template <int N, int... Is> struct mk_iseq : mk_iseq<N - 1, N - 1, Is...> {};
template <int... Is> struct mk_iseq<0, Is...> { typedef iseq<Is...> type; };
template <int N> using make_iseq = typename mk_iseq<N>::type;
template <int I> struct ic { static constexpr int value = I; };
template <class F, int... Is> __device__ __forceinline__ void sfor_impl(F&& f, iseq<Is...>) { (f(ic<Is>{}), ...); }
template <int N, class F> __device__ __forceinline__ void sfor(F&& f) { sfor_impl(f, make_iseq<N>{}); }
#define AINL __attribute__((always_inline))

constexpr int SL = 2048;
constexpr int DM = 512;
constexpr int NS = 128;
constexpr int FF = 1792;
#define KTERMS 1

constexpr size_t WS_XT0 = 0;
constexpr size_t WS_XT1 = WS_XT0 + (size_t)SL * DM * 2;
constexpr size_t WS_XT2 = WS_XT1 + (size_t)SL * DM * 2;
constexpr size_t WS_XT3 = WS_XT2 + (size_t)SL * DM * 2;
constexpr size_t WS_KH  = WS_XT3 + (size_t)SL * DM * 2;
constexpr size_t WS_KL  = WS_KH + (size_t)SL * DM * 2;
constexpr size_t WS_W1F = WS_KL + (size_t)SL * DM * 2;
constexpr size_t WS_W2F = WS_W1F + (size_t)DM * FF * 2;
constexpr size_t WS_HF  = WS_W2F + (size_t)DM * FF * 2;
constexpr size_t WS_HF2 = WS_HF + (size_t)SL * FF * 2;
constexpr size_t WS_ST0 = WS_HF2 + (size_t)SL * FF * 2;
constexpr size_t WS_ST2 = WS_ST0 + (size_t)SL * 8;
constexpr size_t WS_C1  = WS_ST2 + (size_t)SL * 8;
constexpr size_t WS_KSC = WS_C1 + (size_t)FF * 4;
constexpr size_t WS_PST = WS_KSC + (size_t)DM * 4;
constexpr size_t WS_PST2 = WS_PST + (size_t)SL * 32;
constexpr size_t WS_CNT = WS_PST2 + (size_t)SL * 32;

__host__ __device__ __forceinline__ int xoff(int d, int t) { return (t >> 5) * 16384 + d * 32 + (t & 31); }
__device__ __forceinline__ float bf2f(unsigned h) { return __uint_as_float(h << 16); }
__device__ __forceinline__ float bflo(unsigned w) { return __uint_as_float(w << 16); }
__device__ __forceinline__ float bfhi(unsigned w) { return __uint_as_float(w & 0xffff0000u); }
__device__ __forceinline__ unsigned pack_bf(float a, float b) {
  b2 v = {(__bf16)a, (__bf16)b};
  return __builtin_bit_cast(unsigned, v);
}
__device__ __forceinline__ float rbf(float f) {
  return __uint_as_float(((unsigned)__builtin_bit_cast(u16, (__bf16)f)) << 16);
}
__device__ __forceinline__ unsigned pack_h(float a, float b) {
  h2 v = {(_Float16)a, (_Float16)b};
  return __builtin_bit_cast(unsigned, v);
}
__device__ __forceinline__ float dot2(unsigned a, unsigned b, float c) {
  return __builtin_amdgcn_fdot2_f32_bf16(__builtin_bit_cast(b2, a), __builtin_bit_cast(b2, b), c, false);
}
__device__ __forceinline__ s4 tr_read(const char* p) {
  return __builtin_amdgcn_ds_read_tr16_b64_v4i16((lds_s4p)p);
}
__device__ __forceinline__ s8 cat8(s4 a, s4 b) { return __builtin_shufflevector(a, b, 0, 1, 2, 3, 4, 5, 6, 7); }
template <int CTRL>
__device__ __forceinline__ float dppf(float v) {
  return __builtin_bit_cast(float, __builtin_amdgcn_update_dpp(0, __builtin_bit_cast(int, v), CTRL, 0xf, 0xf, true));
}
__device__ __forceinline__ float shr1(float v) { return dppf<0x138>(v); }
__device__ __forceinline__ float row_sum16(float v) {
  v += dppf<0x128>(v);
  v += dppf<0x124>(v);
  v += dppf<0x122>(v);
  v += dppf<0x121>(v);
  return v;
}
constexpr unsigned BF_ONES = 0x3f803f80u;
__device__ __forceinline__ void st16(void* p, u4 v) { asm volatile("global_store_dwordx4 %0, %1, off sc1\n\ts_nop 1" :: "v"(p), "v"(v) : "memory"); }
__device__ __forceinline__ void st8(void* p, u2 v) { asm volatile("global_store_dwordx2 %0, %1, off sc1\n\ts_nop 0" :: "v"(p), "v"(v) : "memory"); }

__device__ __forceinline__ void ln_consts(float s, float ss, float& mb, float& rinv) {
  float mean = s * (1.0f / DM);
  float var = fmaxf(ss * (1.0f / DM) - mean * mean, 0.0f);
  mb = rbf(mean);
  float sd = rbf(__builtin_amdgcn_sqrtf(rbf(rbf(var) + 1e-5f)));
  rinv = __builtin_amdgcn_rcpf(sd);
}

constexpr int PREP_W1 = 56, PREP_W2 = 56, PREP_TR = 64;
constexpr int PK_PAR = 0, PK_PARC = 2048, PK_RED = 3072, PK_KV = PK_RED + 12288, PK_MX = PK_KV + 3072, PK_SZ = PK_MX + 128;
constexpr int PREP_SMEM = (32768 + 1024 > 2 * PK_SZ) ? 32768 + 1024 : 2 * PK_SZ;

__device__ __forceinline__ int alpha_of(int idx) {
  if (idx < 16) return (idx >> 3) * 128 + (idx & 7);
  if (idx < 24) { int j = idx - 16; return (2 + (j >> 2)) * 128 + (j & 3) * 2; }
  if (idx < 32) { int j = idx - 24; return (4 + (j >> 1)) * 128 + (j & 1) * 4; }
  if (idx < 41) return (8 + idx - 32) * 128;
  return 0;
}
__device__ __forceinline__ float exp_acc(float x) {
  const float L2EH = 1.44269504f, L2EL = (float)(1.4426950408889634 - (double)1.44269504f);
  float t = x * L2EH;
  float e = fmaf(x, L2EH, -t) + x * L2EL;
  float r = __builtin_amdgcn_exp2f(t);
  return fmaf(r, e * 0.69314718f, r);
}
__device__ __forceinline__ float row_max16(float v) {
  v = fmaxf(v, dppf<0x128>(v));
  v = fmaxf(v, dppf<0x124>(v));
  v = fmaxf(v, dppf<0x122>(v));
  v = fmaxf(v, dppf<0x121>(v));
  return v;
}
__device__ __forceinline__ void split_h(float a, float b, unsigned& hi, unsigned& lo) {
  float ah = __uint_as_float(__float_as_uint(a) & 0xffffe000u), bh = __uint_as_float(__float_as_uint(b) & 0xffffe000u);
  hi = pack_h(ah, bh);
  lo = pack_h(a - ah, b - bh);
}

__global__ __launch_bounds__(512, 4) void prep_kernel(
    const u16* __restrict__ x, const u16* __restrict__ logA, const u16* __restrict__ Aim,
    const u16* __restrict__ Cre, const u16* __restrict__ Cim, const u16* __restrict__ logdt,
    const u16* __restrict__ w1, const u16* __restrict__ w2, char* __restrict__ ws) {
  __shared__ __attribute__((aligned(16))) char smem[PREP_SMEM];
  const int tid = threadIdx.x, lane = tid & 63;
  const int wid = __builtin_amdgcn_readfirstlane(tid >> 6);
  STAMP_DECL STAMP(0);
  const int b = blockIdx.x;
  const int idx = lane & 15, g = lane >> 4;
  if (b >= 256) {
    const int bj = b - 256;
    const int job = bj < PREP_W1 ? 0 : (bj < PREP_W1 + PREP_W2 ? 1 : 2);
    const int jb = job == 0 ? bj : (job == 1 ? bj - PREP_W1 : bj - PREP_W1 - PREP_W2);
    u4 v[4];
    int lofs;
    int ls1, ls2;
    {
      const u16* src;
      int gs1, gs2;
      if (job == 0) {
        src = w1 + (size_t)(tid >> 2) * FF + jb * 32 + (tid & 3) * 8;
        gs1 = 128 * FF; gs2 = 256 * FF; lofs = tid * 16; ls1 = 8192; ls2 = 16384;
      } else if (job == 1) {
        src = w2 + (size_t)((jb >> 2) * 128 + (tid >> 4)) * DM + (jb & 3) * 128 + (tid & 15) * 8;
        gs1 = 32 * DM; gs2 = 64 * DM; lofs = (tid >> 4) * 256 + (((tid & 15) ^ (((tid >> 4) & 7) << 1)) * 16); ls1 = 8192; ls2 = 16384;
      } else {
        const int r0 = (wid >> 2) * 8 + (wid & 3);
        src = x + (size_t)(jb * 32 + r0) * DM + lane * 8;
        gs1 = 4 * DM; gs2 = 16 * DM; lofs = r0 * 1024 + ((lane ^ (wid << 1)) * 16); ls1 = 4096; ls2 = 16384;
      }
      v[0] = *(const u4*)(src); v[1] = *(const u4*)(src + gs1); v[2] = *(const u4*)(src + gs2); v[3] = *(const u4*)(src + gs1 + gs2);
    }
    *(u4*)(smem + lofs) = v[0];
    *(u4*)(smem + lofs + ls1) = v[1];
    *(u4*)(smem + lofs + ls2) = v[2];
    *(u4*)(smem + lofs + ls1 + ls2) = v[3];
    __syncthreads();
    const int q = (lane >> 2) & 3, p = lane & 3;
    float* csr = (float*)(smem + 32768);
    if (job == 0) {
      const int h = lane >> 5, blk = (lane >> 4) & 1;
      const char* rb = smem + (wid * 64 + 8 * h + q) * 64 + blk * 32 + p * 8;
      u16* W1F = (u16*)(ws + WS_W1F);
      float cs = 0.f;
#pragma unroll
      for (int i = 0; i < 4; ++i) {
        s8 o = cat8(tr_read(rb + i * 1024), tr_read(rb + i * 1024 + 256));
        u4 ou = __builtin_bit_cast(u4, o);
#pragma unroll
        for (int j = 0; j < 4; ++j) cs = dot2(ou[j], BF_ONES, cs);
        st16(W1F + ((size_t)(jb * 32 + wid * 4 + i) * 64 + lane) * 8, ou);
      }
      cs += __shfl_xor(cs, 32);
      if (lane < 32) csr[wid * 32 + lane] = cs;
    } else if (job == 1) {
      const int rl = 16 * (g >> 1) + 4 * (g & 1) + q;
      const int sw = rl & 7;
      u16* W2F = (u16*)(ws + WS_W2F);
#pragma unroll
      for (int i = 0; i < 4; ++i) {
        const int nt = (wid & 1) * 4 + i;
        const char* rb = smem + ((wid >> 1) * 32 + rl) * 256 + ((nt ^ sw) * 32) + p * 8;
        s8 o = cat8(tr_read(rb), tr_read(rb + 2048));
        st16(W2F + ((size_t)(((jb & 3) * 8 + nt) * 56 + (jb >> 2) * 4 + (wid >> 1)) * 64 + lane) * 8, __builtin_bit_cast(u4, o));
      }
    } else if (job == 2) {
      const int sw = q | ((g & 1) << 2);
      u16* xT = (u16*)(ws + WS_XT0);
#pragma unroll
      for (int i = 0; i < 4; ++i) {
        int dt_ = wid * 4 + i;
        const char* base = smem + (8 * g + q) * 1024 + ((dt_ ^ sw) * 32) + 8 * p;
        s8 o = cat8(tr_read(base), tr_read(base + 4096));
        *(s8*)(xT + xoff(dt_ * 16 + idx, jb * 32 + 8 * g)) = o;
      }
      float sv[4], ssv[4];
#pragma unroll
      for (int i = 0; i < 4; ++i) {
        float s = 0.f, ss = 0.f;
#pragma unroll
        for (int j = 0; j < 4; ++j) { s = dot2(v[i][j], BF_ONES, s); ss = dot2(v[i][j], v[i][j], ss); }
        sv[i] = row_sum16(s); ssv[i] = row_sum16(ss);
      }
      float val = sv[0];
#pragma unroll
      for (int i = 1; i < 4; ++i) val = (idx == 2 * i) ? sv[i] : val;
#pragma unroll
      for (int i = 0; i < 4; ++i) val = (idx == 2 * i + 1) ? ssv[i] : val;
      val += __shfl_xor(val, 16);
      val += __shfl_xor(val, 32);
      if (lane < 8) {
        const int i = lane >> 1;
        const int r = jb * 32 + (i >> 1) * 16 + (wid >> 2) * 8 + (i & 1) * 4 + (wid & 3);
        ((float*)(ws + WS_ST0))[r * 2 + (lane & 1)] = val;
        ((float*)(ws + WS_ST2))[r * 2 + (lane & 1)] = 0.f;
      }
    }

    if (job == 0) {
      __syncthreads();
      if (tid < 32) {
        float cs = 0.f;
#pragma unroll
        for (int i = 0; i < 8; ++i) cs += csr[i * 32 + tid];
        ((float*)(ws + WS_C1))[jb * 32 + tid] = cs;
      }
    }
    if (bj == 0 && tid >= 64 && tid < 96) ((unsigned*)(ws + WS_CNT))[(tid - 64) * 32] = 0u;
    return;
  }
  const int ch = wid >> 2, w4 = wid & 3, t8 = tid & 255;
  const int d = b * 2 + ch;
  const int pn = d * NS + (t8 & (NS - 1));
  const u16 pl0 = logdt[d], pl1 = logA[pn], pl2 = Aim[pn], pl3 = Cre[pn], pl4 = Cim[pn];
  int kidx[8];
#pragma unroll
  for (int j = 0; j < 8; ++j) {
    float pf = rbf((float)(t8 * 8 + j));
    int p = (int)pf;
    int e = (int)(__float_as_uint(pf) >> 23) - (127 + 7);
    e = e < 0 ? 0 : (e > 3 ? 3 : e);
    int ai_ = ((((p >> 7) << 3) + (p & 7)) >> e) + 8 * e;
    kidx[j] = ai_ * 16 + ((p & 127) >> 3);
  }
  char* ks = smem + ch * PK_SZ;
  f4* par = (f4*)(ks + PK_PAR);
  f2* parc = (f2*)(ks + PK_PARC);
  f4* red = (f4*)(ks + PK_RED);
  float* kv = (float*)(ks + PK_KV);
  float* mc = (float*)(ks + PK_MX);
  if (w4 < 2) {
    float dt = rbf(exp_acc(bf2f(pl0)));
    float Ar = -rbf(exp_acc(bf2f(pl1)));
    float ar = Ar * dt;
    float ai = bf2f(pl2) * dt;
    const float I2PH = 0.15915494f;
    const float I2PL = (float)(0.15915494309189535 - (double)0.15915494f);
    float fh = ai * I2PH;
    float fl = fmaf(ai, I2PH, -fh) + ai * I2PL;
    fh -= rintf(fh);
    float fhi = __uint_as_float(__float_as_uint(fh) & 0xfffff000u);
    float flo = (fh - fhi) + fl;
    float crv = 2.0f * bf2f(pl3) * dt;
    float civ = 2.0f * bf2f(pl4) * dt;
    float cm = row_sum16(fabsf(crv) + fabsf(civ));
    par[t8] = f4{ar * 1.44269504f, fhi, flo, 0.f};
    parc[t8] = f2{crv, civ};
    if (idx == 0) mc[t8 >> 4] = cm;
  }
  __syncthreads();
  STAMP(1);
  int ce;
  {
    f4 m0 = *(const f4*)(mc), m1 = *(const f4*)(mc + 4);
    float m = ((m0[0] + m0[1]) + (m0[2] + m0[3])) + ((m1[0] + m1[1]) + (m1[2] + m1[3]));
    ce = (int)((__float_as_uint(m) >> 23) & 0xff) - 127;
    ce = ce < -100 ? -100 : (ce > 100 ? 100 : ce);
  }
  {
    const f2 pos01 = {(float)(8 * idx), (float)alpha_of(idx)};
    const f2 pos23 = {(float)alpha_of(16 + idx), (float)alpha_of(32 + idx)};
    const f2 eo01 = {(float)(12 - ce), 13.f}, eo23 = {13.f, 13.f};
    f4 acc0 = {0, 0, 0, 0}, acc1 = {0, 0, 0, 0}, acc2 = {0, 0, 0, 0};
#pragma unroll
    for (int k2 = 0; k2 < 2; ++k2) {
      u4 hi[4], lo[4];
#pragma unroll
      for (int e = 0; e < 4; ++e) {
        const int n = (w4 * 2 + k2) * 16 + 4 * g + e;
        const f4 pp = par[n];
        const f2 pc = parc[n];
        f2 t01 = pos01 * pp[1], t23 = pos23 * pp[1];
        f2 r01 = {__builtin_amdgcn_fractf(t01[0]), __builtin_amdgcn_fractf(t01[1])};
        f2 r23 = {__builtin_amdgcn_fractf(t23[0]), __builtin_amdgcn_fractf(t23[1])};
        r01 = __builtin_elementwise_fma(pos01, f2{pp[2], pp[2]}, r01);
        r23 = __builtin_elementwise_fma(pos23, f2{pp[2], pp[2]}, r23);
        f2 a01 = __builtin_elementwise_fma(pos01, f2{pp[0], pp[0]}, eo01);
        f2 a23 = __builtin_elementwise_fma(pos23, f2{pp[0], pp[0]}, eo23);
        float rv[4] = {r01[0], r01[1], r23[0], r23[1]};
        float av[4] = {a01[0], a01[1], a23[0], a23[1]};
#pragma unroll
        for (int qq = 0; qq < 4; ++qq) {
          float ex = __builtin_amdgcn_exp2f(av[qq]);
          float cs = __builtin_amdgcn_cosf(rv[qq]), sn = __builtin_amdgcn_sinf(rv[qq]);
          f2 z;
          if (qq == 0) {
            f2 t = f2{cs, sn} * pc[0];
            t = __builtin_elementwise_fma(f2{-sn, cs}, f2{pc[1], pc[1]}, t);
            z = t * ex;
          } else {
            z = f2{cs, -sn} * ex;
          }
          unsigned hh, ll;
          split_h(z[0], z[1], hh, ll);
          hi[qq][e] = hh; lo[qq][e] = ll;
        }
      }
      const h8 bh = __builtin_bit_cast(h8, hi[0]), bl = __builtin_bit_cast(h8, lo[0]);
      acc0 = __builtin_amdgcn_mfma_f32_16x16x32_f16(__builtin_bit_cast(h8, lo[1]), bh, acc0, 0, 0, 0);
      acc1 = __builtin_amdgcn_mfma_f32_16x16x32_f16(__builtin_bit_cast(h8, lo[2]), bh, acc1, 0, 0, 0);
      acc2 = __builtin_amdgcn_mfma_f32_16x16x32_f16(__builtin_bit_cast(h8, lo[3]), bh, acc2, 0, 0, 0);
      acc0 = __builtin_amdgcn_mfma_f32_16x16x32_f16(__builtin_bit_cast(h8, hi[1]), bl, acc0, 0, 0, 0);
      acc1 = __builtin_amdgcn_mfma_f32_16x16x32_f16(__builtin_bit_cast(h8, hi[2]), bl, acc1, 0, 0, 0);
      acc2 = __builtin_amdgcn_mfma_f32_16x16x32_f16(__builtin_bit_cast(h8, hi[3]), bl, acc2, 0, 0, 0);
      acc0 = __builtin_amdgcn_mfma_f32_16x16x32_f16(__builtin_bit_cast(h8, hi[1]), bh, acc0, 0, 0, 0);
      acc1 = __builtin_amdgcn_mfma_f32_16x16x32_f16(__builtin_bit_cast(h8, hi[2]), bh, acc1, 0, 0, 0);
      acc2 = __builtin_amdgcn_mfma_f32_16x16x32_f16(__builtin_bit_cast(h8, hi[3]), bh, acc2, 0, 0, 0);
    }
    red[(w4 * 3 + 0) * 64 + lane] = acc0;
    red[(w4 * 3 + 1) * 64 + lane] = acc1;
    red[(w4 * 3 + 2) * 64 + lane] = acc2;
  }
  STAMP(3);
  __syncthreads();
  STAMP(4);
  if (w4 < 3) {
    f4 vv = red[(0 * 3 + w4) * 64 + lane] + red[(1 * 3 + w4) * 64 + lane] +
            (red[(2 * 3 + w4) * 64 + lane] + red[(3 * 3 + w4) * 64 + lane]);
#pragma unroll
    for (int r = 0; r < 4; ++r) kv[(16 * w4 + 4 * g + r) * 16 + idx] = vv[r] * (1.0f / 2048.0f);
  }
  if (t8 == 0) ((float*)(ws + WS_KSC))[d] = __uint_as_float((unsigned)(127 + ce - 14) << 23);
  __syncthreads();
  u4 hi, lo;
#pragma unroll
  for (int j = 0; j < 4; ++j) {
    if (KTERMS > 1) {
      unsigned hh, ll;
      split_h(kv[kidx[2 * j]], kv[kidx[2 * j + 1]], hh, ll);
      hi[j] = hh; lo[j] = ll;
    } else {
      hi[j] = pack_h(kv[kidx[2 * j]], kv[kidx[2 * j + 1]]);
    }
  }
  st16(ws + WS_KH + ((size_t)d * SL + t8 * 8) * 2, hi);
  if (KTERMS > 1) st16(ws + WS_KL + ((size_t)d * SL + t8 * 8) * 2, lo);
  STAMP(5); STAMP_WAITV STAMP(6); STAMP_OUT(0);
}

constexpr int CV_U = 2064 * 2;
constexpr int CV_K = 2160 * 2;
constexpr int CV_OFF_K = 2 * CV_U;
constexpr int CV_OFF_Y = CV_OFF_K + 2 * KTERMS * CV_K;
constexpr int CV_Q = (SL + 4) * 4;
constexpr int CV_SMEM = CV_OFF_Y + 2 * 4 * CV_Q;

constexpr int CV_RPF = 2 + 2 * KTERMS;
constexpr int CV_R0 = (KTERMS == 2 ? 6 : 10) * CV_RPF;
constexpr int CV_NM = 68 * KTERMS;
struct CvStep { int a, n, k, first, op, arg, arg2; };
template <int SUB>
struct CvSched {
  CvStep s[CV_NM];
  int wt, wc, slack;
  constexpr CvSched() : s{}, wt(-1), wc(4), slack(1000) {
    const int AMAX = SUB == 0 ? 15 : 14;
    const int NR = (AMAX + 1) * CV_RPF;
    int nr = CV_R0, mi = 0;
    int endat[16] = {};
    for (int a = 0; a < 16; ++a) endat[a] = 1 << 20;
    for (int a = 0; a <= AMAX; ++a) {
      if ((((a + 1) >> 1) & 1) != SUB) continue;
      for (int nn = 0; nn <= a; ++nn) {
        const int n = (a == 0) ? 0 : (nn + 1 < a ? nn + 1 : (nn + 1 == a ? 0 : a));
        for (int k = 0; k < KTERMS; ++k) {
          CvStep st{a, n, k, (nn == 0 && k == 0) ? 1 : 0, 0, 0, 0};
          { const int need = ((n > a - n ? n : a - n) + 1) * CV_RPF; if (nr < NR && nr - need < slack) slack = nr - need; }
          ++mi;
          if (wc == 4) {
            for (int t = wt + 1; t < 16; ++t)
              if (endat[t] < (1 << 20)) { if (endat[t] + 8 <= mi) { wt = t; wc = 0; } break; }
          }
          if (wc < 4) { st.op = 2; st.arg = wt; st.arg2 = wc; ++wc; }
          else if (nr < NR) { st.op = 1; st.arg = nr; ++nr; }
          s[mi - 1] = st;
        }
      }
      endat[a] = mi;
    }
  }
};
template <int R>
__device__ __forceinline__ void cv_rd(s4 (&A)[16][KTERMS][2], s4 (&B)[16][2], const char* ub, const char* kb) {
  constexpr int i = R / CV_RPF, part = R % CV_RPF;
  if constexpr (part < 2) B[i][part] = tr_read(ub + 256 * i + 32 * part);
  else A[i][(part - 2) >> 1][part & 1] = tr_read(kb + ((part - 2) >> 1) * CV_K + 256 * i + 32 * (1 - (part & 1)));
}
template <int SUB, int I>
__device__ __forceinline__ void cv_step(s4 (&A)[16][KTERMS][2], s4 (&B)[16][2], f4 (&acc)[16], const char* ub, const char* kb, char* qb) {
  constexpr CvSched<SUB> S{};
  constexpr CvStep st = S.s[I];
  const s8 av = cat8(A[st.a - st.n][st.k][0], A[st.a - st.n][st.k][1]), bv = cat8(B[st.n][0], B[st.n][1]);
  if constexpr (st.first) asm volatile("v_mfma_f32_16x16x32_f16 %0, %1, %2, 0" : "=&v"(acc[st.a]) : "v"(av), "v"(bv));
  else asm volatile("v_mfma_f32_16x16x32_f16 %0, %1, %2, %0" : "+v"(acc[st.a]) : "v"(av), "v"(bv));
  if constexpr (st.op == 2) *(float*)(qb + st.arg2 * (CV_Q + 4) + st.arg * 512) = acc[st.arg][st.arg2];
  else if constexpr (st.op == 1) cv_rd<st.arg>(A, B, ub, kb);
  SCHED;
}
template <int SUB, int... Rs, int... Is>
__device__ __forceinline__ void conv_mfma_impl(const char* ub, const char* kb, char* qb, iseq<Rs...>, iseq<Is...>) {
  s4 A[16][KTERMS][2];
  s4 B[16][2];
  f4 acc[16];
  (cv_rd<Rs>(A, B, ub, kb), ...);
  SCHED;
  (cv_step<SUB, Is>(A, B, acc, ub, kb, qb), ...);
  asm volatile("s_nop 7");
  SCHED;
  constexpr CvSched<SUB> S{};
  static_assert(S.slack >= 4, "fragment read scheduled too late");
#pragma unroll
  for (int t = 0; t < 16; ++t) {
    if ((((t + 1) >> 1) & 1) != SUB) continue;
#pragma unroll
    for (int r = 0; r < 4; ++r)
      if (t > S.wt || (t == S.wt && r >= S.wc)) *(float*)(qb + r * (CV_Q + 4) + t * 512) = acc[t][r];
  }
}
template <int SUB>
__device__ __forceinline__ void conv_mfma(const char* ub, const char* kb, char* qb) {
  conv_mfma_impl<SUB>(ub, kb, qb, make_iseq<CV_R0>{}, make_iseq<CV_NM>{});
}

__global__ __launch_bounds__(512, 2) void conv_kernel(
    const u16* __restrict__ xT, const float* __restrict__ stats, const u16* __restrict__ Kh,
    const u16* __restrict__ Kl, const float* __restrict__ ksc, const u16* __restrict__ dskip,
    u16* __restrict__ outT) {
  extern __shared__ __attribute__((aligned(16))) char smem[];
  const int tid = threadIdx.x, lane = tid & 63;
  const int wid = __builtin_amdgcn_readfirstlane(tid >> 6);
  STAMP_DECL STAMP(0);
  const int d0 = blockIdx.x * 2;
  const int t0 = tid * 4;
  u2 xr[2];
  u4 kr[KTERMS];
  f4 sr[2];
#pragma unroll
  for (int i = 0; i < 2; ++i) sr[i] = *(const f4*)(stats + t0 * 2 + i * 4);
#pragma unroll
  for (int c = 0; c < 2; ++c) xr[c] = *(const u2*)(xT + xoff(d0 + c, t0));
  kr[0] = *(const u4*)(Kh + (size_t)d0 * SL + tid * 8);
  if (KTERMS > 1) kr[KTERMS - 1] = *(const u4*)(Kl + (size_t)d0 * SL + tid * 8);
  const float2 isc2 = *(const float2*)(ksc + d0);
  const unsigned ds2 = *(const unsigned*)(dskip + d0);
  if (tid < 14 * 2 * KTERMS) {
    int which = tid / 14, j = tid % 14;
    *(u4*)(smem + CV_OFF_K + which * CV_K + j * 16) = u4{0, 0, 0, 0};
  } else if (tid >= 64 && tid < 70) {
    int c = (tid - 64) / 3, j = (tid - 64) % 3;
    *(u2*)(smem + c * CV_U + j * 8) = u2{0, 0};
  } else if (tid >= 128 && tid < 136) {
    *(f4*)(smem + CV_OFF_Y + (tid - 128) * CV_Q) = f4{0, 0, 0, 0};
  }
  STAMP_WAITV STAMP(1);
  float mb[4], ri[4];
#pragma unroll
  for (int j = 0; j < 4; ++j) ln_consts(sr[j >> 1][(j & 1) * 2], sr[j >> 1][(j & 1) * 2 + 1], mb[j], ri[j]);
  u2 ub16[2];
#pragma unroll
  for (int c = 0; c < 2; ++c) {
    unsigned d01 = pack_bf(bflo(xr[c][0]) - mb[0], bfhi(xr[c][0]) - mb[1]);
    unsigned d23 = pack_bf(bflo(xr[c][1]) - mb[2], bfhi(xr[c][1]) - mb[3]);
    ub16[c][0] = pack_bf(bflo(d01) * ri[0], bfhi(d01) * ri[1]);
    ub16[c][1] = pack_bf(bflo(d23) * ri[2], bfhi(d23) * ri[3]);
    u2 a = {pack_h(bflo(ub16[c][0]), bfhi(ub16[c][0])), pack_h(bflo(ub16[c][1]), bfhi(ub16[c][1]))};
    *(u2*)(smem + c * CV_U + 24 + tid * 8) = a;
  }
#pragma unroll
  for (int k = 0; k < KTERMS; ++k)
    *(u4*)(smem + CV_OFF_K + ((tid >> 8) * KTERMS + k) * CV_K + 224 + (tid & 255) * 16) = kr[k];
  __syncthreads();
  STAMP(2);
  {
    const int ch = wid >> 2, par = (wid >> 1) & 1;
    const int g = lane >> 4, q = (lane >> 2) & 3, p = lane & 3;
    const char* ub = smem + ch * CV_U + 8 * (p + q + 8 * g);
    const char* kb = smem + CV_OFF_K + ch * KTERMS * CV_K + 8 * (31 + 16 * par + 4 * p - 8 * g - q) - 32;
    char* qb = smem + CV_OFF_Y + ch * 4 * CV_Q + par * 256 + lane * 4;
    if (wid & 1) conv_mfma<1>(ub, kb, qb);
    else conv_mfma<0>(ub, kb, qb);
  }
  STAMP(3);
  __syncthreads();
  STAMP(4);
#pragma unroll
  for (int c = 0; c < 2; ++c) {
    const char* Q = smem + CV_OFF_Y + c * 4 * CV_Q + t0 * 4;
    f4 y0 = (*(const f4*)(Q) + *(const f4*)(Q + CV_Q)) + (*(const f4*)(Q + 2 * CV_Q) + *(const f4*)(Q + 3 * CV_Q));
    float isc = c ? isc2.y : isc2.x;
    float ds = c ? bfhi(ds2) : bflo(ds2);
    unsigned y01 = pack_bf(y0[0] * isc, y0[1] * isc), y23 = pack_bf(y0[2] * isc, y0[3] * isc);
    unsigned s01 = pack_bf(bflo(ub16[c][0]) * ds, bfhi(ub16[c][0]) * ds), s23 = pack_bf(bflo(ub16[c][1]) * ds, bfhi(ub16[c][1]) * ds);
    unsigned a01 = pack_bf(bflo(y01) + bflo(s01), bfhi(y01) + bfhi(s01)), a23 = pack_bf(bflo(y23) + bflo(s23), bfhi(y23) + bfhi(s23));
    u2 ov = {pack_bf(bflo(xr[c][0]) + bflo(a01), bfhi(xr[c][0]) + bfhi(a01)), pack_bf(bflo(xr[c][1]) + bflo(a23), bfhi(xr[c][1]) + bfhi(a23))};
    st8(outT + xoff(d0 + c, t0), ov);
  }
  STAMP(5); STAMP_WAITV STAMP(6); STAMP_OUT(1);
}

constexpr int G1_SMEM = 512 * 256 + 512 * 8;

__device__ __forceinline__ void glds16(const void* gsrc, unsigned lds_dst) {
  unsigned keep;
  asm volatile("s_mov_b32 %0, m0\n\ts_mov_b32 m0, %2\n\ts_nop 0\n\tglobal_load_lds_dwordx4 %1, off\n\ts_mov_b32 m0, %0"
               : "=&s"(keep) : "v"(gsrc), "s"(lds_dst) : "memory");
}
template <int OFF>
__device__ __forceinline__ void gload16(u4& dst, const void* p) {
  asm volatile("global_load_dwordx4 %0, %1, off offset:%2" : "=v"(dst) : "v"(p), "i"(OFF) : "memory");
}
template <int N>
__device__ __forceinline__ void wait_w(u4& a, u4& b, u4& c, u4& d) {
  asm volatile("s_waitcnt vmcnt(%4)\n\ts_barrier" : "+v"(a), "+v"(b), "+v"(c), "+v"(d) : "i"(N) : "memory");
}

template <int N>
__device__ __forceinline__ void wait_only(u4& a) {
  asm volatile("s_waitcnt vmcnt(%1)" : "+v"(a) : "i"(N) : "memory");
}
template <int N>
__device__ __forceinline__ void wait_vm() { asm volatile("s_waitcnt vmcnt(%0)" :: "i"(N) : "memory"); }
__device__ __forceinline__ void add1(float& a, float b) { asm("v_add_f32 %0, %0, %1" : "+v"(a) : "v"(b)); }
__device__ __forceinline__ void sqa1(float& a, float b) { asm("v_fmac_f32 %0, %1, %1" : "+v"(a) : "v"(b)); }
__device__ __forceinline__ void sth(void* p, u4 v) {
  st16(p, v);
}
__device__ __forceinline__ void bar() { asm volatile("s_waitcnt lgkmcnt(0)\n\ts_barrier" ::: "memory"); }
constexpr int G1_ST = 4 * 32768;
constexpr int G1_ARR = G1_ST + 2048;
#define X_EPI 1
#define F_BPRE 5
#define G1_XPF 3
#define G1_WPF 8

template <int OFF>
__device__ __forceinline__ void gload16_sc1(u4& dst, const void* p) {
  asm volatile("global_load_dwordx4 %0, %1, off offset:%2 sc1" : "=v"(dst) : "v"(p), "i"(OFF) : "memory");
}
template <int N>
__device__ __forceinline__ void wait4(u4 (&a)[4]) {
  asm volatile("s_waitcnt vmcnt(%4)" : "+v"(a[0]), "+v"(a[1]), "+v"(a[2]), "+v"(a[3]) : "i"(N) : "memory");
}

template <bool FINAL>
__global__ __launch_bounds__(512, 2) void ffn_kernel(
    const u16* __restrict__ xT, const u16* __restrict__ W1F, const float* __restrict__ c1,
    u16* HF, float* pst, const u16* __restrict__ W2F, unsigned* cnt,
    u16* __restrict__ out, float* __restrict__ stats) {
  extern __shared__ __attribute__((aligned(16))) char smem[];
  const int tid = threadIdx.x, lane = tid & 63;
  const int wid = __builtin_amdgcn_readfirstlane(tid >> 6);
  STAMP_DECL STAMP(0);
  const int bx = blockIdx.x, xj = bx & 7, r = bx >> 3;
  const int tp = 4 * (xj >> 1) + (r & 3);
  const int fp = 7 * (xj & 1) + (r >> 2);
  if (r < 28) {
  const int h = lane >> 5, blk = (lane >> 4) & 1, q = (lane >> 2) & 3, p = lane & 3;
  const char* rbase = smem + (8 * h + q) * 64 + blk * 32 + 8 * p;
  float* st = (float*)(smem + G1_ST);
  if (wid >= 4) {
    const int hq = wid - 4;
    if (tid == 256) *(unsigned*)(smem + G1_ARR) = 0u;
    const char* xsrc = (const char*)xT + (size_t)tp * 131072 + hq * 1024 + lane * 16;
    const unsigned ldst = (unsigned)(size_t)smem + hq * 1024;
    auto issue_c = [&](int c) AINL {
#pragma unroll
      for (int i = 0; i < 3; ++i) glds16(xsrc + c * 4096 + i * 32768, ldst + c * 4096 + i * 32768);
    };
#pragma unroll
    for (int c = 0; c < G1_XPF; ++c) issue_c(c);
#pragma unroll 1
    for (int c = 0; c < 8 - G1_XPF; ++c) {
      wait_vm<3 * (G1_XPF - 1)>(); bar();
      issue_c(c + G1_XPF);
    }
    sfor<G1_XPF>([&](auto I) AINL {
      constexpr int i = decltype(I)::value;
      if constexpr (i == 0) wait_vm<3 * (G1_XPF - 1)>(); else wait_vm<3 * (G1_XPF - 1 - i) + 8>();
      bar();
      if constexpr (i == 0) {
#pragma unroll
        for (int k = 0; k < 8; ++k) glds16(xsrc + 3 * 32768 + k * 4096, ldst + 3 * 32768 + k * 4096);
      }
    });
    wait_vm<0>();
    bar();
#pragma unroll 1
    for (int it = 0; it < 2; ++it) {
      const int u = it == 0 ? 12 + hq : (fp - 1) * 4 + hq;
      if (it == 1 && (fp < 1 || fp > 3)) break;
      const char* rq = rbase + u * 8192;
      s4 fr[8][2];
      sfor<8>([&](auto I) AINL {
        constexpr int i = decltype(I)::value;
        fr[i][0] = tr_read(rq + i * 1024);
        fr[i][1] = tr_read(rq + i * 1024 + 256);
      });
      float sa = 0.f, sb = 0.f, qa = 0.f, qb = 0.f;
      sfor<8>([&](auto I) AINL {
        constexpr int i = decltype(I)::value;
        u4 xv = __builtin_bit_cast(u4, cat8(fr[i][0], fr[i][1]));
#pragma unroll
        for (int j = 0; j < 4; ++j) {
          float lo = bflo(xv[j]), hi = bfhi(xv[j]);
          add1(sa, lo); add1(sb, hi); sqa1(qa, lo); sqa1(qb, hi);
        }
      });
      float s0 = sa + sb, ss0 = qa + qb;
      s0 += __shfl_xor(s0, 32);
      ss0 += __shfl_xor(ss0, 32);
      if (it == 0) {
        if (lane < 32) st[128 + hq * 32 + lane] = s0;
        bar();
      }
      if (lane < 32 && (it == 1 || fp == 0))
        st8(pst + ((tp * 128 + (u >> 2) * 32 + lane) * 4 + (u & 3)) * 2, u2{__float_as_uint(s0), __float_as_uint(ss0)});
    }
  } else {
  const int ft = fp * 4 + wid;
  u4 wf[32];
  u4 cfu[4];
  const char* wsrc = (const char*)W1F + ((size_t)ft * 32 * 64 + lane) * 16;
  auto issue_w = [&](auto S) AINL {
    constexpr int s = decltype(S)::value;
    gload16<(s & 3) * 1024>(wf[s], wsrc + (s >> 2) * 4096);
  };
  sfor<G1_WPF>(issue_w);
  f16v acc[4], asum;
  s4 xa[3][3][2];
  constexpr int XD = 4, XR = XD + 1;
  s4 xc[XR][2];
  u4 ov;
  float nm[4];
  const int w3 = wid == 3 ? 0 : wid;
  int tto[3];
#pragma unroll
  for (int i = 0; i < 3; ++i) { int tt = i + w3; tto[i] = tt >= 3 ? tt - 3 : tt; }
  const char* rb[3];
#pragma unroll
  for (int i = 0; i < 3; ++i) rb[i] = rbase + tto[i] * 32768;
  u16* dst = HF + ((size_t)((tp * 8 + blk) * 56 + ft) * 64 + (lane & 15) + 16 * h) * 8;
  auto epi_pair = [&](const f16v& a, float m, int e) AINL -> unsigned {
    f4 cf = __builtin_bit_cast(f4, cfu[e >> 2]);
    unsigned pu = pack_bf(fmaf(cf[e & 3], m, a[e]), fmaf(cf[(e & 3) + 1], m, a[e + 1]));
    asm("" : "+v"(pu));
    return __builtin_bit_cast(unsigned, __builtin_elementwise_max(__builtin_bit_cast(s2v, pu), s2v{0, 0}));
  };
  f16v z;
#pragma unroll
  for (int j = 0; j < 16; ++j) z[j] = 0.f;
  const s8 ones8 = {0x3f80, 0x3f80, 0x3f80, 0x3f80, 0x3f80, 0x3f80, 0x3f80, 0x3f80};
  SCHED;
  asm volatile("s_barrier" ::: "memory");
#pragma unroll
  for (int k = 0; k < 2; ++k)
#pragma unroll
    for (int i = 0; i < 3; ++i) { xa[k][i][0] = tr_read(rb[i] + k * 1024); xa[k][i][1] = tr_read(rb[i] + k * 1024 + 256); }
  SCHED;
  sfor<32>([&](auto S) AINL {
    constexpr int s = decltype(S)::value;
    constexpr int issued = (G1_WPF + s <= 32) ? G1_WPF + s : 36;
    asum = __builtin_amdgcn_mfma_f32_32x32x16_bf16(__builtin_bit_cast(b8, ones8), __builtin_bit_cast(b8, cat8(xa[s % 3][0][0], xa[s % 3][0][1])), s == 0 ? z : asum, 0, 0, 0);
    wait_only<issued - s - 1>(wf[s]);
    if constexpr ((s & 3) == 2 && s < 30) asm volatile("s_barrier" ::: "memory");
    SCHED;
    sfor<3>([&](auto I) AINL {
      constexpr int i = decltype(I)::value;
      const b8 xv = __builtin_bit_cast(b8, cat8(xa[s % 3][i][0], xa[s % 3][i][1]));
      acc[i] = __builtin_amdgcn_mfma_f32_32x32x16_bf16(__builtin_bit_cast(b8, wf[s]), xv, s == 0 ? z : acc[i], 0, 0, 0);
      if constexpr (s + 2 < 32) {
        xa[(s + 2) % 3][i][0] = tr_read(rb[i] + (s + 2) * 1024);
        xa[(s + 2) % 3][i][1] = tr_read(rb[i] + (s + 2) * 1024 + 256);
      }
      if constexpr (i == 0) {
        if constexpr (G1_WPF + s < 32) issue_w(ic<(G1_WPF + s < 32) ? G1_WPF + s : 0>{});
        if constexpr (G1_WPF + s == 32) {
          gload16<0>(cfu[0], c1 + ft * 32 + 4 * h);
          gload16<32>(cfu[1], c1 + ft * 32 + 4 * h);
          gload16<64>(cfu[2], c1 + ft * 32 + 4 * h);
          gload16<96>(cfu[3], c1 + ft * 32 + 4 * h);
        }
      }
      SCHED;
    });
  });
  STAMP(2);
  if (lane < 32) st[wid * 32 + lane] = -rbf(asum[0] * (1.0f / DM));
  asm volatile("s_waitcnt vmcnt(0) lgkmcnt(0)\n\ts_barrier" : "+v"(cfu[0]), "+v"(cfu[1]), "+v"(cfu[2]), "+v"(cfu[3]) :: "memory");
  {
    unsigned rbo = (unsigned)(rbase - smem) + 3 * 32768;
    asm volatile("" : "+v"(rbo));
    const char* rbb = smem + rbo;
    sfor<XD>([&](auto I) AINL {
      constexpr int i = decltype(I)::value;
      xc[i][0] = tr_read(rbb + i * 1024); xc[i][1] = tr_read(rbb + i * 1024 + 256);
    });
#pragma unroll
    for (int i = 0; i < 3; ++i) nm[i] = st[tto[i] * 32 + (lane & 31)];
    SCHED;
    sfor<32>([&](auto S) AINL {
      constexpr int s = decltype(S)::value;
      const b8 xv = __builtin_bit_cast(b8, cat8(xc[s % XR][0], xc[s % XR][1]));
      acc[3] = __builtin_amdgcn_mfma_f32_32x32x16_bf16(__builtin_bit_cast(b8, wf[s]), xv, s == 0 ? z : acc[3], 0, 0, 0);
      if constexpr (s + XD < 32) {
        xc[(s + XD) % XR][0] = tr_read(rbb + (s + XD) * 1024);
        xc[(s + XD) % XR][1] = tr_read(rbb + (s + XD) * 1024 + 256);
      }
      if constexpr (s < 24) {
        constexpr int i = s >> 3, pr = s & 7;
        ov[pr & 3] = epi_pair(acc[i], nm[i], 2 * pr);
        if constexpr ((pr & 3) == 3) sth(dst + tto[i] * (2 * 56 * 512) + (pr >> 2) * 256, ov);
      }
      SCHED;
    });
  }
  asm volatile("s_barrier" ::: "memory");
  {
    const float* sp = st + 128 + (lane & 31);
    nm[3] = -rbf(((sp[0] + sp[32]) + (sp[64] + sp[96])) * (1.0f / DM));
  }
#pragma unroll
  for (int c = 0; c < 2; ++c) {
#pragma unroll
    for (int j = 0; j < 4; ++j) ov[j] = epi_pair(acc[3], nm[3], 8 * c + 2 * j);
    sth(dst + 3 * (2 * 56 * 512) + c * 256, ov);
  }
  }
  }
  STAMP(3);
  const int tb = 8 * (xj >> 1) + (r & 7), db = 4 * (xj & 1) + (r >> 3);
  const int g = lane >> 4, n = lane & 15;
  const int mo = wid >> 1, no = 2 * (wid & 1);
  u4 bf[7][4], af[7][4];
  {
    const char* bp[4];
#pragma unroll
    for (int j = 0; j < 4; ++j) bp[j] = (const char*)W2F + ((size_t)((db * 4 + ((j + no) & 3)) * 56 + wid * 7) * 64 + lane) * 16;
    auto issue_b = [&](auto KS) AINL {
      constexpr int ks = decltype(KS)::value;
      sfor<4>([&](auto J) AINL {
        constexpr int j = decltype(J)::value;
        gload16<(ks & 3) * 1024>(bf[ks][j], bp[j] + (ks >> 2) * 4096);
      });
    };
    wait_vm<0>();
    bar();
    STAMP(1);
    if (r < 28 && tid == 0) __hip_atomic_fetch_add(cnt + tp * 32, 1u, __ATOMIC_RELAXED, __HIP_MEMORY_SCOPE_AGENT);
    if (wid == 7) {
      const unsigned* cp = cnt + (tb >> 1) * 32;
      for (int spin = 0; spin < (1 << 22); ++spin) {
        if (__hip_atomic_load(cp, __ATOMIC_RELAXED, __HIP_MEMORY_SCOPE_AGENT) >= 14u) break;
        __builtin_amdgcn_s_sleep(2);
      }
    }
    sfor<F_BPRE>(issue_b);
    bar();
    STAMP(4);
    sfor<7 - F_BPRE>([&](auto I) AINL { issue_b(ic<F_BPRE + decltype(I)::value>{}); });
  }
  u2 res[2];
  u16 resf[2][4];
  if (!FINAL) {
#pragma unroll
    for (int j = 0; j < 2; ++j) res[j] = *(const u2*)(xT + xoff(db * 64 + (no + j) * 16 + n, tb * 64 + mo * 16 + 4 * g));
  } else {
#pragma unroll
    for (int j = 0; j < 2; ++j)
#pragma unroll
      for (int e = 0; e < 4; ++e) resf[j][e] = xT[xoff(db * 64 + (no + j) * 16 + 4 * g + e, tb * 64 + mo * 16 + n)];
  }
  const unsigned long long psu = __hip_atomic_load((const unsigned long long*)(pst + ((tb * 64 + mo * 16 + n) * 4 + g) * 2), __ATOMIC_RELAXED, __HIP_MEMORY_SCOPE_AGENT);
  const char* ap[4];
#pragma unroll
  for (int i = 0; i < 4; ++i) ap[i] = (const char*)HF + ((size_t)((tb * 4 + ((i + mo) & 3)) * 56 + wid * 7) * 64 + lane) * 16;
  auto issue_a = [&](auto KS) AINL {
    constexpr int ks = decltype(KS)::value;
    sfor<4>([&](auto I) AINL {
      constexpr int i = decltype(I)::value;
      gload16_sc1<(ks & 3) * 1024>(af[ks][i], ap[i] + (ks >> 2) * 4096);
    });
  };
  sfor<4>(issue_a);
  f4 acc[4][4];
#pragma unroll
  for (int i = 0; i < 4; ++i)
#pragma unroll
    for (int j = 0; j < 4; ++j) acc[i][j] = f4{0, 0, 0, 0};
  SCHED;
  sfor<7>([&](auto KS) AINL {
    constexpr int ks = decltype(KS)::value;
    wait4<(ks < 4 ? 12 : 4 * (6 - ks))>(af[ks]);
    wait4<63>(bf[ks]);
    SCHED;
#pragma unroll
    for (int i = 0; i < 4; ++i)
#pragma unroll
      for (int j = 0; j < 4; ++j) {
        if (!FINAL) acc[i][j] = __builtin_amdgcn_mfma_f32_16x16x32_bf16(__builtin_bit_cast(b8, af[ks][i]), __builtin_bit_cast(b8, bf[ks][j]), acc[i][j], 0, 0, 0);
        else acc[i][j] = __builtin_amdgcn_mfma_f32_16x16x32_bf16(__builtin_bit_cast(b8, bf[ks][j]), __builtin_bit_cast(b8, af[ks][i]), acc[i][j], 0, 0, 0);
      }
    SCHED;
    if constexpr (ks + 4 < 7) issue_a(ic<(ks + 4 < 7) ? ks + 4 : 0>{});
    SCHED;
  });
  STAMP(5);
  f4* red = (f4*)smem;
#pragma unroll
  for (int i = 0; i < 4; ++i)
#pragma unroll
    for (int jp = 0; jp < 2; ++jp) {
      if (i == 0 && jp == 0) continue;
      const int owner = ((i + mo) & 3) * 2 + ((jp + wid) & 1);
      red[((owner * 7 + i * 2 + jp - 1) * 2 + 0) * 64 + lane] = acc[i][2 * jp];
      red[((owner * 7 + i * 2 + jp - 1) * 2 + 1) * 64 + lane] = acc[i][2 * jp + 1];
    }
  f4 rr;
  {
    float sx = __uint_as_float((unsigned)psu), sy = __uint_as_float((unsigned)(psu >> 32));
    sx += __shfl_xor(sx, 16); sy += __shfl_xor(sy, 16);
    sx += __shfl_xor(sx, 32); sy += __shfl_xor(sy, 32);
    float mb, ri;
    ln_consts(sx, sy, mb, ri);
    if (FINAL) rr = f4{ri, ri, ri, ri};
    else {
#pragma unroll
      for (int e = 0; e < 4; ++e) rr[e] = __shfl(ri, 4 * g + e);
    }
  }
  bar();
  STAMP(6);
  float s[4] = {0, 0, 0, 0}, ss[4] = {0, 0, 0, 0};
#pragma unroll
  for (int j = 0; j < 2; ++j) {
    const f4* rp = red + (wid * 14 + j) * 64 + lane;
    f4 v = ((acc[0][j] + rp[0]) + (rp[128] + rp[256])) + ((rp[384] + rp[512]) + (rp[640] + rp[768]));
    v *= rr;
    unsigned m01 = pack_bf(v[0], v[1]), m23 = pack_bf(v[2], v[3]);
    u2 ov;
    if (!FINAL) {
      ov[0] = pack_bf(bflo(res[j][0]) + bflo(m01), bfhi(res[j][0]) + bfhi(m01));
      ov[1] = pack_bf(bflo(res[j][1]) + bflo(m23), bfhi(res[j][1]) + bfhi(m23));
      *(u2*)(out + xoff(db * 64 + (no + j) * 16 + n, tb * 64 + mo * 16 + 4 * g)) = ov;
      float o[4] = {bflo(ov[0]), bfhi(ov[0]), bflo(ov[1]), bfhi(ov[1])};
#pragma unroll
      for (int e = 0; e < 4; ++e) { s[e] += o[e]; ss[e] = fmaf(o[e], o[e], ss[e]); }
    } else {
      ov[0] = pack_bf(bf2f(resf[j][0]) + bflo(m01), bf2f(resf[j][1]) + bfhi(m01));
      ov[1] = pack_bf(bf2f(resf[j][2]) + bflo(m23), bf2f(resf[j][3]) + bfhi(m23));
      *(u2*)(out + (size_t)(tb * 64 + mo * 16 + n) * DM + db * 64 + (no + j) * 16 + 4 * g) = ov;
    }
  }
  if (!FINAL) {
#pragma unroll
    for (int e = 0; e < 4; ++e) { s[e] = row_sum16(s[e]); ss[e] = row_sum16(ss[e]); }
    if (n < 8) {
      int e = n >> 1;
      float v = (n & 1) ? (e == 0 ? ss[0] : e == 1 ? ss[1] : e == 2 ? ss[2] : ss[3]) : (e == 0 ? s[0] : e == 1 ? s[1] : e == 2 ? s[2] : s[3]);
      atomicAdd(stats + (tb * 64 + mo * 16 + 4 * g) * 2 + n, v);
    }
  }
  STAMP_WAITV STAMP(7); STAMP_OUT(FINAL ? 3 : 2);
}

extern "C" void kernel_launch(void* const* d_in, const int* in_sizes, int n_in,
                              void* d_out, int out_size, void* d_ws, size_t ws_size,
                              hipStream_t stream) {
  const u16* x = (const u16*)d_in[0];
  const u16* logA = (const u16*)d_in[1];
  const u16* Aim = (const u16*)d_in[2];
  const u16* Cre = (const u16*)d_in[3];
  const u16* Cim = (const u16*)d_in[4];
  const u16* logdt = (const u16*)d_in[5];
  const u16* dskip = (const u16*)d_in[6];
  const u16* w1 = (const u16*)d_in[8];
  const u16* w2 = (const u16*)d_in[9];
  char* ws = (char*)d_ws;
  u16* out = (u16*)d_out;
  (void)hipFuncSetAttribute((const void*)ffn_kernel<false>, hipFuncAttributeMaxDynamicSharedMemorySize, G1_SMEM);
  (void)hipFuncSetAttribute((const void*)ffn_kernel<true>, hipFuncAttributeMaxDynamicSharedMemorySize, G1_SMEM);
  (void)hipFuncSetAttribute((const void*)conv_kernel, hipFuncAttributeMaxDynamicSharedMemorySize, CV_SMEM);

  prep_kernel<<<256 + PREP_W1 + PREP_W2 + PREP_TR, 512, 0, stream>>>(x, logA, Aim, Cre, Cim, logdt, w1, w2, ws);
  conv_kernel<<<256, 512, CV_SMEM, stream>>>((const u16*)(ws + WS_XT0), (const float*)(ws + WS_ST0), (const u16*)(ws + WS_KH),
                                             (const u16*)(ws + WS_KL), (const float*)(ws + WS_KSC), dskip, (u16*)(ws + WS_XT1));
  ffn_kernel<false><<<256, 512, G1_SMEM, stream>>>((const u16*)(ws + WS_XT1), (const u16*)(ws + WS_W1F), (const float*)(ws + WS_C1),
                                                   (u16*)(ws + WS_HF), (float*)(ws + WS_PST), (const u16*)(ws + WS_W2F),
                                                   (unsigned*)(ws + WS_CNT), (u16*)(ws + WS_XT2), (float*)(ws + WS_ST2));
  conv_kernel<<<256, 512, CV_SMEM, stream>>>((const u16*)(ws + WS_XT2), (const float*)(ws + WS_ST2), (const u16*)(ws + WS_KH),
                                             (const u16*)(ws + WS_KL), (const float*)(ws + WS_KSC), dskip, (u16*)(ws + WS_XT3));
  ffn_kernel<true><<<256, 512, G1_SMEM, stream>>>((const u16*)(ws + WS_XT3), (const u16*)(ws + WS_W1F), (const float*)(ws + WS_C1),
                                                  (u16*)(ws + WS_HF2), (float*)(ws + WS_PST2), (const u16*)(ws + WS_W2F),
                                                  (unsigned*)(ws + WS_CNT) + 512, out, nullptr);
}
```

```cpp
#include <hip/hip_runtime.h>
#include <hip/hip_bf16.h>

typedef unsigned short u16;
typedef short s4 __attribute__((ext_vector_type(4)));
typedef short s2v __attribute__((ext_vector_type(2)));
typedef short s8 __attribute__((ext_vector_type(8)));
typedef _Float16 h8 __attribute__((ext_vector_type(8)));
typedef _Float16 h2 __attribute__((ext_vector_type(2)));
typedef __bf16 b2 __attribute__((ext_vector_type(2)));
typedef __bf16 b8 __attribute__((ext_vector_type(8)));
typedef float f4 __attribute__((ext_vector_type(4)));
typedef float f2 __attribute__((ext_vector_type(2)));
typedef __bf16 b4 __attribute__((ext_vector_type(4)));
typedef float f16v __attribute__((ext_vector_type(16)));
typedef unsigned u4 __attribute__((ext_vector_type(4)));
typedef unsigned u2 __attribute__((ext_vector_type(2)));
typedef __attribute__((address_space(3))) s4* lds_s4p;

#define SCHED __builtin_amdgcn_sched_barrier(0)
template <int... Is> struct iseq {};
template <int N, int... Is> struct mk_iseq : mk_iseq<N - 1, N - 1, Is...> {};
template <int... Is> struct mk_iseq<0, Is...> { typedef iseq<Is...> type; };
template <int N> using make_iseq = typename mk_iseq<N>::type;
template <int I> struct ic { static constexpr int value = I; };
template <class F, int... Is> __device__ __forceinline__ void sfor_impl(F&& f, iseq<Is...>) { (f(ic<Is>{}), ...); }
template <int N, class F> __device__ __forceinline__ void sfor(F&& f) { sfor_impl(f, make_iseq<N>{}); }
#define AINL __attribute__((always_inline))

constexpr int SL = 2048;
constexpr int DM = 512;
constexpr int NS = 128;
constexpr int FF = 1792;
#define KTERMS 1

constexpr size_t WS_XT0 = 0;
constexpr size_t WS_XT1 = WS_XT0 + (size_t)SL * DM * 2;
constexpr size_t WS_XT2 = WS_XT1 + (size_t)SL * DM * 2;
constexpr size_t WS_XT3 = WS_XT2 + (size_t)SL * DM * 2;
constexpr size_t WS_KH  = WS_XT3 + (size_t)SL * DM * 2;
constexpr size_t WS_KL  = WS_KH + (size_t)SL * DM * 2;
constexpr size_t WS_W1F = WS_KL + (size_t)SL * DM * 2;
constexpr size_t WS_W2F = WS_W1F + (size_t)DM * FF * 2;
constexpr size_t WS_HF  = WS_W2F + (size_t)DM * FF * 2;
constexpr size_t WS_HF2 = WS_HF + (size_t)SL * FF * 2;
constexpr size_t WS_ST0 = WS_HF2 + (size_t)SL * FF * 2;
constexpr size_t WS_ST2 = WS_ST0 + (size_t)SL * 8;
constexpr size_t WS_C1  = WS_ST2 + (size_t)SL * 8;
constexpr size_t WS_KSC = WS_C1 + (size_t)FF * 4;
constexpr size_t WS_PST = WS_KSC + (size_t)DM * 4;
constexpr size_t WS_PST2 = WS_PST + (size_t)SL * 32;
constexpr size_t WS_CNT = WS_PST2 + (size_t)SL * 32;

__host__ __device__ __forceinline__ int xoff(int d, int t) { return (t >> 5) * 16384 + d * 32 + (t & 31); }
__device__ __forceinline__ float bf2f(unsigned h) { return __uint_as_float(h << 16); }
__device__ __forceinline__ float bflo(unsigned w) { return __uint_as_float(w << 16); }
__device__ __forceinline__ float bfhi(unsigned w) { return __uint_as_float(w & 0xffff0000u); }
__device__ __forceinline__ unsigned pack_bf(float a, float b) {
  b2 v = {(__bf16)a, (__bf16)b};
  return __builtin_bit_cast(unsigned, v);
}
__device__ __forceinline__ float rbf(float f) {
  return __uint_as_float(((unsigned)__builtin_bit_cast(u16, (__bf16)f)) << 16);
}
__device__ __forceinline__ unsigned pack_h(float a, float b) {
  h2 v = {(_Float16)a, (_Float16)b};
  return __builtin_bit_cast(unsigned, v);
}
__device__ __forceinline__ float dot2(unsigned a, unsigned b, float c) {
  return __builtin_amdgcn_fdot2_f32_bf16(__builtin_bit_cast(b2, a), __builtin_bit_cast(b2, b), c, false);
}
__device__ __forceinline__ s4 tr_read(const char* p) {
  return __builtin_amdgcn_ds_read_tr16_b64_v4i16((lds_s4p)p);
}
__device__ __forceinline__ s8 cat8(s4 a, s4 b) { return __builtin_shufflevector(a, b, 0, 1, 2, 3, 4, 5, 6, 7); }
template <int CTRL>
__device__ __forceinline__ float dppf(float v) {
  return __builtin_bit_cast(float, __builtin_amdgcn_update_dpp(0, __builtin_bit_cast(int, v), CTRL, 0xf, 0xf, true));
}
__device__ __forceinline__ float shr1(float v) { return dppf<0x138>(v); }
__device__ __forceinline__ float row_sum16(float v) {
  v += dppf<0x128>(v);
  v += dppf<0x124>(v);
  v += dppf<0x122>(v);
  v += dppf<0x121>(v);
  return v;
}
constexpr unsigned BF_ONES = 0x3f803f80u;
__device__ __forceinline__ void st16(void* p, u4 v) { asm volatile("global_store_dwordx4 %0, %1, off sc1\n\ts_nop 1" :: "v"(p), "v"(v) : "memory"); }
__device__ __forceinline__ void st8(void* p, u2 v) { asm volatile("global_store_dwordx2 %0, %1, off sc1\n\ts_nop 0" :: "v"(p), "v"(v) : "memory"); }

__device__ __forceinline__ void ln_consts(float s, float ss, float& mb, float& rinv) {
  float mean = s * (1.0f / DM);
  float var = fmaxf(ss * (1.0f / DM) - mean * mean, 0.0f);
  mb = rbf(mean);
  float sd = rbf(__builtin_amdgcn_sqrtf(rbf(rbf(var) + 1e-5f)));
  rinv = __builtin_amdgcn_rcpf(sd);
}

constexpr int PREP_W1 = 56, PREP_W2 = 56, PREP_TR = 64;
constexpr int PK_PAR = 0, PK_PARC = 2048, PK_RED = 3072, PK_KV = PK_RED + 12288, PK_MX = PK_KV + 3072, PK_SZ = PK_MX + 128;
constexpr int PREP_SMEM = (32768 + 1024 > 2 * PK_SZ) ? 32768 + 1024 : 2 * PK_SZ;

__device__ __forceinline__ int alpha_of(int idx) {
  if (idx < 16) return (idx >> 3) * 128 + (idx & 7);
  if (idx < 24) { int j = idx - 16; return (2 + (j >> 2)) * 128 + (j & 3) * 2; }
  if (idx < 32) { int j = idx - 24; return (4 + (j >> 1)) * 128 + (j & 1) * 4; }
  if (idx < 41) return (8 + idx - 32) * 128;
  return 0;
}
__device__ __forceinline__ float exp_acc(float x) {
  const float L2EH = 1.44269504f, L2EL = (float)(1.4426950408889634 - (double)1.44269504f);
  float t = x * L2EH;
  float e = fmaf(x, L2EH, -t) + x * L2EL;
  float r = __builtin_amdgcn_exp2f(t);
  return fmaf(r, e * 0.69314718f, r);
}
__device__ __forceinline__ float row_max16(float v) {
  v = fmaxf(v, dppf<0x128>(v));
  v = fmaxf(v, dppf<0x124>(v));
  v = fmaxf(v, dppf<0x122>(v));
  v = fmaxf(v, dppf<0x121>(v));
  return v;
}
__device__ __forceinline__ void split_h(float a, float b, unsigned& hi, unsigned& lo) {
  float ah = __uint_as_float(__float_as_uint(a) & 0xffffe000u), bh = __uint_as_float(__float_as_uint(b) & 0xffffe000u);
  hi = pack_h(ah, bh);
  lo = pack_h(a - ah, b - bh);
}

__global__ __launch_bounds__(512, 4) void prep_kernel(
    const u16* __restrict__ x, const u16* __restrict__ logA, const u16* __restrict__ Aim,
    const u16* __restrict__ Cre, const u16* __restrict__ Cim, const u16* __restrict__ logdt,
    const u16* __restrict__ w1, const u16* __restrict__ w2, char* __restrict__ ws) {
  __shared__ __attribute__((aligned(16))) char smem[PREP_SMEM];
  const int tid = threadIdx.x, lane = tid & 63;
  const int wid = __builtin_amdgcn_readfirstlane(tid >> 6);
  const int b = blockIdx.x;
  const int idx = lane & 15, g = lane >> 4;
  if (b >= 256) {
    const int bj = b - 256;
    const int job = bj < PREP_W1 ? 0 : (bj < PREP_W1 + PREP_W2 ? 1 : 2);
    const int jb = job == 0 ? bj : (job == 1 ? bj - PREP_W1 : bj - PREP_W1 - PREP_W2);
    u4 v[4];
    int lofs;
    int ls1, ls2;
    {
      const u16* src;
      int gs1, gs2;
      if (job == 0) {
        src = w1 + (size_t)(tid >> 2) * FF + jb * 32 + (tid & 3) * 8;
        gs1 = 128 * FF; gs2 = 256 * FF; lofs = tid * 16; ls1 = 8192; ls2 = 16384;
      } else if (job == 1) {
        src = w2 + (size_t)((jb >> 2) * 128 + (tid >> 4)) * DM + (jb & 3) * 128 + (tid & 15) * 8;
        gs1 = 32 * DM; gs2 = 64 * DM; lofs = (tid >> 4) * 256 + (((tid & 15) ^ (((tid >> 4) & 7) << 1)) * 16); ls1 = 8192; ls2 = 16384;
      } else {
        const int r0 = (wid >> 2) * 8 + (wid & 3);
        src = x + (size_t)(jb * 32 + r0) * DM + lane * 8;
        gs1 = 4 * DM; gs2 = 16 * DM; lofs = r0 * 1024 + ((lane ^ (wid << 1)) * 16); ls1 = 4096; ls2 = 16384;
      }
      v[0] = *(const u4*)(src); v[1] = *(const u4*)(src + gs1); v[2] = *(const u4*)(src + gs2); v[3] = *(const u4*)(src + gs1 + gs2);
    }
    *(u4*)(smem + lofs) = v[0];
    *(u4*)(smem + lofs + ls1) = v[1];
    *(u4*)(smem + lofs + ls2) = v[2];
    *(u4*)(smem + lofs + ls1 + ls2) = v[3];
    __syncthreads();
    const int q = (lane >> 2) & 3, p = lane & 3;
    float* csr = (float*)(smem + 32768);
    if (job == 0) {
      const int h = lane >> 5, blk = (lane >> 4) & 1;
      const char* rb = smem + (wid * 64 + 8 * h + q) * 64 + blk * 32 + p * 8;
      u16* W1F = (u16*)(ws + WS_W1F);
      float cs = 0.f;
#pragma unroll
      for (int i = 0; i < 4; ++i) {
        s8 o = cat8(tr_read(rb + i * 1024), tr_read(rb + i * 1024 + 256));
        u4 ou = __builtin_bit_cast(u4, o);
#pragma unroll
        for (int j = 0; j < 4; ++j) cs = dot2(ou[j], BF_ONES, cs);
        st16(W1F + ((size_t)(jb * 32 + wid * 4 + i) * 64 + lane) * 8, ou);
      }
      cs += __shfl_xor(cs, 32);
      if (lane < 32) csr[wid * 32 + lane] = cs;
    } else if (job == 1) {
      const int rl = 16 * (g >> 1) + 4 * (g & 1) + q;
      const int sw = rl & 7;
      u16* W2F = (u16*)(ws + WS_W2F);
#pragma unroll
      for (int i = 0; i < 4; ++i) {
        const int nt = (wid & 1) * 4 + i;
        const char* rb = smem + ((wid >> 1) * 32 + rl) * 256 + ((nt ^ sw) * 32) + p * 8;
        s8 o = cat8(tr_read(rb), tr_read(rb + 2048));
        st16(W2F + ((size_t)(((jb & 3) * 8 + nt) * 56 + (jb >> 2) * 4 + (wid >> 1)) * 64 + lane) * 8, __builtin_bit_cast(u4, o));
      }
    } else if (job == 2) {
      const int sw = q | ((g & 1) << 2);
      u16* xT = (u16*)(ws + WS_XT0);
#pragma unroll
      for (int i = 0; i < 4; ++i) {
        int dt_ = wid * 4 + i;
        const char* base = smem + (8 * g + q) * 1024 + ((dt_ ^ sw) * 32) + 8 * p;
        s8 o = cat8(tr_read(base), tr_read(base + 4096));
        *(s8*)(xT + xoff(dt_ * 16 + idx, jb * 32 + 8 * g)) = o;
      }
      float sv[4], ssv[4];
#pragma unroll
      for (int i = 0; i < 4; ++i) {
        float s = 0.f, ss = 0.f;
#pragma unroll
        for (int j = 0; j < 4; ++j) { s = dot2(v[i][j], BF_ONES, s); ss = dot2(v[i][j], v[i][j], ss); }
        sv[i] = row_sum16(s); ssv[i] = row_sum16(ss);
      }
      float val = sv[0];
#pragma unroll
      for (int i = 1; i < 4; ++i) val = (idx == 2 * i) ? sv[i] : val;
#pragma unroll
      for (int i = 0; i < 4; ++i) val = (idx == 2 * i + 1) ? ssv[i] : val;
      val += __shfl_xor(val, 16);
      val += __shfl_xor(val, 32);
      if (lane < 8) {
        const int i = lane >> 1;
        const int r = jb * 32 + (i >> 1) * 16 + (wid >> 2) * 8 + (i & 1) * 4 + (wid & 3);
        ((float*)(ws + WS_ST0))[r * 2 + (lane & 1)] = val;
        ((float*)(ws + WS_ST2))[r * 2 + (lane & 1)] = 0.f;
      }
    }

    if (job == 0) {
      __syncthreads();
      if (tid < 32) {
        float cs = 0.f;
#pragma unroll
        for (int i = 0; i < 8; ++i) cs += csr[i * 32 + tid];
        ((float*)(ws + WS_C1))[jb * 32 + tid] = cs;
      }
    }
    if (bj == 0 && tid >= 64 && tid < 96) ((unsigned*)(ws + WS_CNT))[(tid - 64) * 32] = 0u;
    return;
  }
  const int ch = wid >> 2, w4 = wid & 3, t8 = tid & 255;
  const int d = b * 2 + ch;
  const int pn = d * NS + (t8 & (NS - 1));
  const u16 pl0 = logdt[d], pl1 = logA[pn], pl2 = Aim[pn], pl3 = Cre[pn], pl4 = Cim[pn];
  SCHED;
  int kidx[8];
#pragma unroll
  for (int j = 0; j < 8; ++j) {
    float pf = rbf((float)(t8 * 8 + j));
    int p = (int)pf;
    int e = (int)(__float_as_uint(pf) >> 23) - (127 + 7);
    e = e < 0 ? 0 : (e > 3 ? 3 : e);
    int ai_ = ((((p >> 7) << 3) + (p & 7)) >> e) + 8 * e;
    kidx[j] = ch * PK_SZ + PK_KV + (ai_ * 16 + ((p & 127) >> 3)) * 4;
    asm volatile("" : "+v"(kidx[j]));
  }
  char* ks = smem + ch * PK_SZ;
  f4* par = (f4*)(ks + PK_PAR);
  f2* parc = (f2*)(ks + PK_PARC);
  f4* red = (f4*)(ks + PK_RED);
  float* kv = (float*)(ks + PK_KV);
  float* mc = (float*)(ks + PK_MX);
  SCHED;
  {
    float dt = rbf(exp_acc(bf2f(pl0)));
    float Ar = -rbf(exp_acc(bf2f(pl1)));
    float ar = Ar * dt;
    float ai = bf2f(pl2) * dt;
    const float I2PH = 0.15915494f;
    const float I2PL = (float)(0.15915494309189535 - (double)0.15915494f);
    float fh = ai * I2PH;
    float fl = fmaf(ai, I2PH, -fh) + ai * I2PL;
    fh -= rintf(fh);
    float fhi = __uint_as_float(__float_as_uint(fh) & 0xfffff000u);
    float flo = (fh - fhi) + fl;
    float crv = 2.0f * bf2f(pl3) * dt;
    float civ = 2.0f * bf2f(pl4) * dt;
    float cm = row_sum16(fabsf(crv) + fabsf(civ));
    float p0 = ar * 1.44269504f;
    asm volatile("" : "+v"(p0), "+v"(fhi), "+v"(flo), "+v"(cm));
    if (w4 < 2) {
      par[t8] = f4{p0, fhi, flo, 0.f};
      parc[t8] = f2{crv, civ};
      if (idx == 0) mc[t8 >> 4] = cm;
    }
  }
  __syncthreads();
  int ce;
  {
    f4 m0 = *(const f4*)(mc), m1 = *(const f4*)(mc + 4);
    float m = ((m0[0] + m0[1]) + (m0[2] + m0[3])) + ((m1[0] + m1[1]) + (m1[2] + m1[3]));
    ce = (int)((__float_as_uint(m) >> 23) & 0xff) - 127;
    ce = ce < -100 ? -100 : (ce > 100 ? 100 : ce);
  }
  {
    const f2 pos01 = {(float)(8 * idx), (float)alpha_of(idx)};
    const f2 pos23 = {(float)alpha_of(16 + idx), (float)alpha_of(32 + idx)};
    const f2 eo01 = {(float)(12 - ce), 13.f}, eo23 = {13.f, 13.f};
    f4 acc0 = {0, 0, 0, 0}, acc1 = {0, 0, 0, 0}, acc2 = {0, 0, 0, 0};
#pragma unroll
    for (int k2 = 0; k2 < 2; ++k2) {
      u4 hi[4], lo[4];
#pragma unroll
      for (int e = 0; e < 4; ++e) {
        const int n = (w4 * 2 + k2) * 16 + 4 * g + e;
        const f4 pp = par[n];
        const f2 pc = parc[n];
        f2 t01 = pos01 * pp[1], t23 = pos23 * pp[1];
        f2 r01 = {__builtin_amdgcn_fractf(t01[0]), __builtin_amdgcn_fractf(t01[1])};
        f2 r23 = {__builtin_amdgcn_fractf(t23[0]), __builtin_amdgcn_fractf(t23[1])};
        r01 = __builtin_elementwise_fma(pos01, f2{pp[2], pp[2]}, r01);
        r23 = __builtin_elementwise_fma(pos23, f2{pp[2], pp[2]}, r23);
        f2 a01 = __builtin_elementwise_fma(pos01, f2{pp[0], pp[0]}, eo01);
        f2 a23 = __builtin_elementwise_fma(pos23, f2{pp[0], pp[0]}, eo23);
        float rv[4] = {r01[0], r01[1], r23[0], r23[1]};
        float av[4] = {a01[0], a01[1], a23[0], a23[1]};
#pragma unroll
        for (int qq = 0; qq < 4; ++qq) {
          float ex = __builtin_amdgcn_exp2f(av[qq]);
          float cs = __builtin_amdgcn_cosf(rv[qq]), sn = __builtin_amdgcn_sinf(rv[qq]);
          f2 z;
          if (qq == 0) {
            f2 t = f2{cs, sn} * pc[0];
            t = __builtin_elementwise_fma(f2{-sn, cs}, f2{pc[1], pc[1]}, t);
            z = t * ex;
          } else {
            z = f2{cs, -sn} * ex;
          }
          unsigned hh, ll;
          split_h(z[0], z[1], hh, ll);
          hi[qq][e] = hh; lo[qq][e] = ll;
        }
      }
      const h8 bh = __builtin_bit_cast(h8, hi[0]), bl = __builtin_bit_cast(h8, lo[0]);
      acc0 = __builtin_amdgcn_mfma_f32_16x16x32_f16(__builtin_bit_cast(h8, lo[1]), bh, acc0, 0, 0, 0);
      acc1 = __builtin_amdgcn_mfma_f32_16x16x32_f16(__builtin_bit_cast(h8, lo[2]), bh, acc1, 0, 0, 0);
      acc2 = __builtin_amdgcn_mfma_f32_16x16x32_f16(__builtin_bit_cast(h8, lo[3]), bh, acc2, 0, 0, 0);
      acc0 = __builtin_amdgcn_mfma_f32_16x16x32_f16(__builtin_bit_cast(h8, hi[1]), bl, acc0, 0, 0, 0);
      acc1 = __builtin_amdgcn_mfma_f32_16x16x32_f16(__builtin_bit_cast(h8, hi[2]), bl, acc1, 0, 0, 0);
      acc2 = __builtin_amdgcn_mfma_f32_16x16x32_f16(__builtin_bit_cast(h8, hi[3]), bl, acc2, 0, 0, 0);
      acc0 = __builtin_amdgcn_mfma_f32_16x16x32_f16(__builtin_bit_cast(h8, hi[1]), bh, acc0, 0, 0, 0);
      acc1 = __builtin_amdgcn_mfma_f32_16x16x32_f16(__builtin_bit_cast(h8, hi[2]), bh, acc1, 0, 0, 0);
      acc2 = __builtin_amdgcn_mfma_f32_16x16x32_f16(__builtin_bit_cast(h8, hi[3]), bh, acc2, 0, 0, 0);
    }
    red[(w4 * 3 + 0) * 64 + lane] = acc0;
    red[(w4 * 3 + 1) * 64 + lane] = acc1;
    red[(w4 * 3 + 2) * 64 + lane] = acc2;
  }
  __syncthreads();
  if (w4 < 3) {
    f4 vv = red[(0 * 3 + w4) * 64 + lane] + red[(1 * 3 + w4) * 64 + lane] +
            (red[(2 * 3 + w4) * 64 + lane] + red[(3 * 3 + w4) * 64 + lane]);
#pragma unroll
    for (int r = 0; r < 4; ++r) kv[(16 * w4 + 4 * g + r) * 16 + idx] = vv[r] * (1.0f / 2048.0f);
  }
  if (t8 == 0) ((float*)(ws + WS_KSC))[d] = __uint_as_float((unsigned)(127 + ce - 14) << 23);
  __syncthreads();
  u4 hi, lo;
#pragma unroll
  for (int j = 0; j < 4; ++j) {
    if (KTERMS > 1) {
      unsigned hh, ll;
      split_h(*(const float*)(smem + kidx[2 * j]), *(const float*)(smem + kidx[2 * j + 1]), hh, ll);
      hi[j] = hh; lo[j] = ll;
    } else {
      hi[j] = pack_h(*(const float*)(smem + kidx[2 * j]), *(const float*)(smem + kidx[2 * j + 1]));
    }
  }
  st16(ws + WS_KH + ((size_t)d * SL + t8 * 8) * 2, hi);
  if (KTERMS > 1) st16(ws + WS_KL + ((size_t)d * SL + t8 * 8) * 2, lo);
}

constexpr int CV_U = 2064 * 2;
constexpr int CV_K = 2160 * 2;
constexpr int CV_OFF_K = 2 * CV_U;
constexpr int CV_OFF_Y = CV_OFF_K + 2 * KTERMS * CV_K;
constexpr int CV_Q = (SL + 4) * 4;
constexpr int CV_SMEM = CV_OFF_Y + 2 * 4 * CV_Q;

constexpr int CV_RPF = 2 + 2 * KTERMS;
constexpr int CV_R0 = (KTERMS == 2 ? 6 : 10) * CV_RPF;
constexpr int CV_NM = 68 * KTERMS;
struct CvStep { int a, n, k, first, op, arg, arg2; };
template <int SUB>
struct CvSched {
  CvStep s[CV_NM];
  int wt, wc, slack;
  constexpr CvSched() : s{}, wt(-1), wc(4), slack(1000) {
    const int AMAX = SUB == 0 ? 15 : 14;
    const int NR = (AMAX + 1) * CV_RPF;
    int nr = CV_R0, mi = 0;
    int endat[16] = {};
    for (int a = 0; a < 16; ++a) endat[a] = 1 << 20;
    for (int a = 0; a <= AMAX; ++a) {
      if ((((a + 1) >> 1) & 1) != SUB) continue;
      for (int nn = 0; nn <= a; ++nn) {
        const int n = (a == 0) ? 0 : (nn + 1 < a ? nn + 1 : (nn + 1 == a ? 0 : a));
        for (int k = 0; k < KTERMS; ++k) {
          CvStep st{a, n, k, (nn == 0 && k == 0) ? 1 : 0, 0, 0, 0};
          { const int need = ((n > a - n ? n : a - n) + 1) * CV_RPF; if (nr < NR && nr - need < slack) slack = nr - need; }
          ++mi;
          if (wc == 4) {
            for (int t = wt + 1; t < 16; ++t)
              if (endat[t] < (1 << 20)) { if (endat[t] + 8 <= mi) { wt = t; wc = 0; } break; }
          }
          if (wc < 4) { st.op = 2; st.arg = wt; st.arg2 = wc; ++wc; }
          else if (nr < NR) { st.op = 1; st.arg = nr; ++nr; }
          s[mi - 1] = st;
        }
      }
      endat[a] = mi;
    }
  }
};
template <int R>
__device__ __forceinline__ void cv_rd(s4 (&A)[16][KTERMS][2], s4 (&B)[16][2], const char* ub, const char* kb) {
  constexpr int i = R / CV_RPF, part = R % CV_RPF;
  if constexpr (part < 2) B[i][part] = tr_read(ub + 256 * i + 32 * part);
  else A[i][(part - 2) >> 1][part & 1] = tr_read(kb + ((part - 2) >> 1) * CV_K + 256 * i + 32 * (1 - (part & 1)));
}
template <int SUB, int I>
__device__ __forceinline__ void cv_step(s4 (&A)[16][KTERMS][2], s4 (&B)[16][2], f4 (&acc)[16], const char* ub, const char* kb, char* qb) {
  constexpr CvSched<SUB> S{};
  constexpr CvStep st = S.s[I];
  const s8 av = cat8(A[st.a - st.n][st.k][0], A[st.a - st.n][st.k][1]), bv = cat8(B[st.n][0], B[st.n][1]);
  if constexpr (st.first) asm volatile("v_mfma_f32_16x16x32_f16 %0, %1, %2, 0" : "=&v"(acc[st.a]) : "v"(av), "v"(bv));
  else asm volatile("v_mfma_f32_16x16x32_f16 %0, %1, %2, %0" : "+v"(acc[st.a]) : "v"(av), "v"(bv));
  if constexpr (st.op == 2) *(float*)(qb + st.arg2 * (CV_Q + 4) + st.arg * 512) = acc[st.arg][st.arg2];
  else if constexpr (st.op == 1) cv_rd<st.arg>(A, B, ub, kb);
  SCHED;
}
template <int SUB, int... Rs, int... Is>
__device__ __forceinline__ void conv_mfma_impl(const char* ub, const char* kb, char* qb, iseq<Rs...>, iseq<Is...>) {
  s4 A[16][KTERMS][2];
  s4 B[16][2];
  f4 acc[16];
  (cv_rd<Rs>(A, B, ub, kb), ...);
  SCHED;
  (cv_step<SUB, Is>(A, B, acc, ub, kb, qb), ...);
  asm volatile("s_nop 7");
  SCHED;
  constexpr CvSched<SUB> S{};
  static_assert(S.slack >= 4, "fragment read scheduled too late");
#pragma unroll
  for (int t = 0; t < 16; ++t) {
    if ((((t + 1) >> 1) & 1) != SUB) continue;
#pragma unroll
    for (int r = 0; r < 4; ++r)
      if (t > S.wt || (t == S.wt && r >= S.wc)) *(float*)(qb + r * (CV_Q + 4) + t * 512) = acc[t][r];
  }
}
template <int SUB>
__device__ __forceinline__ void conv_mfma(const char* ub, const char* kb, char* qb) {
  conv_mfma_impl<SUB>(ub, kb, qb, make_iseq<CV_R0>{}, make_iseq<CV_NM>{});
}

__global__ __launch_bounds__(512, 2) void conv_kernel(
    const u16* __restrict__ xT, const float* __restrict__ stats, const u16* __restrict__ Kh,
    const u16* __restrict__ Kl, const float* __restrict__ ksc, const u16* __restrict__ dskip,
    u16* __restrict__ outT) {
  extern __shared__ __attribute__((aligned(16))) char smem[];
  const int tid = threadIdx.x, lane = tid & 63;
  const int wid = __builtin_amdgcn_readfirstlane(tid >> 6);
  const int d0 = blockIdx.x * 2;
  const int t0 = tid * 4;
  u2 xr[2];
  u4 kr[KTERMS];
  f4 sr[2];
#pragma unroll
  for (int i = 0; i < 2; ++i) sr[i] = *(const f4*)(stats + t0 * 2 + i * 4);
#pragma unroll
  for (int c = 0; c < 2; ++c) xr[c] = *(const u2*)(xT + xoff(d0 + c, t0));
  kr[0] = *(const u4*)(Kh + (size_t)d0 * SL + tid * 8);
  if (KTERMS > 1) kr[KTERMS - 1] = *(const u4*)(Kl + (size_t)d0 * SL + tid * 8);
  const float2 isc2 = *(const float2*)(ksc + d0);
  const unsigned ds2 = *(const unsigned*)(dskip + d0);
  if (tid < 14 * 2 * KTERMS) {
    int which = tid / 14, j = tid % 14;
    *(u4*)(smem + CV_OFF_K + which * CV_K + j * 16) = u4{0, 0, 0, 0};
  } else if (tid >= 64 && tid < 70) {
    int c = (tid - 64) / 3, j = (tid - 64) % 3;
    *(u2*)(smem + c * CV_U + j * 8) = u2{0, 0};
  } else if (tid >= 128 && tid < 136) {
    *(f4*)(smem + CV_OFF_Y + (tid - 128) * CV_Q) = f4{0, 0, 0, 0};
  }
  float mb[4], ri[4];
#pragma unroll
  for (int j = 0; j < 4; ++j) ln_consts(sr[j >> 1][(j & 1) * 2], sr[j >> 1][(j & 1) * 2 + 1], mb[j], ri[j]);
  u2 ub16[2];
#pragma unroll
  for (int c = 0; c < 2; ++c) {
    unsigned d01 = pack_bf(bflo(xr[c][0]) - mb[0], bfhi(xr[c][0]) - mb[1]);
    unsigned d23 = pack_bf(bflo(xr[c][1]) - mb[2], bfhi(xr[c][1]) - mb[3]);
    ub16[c][0] = pack_bf(bflo(d01) * ri[0], bfhi(d01) * ri[1]);
    ub16[c][1] = pack_bf(bflo(d23) * ri[2], bfhi(d23) * ri[3]);
    u2 a = {pack_h(bflo(ub16[c][0]), bfhi(ub16[c][0])), pack_h(bflo(ub16[c][1]), bfhi(ub16[c][1]))};
    *(u2*)(smem + c * CV_U + 24 + tid * 8) = a;
  }
#pragma unroll
  for (int k = 0; k < KTERMS; ++k)
    *(u4*)(smem + CV_OFF_K + ((tid >> 8) * KTERMS + k) * CV_K + 224 + (tid & 255) * 16) = kr[k];
  __syncthreads();
  {
    const int ch = wid >> 2, par = (wid >> 1) & 1;
    const int g = lane >> 4, q = (lane >> 2) & 3, p = lane & 3;
    const char* ub = smem + ch * CV_U + 8 * (p + q + 8 * g);
    const char* kb = smem + CV_OFF_K + ch * KTERMS * CV_K + 8 * (31 + 16 * par + 4 * p - 8 * g - q) - 32;
    char* qb = smem + CV_OFF_Y + ch * 4 * CV_Q + par * 256 + lane * 4;
    if (wid & 1) conv_mfma<1>(ub, kb, qb);
    else conv_mfma<0>(ub, kb, qb);
  }
  __syncthreads();
#pragma unroll
  for (int c = 0; c < 2; ++c) {
    const char* Q = smem + CV_OFF_Y + c * 4 * CV_Q + t0 * 4;
    f4 y0 = (*(const f4*)(Q) + *(const f4*)(Q + CV_Q)) + (*(const f4*)(Q + 2 * CV_Q) + *(const f4*)(Q + 3 * CV_Q));
    float isc = c ? isc2.y : isc2.x;
    float ds = c ? bfhi(ds2) : bflo(ds2);
    unsigned y01 = pack_bf(y0[0] * isc, y0[1] * isc), y23 = pack_bf(y0[2] * isc, y0[3] * isc);
    unsigned s01 = pack_bf(bflo(ub16[c][0]) * ds, bfhi(ub16[c][0]) * ds), s23 = pack_bf(bflo(ub16[c][1]) * ds, bfhi(ub16[c][1]) * ds);
    unsigned a01 = pack_bf(bflo(y01) + bflo(s01), bfhi(y01) + bfhi(s01)), a23 = pack_bf(bflo(y23) + bflo(s23), bfhi(y23) + bfhi(s23));
    u2 ov = {pack_bf(bflo(xr[c][0]) + bflo(a01), bfhi(xr[c][0]) + bfhi(a01)), pack_bf(bflo(xr[c][1]) + bflo(a23), bfhi(xr[c][1]) + bfhi(a23))};
    st8(outT + xoff(d0 + c, t0), ov);
  }
}

constexpr int G1_SMEM = 512 * 256 + 512 * 8;

__device__ __forceinline__ void glds16(const void* gsrc, unsigned lds_dst) {
  unsigned keep;
  asm volatile("s_mov_b32 %0, m0\n\ts_mov_b32 m0, %2\n\ts_nop 0\n\tglobal_load_lds_dwordx4 %1, off\n\ts_mov_b32 m0, %0"
               : "=&s"(keep) : "v"(gsrc), "s"(lds_dst) : "memory");
}
template <int OFF>
__device__ __forceinline__ void gload16(u4& dst, const void* p) {
  asm volatile("global_load_dwordx4 %0, %1, off offset:%2" : "=v"(dst) : "v"(p), "i"(OFF) : "memory");
}
template <int N>
__device__ __forceinline__ void wait_only(u4& a) {
  asm volatile("s_waitcnt vmcnt(%1)" : "+v"(a) : "i"(N) : "memory");
}
template <int N>
__device__ __forceinline__ void wait_vm() { asm volatile("s_waitcnt vmcnt(%0)" :: "i"(N) : "memory"); }
__device__ __forceinline__ void add1(float& a, float b) { asm("v_add_f32 %0, %0, %1" : "+v"(a) : "v"(b)); }
__device__ __forceinline__ void sqa1(float& a, float b) { asm("v_fmac_f32 %0, %1, %1" : "+v"(a) : "v"(b)); }
__device__ __forceinline__ void bar() { asm volatile("s_waitcnt lgkmcnt(0)\n\ts_barrier" ::: "memory"); }
constexpr int G1_ST = 4 * 32768;
constexpr int F_BPRE = 5;
constexpr int G1_XPF = 3;
constexpr int G1_WPF = 8;

template <int OFF>
__device__ __forceinline__ void gload16_sc1(u4& dst, const void* p) {
  asm volatile("global_load_dwordx4 %0, %1, off offset:%2 sc1" : "=v"(dst) : "v"(p), "i"(OFF) : "memory");
}
template <int N>
__device__ __forceinline__ void wait4(u4 (&a)[4]) {
  asm volatile("s_waitcnt vmcnt(%4)" : "+v"(a[0]), "+v"(a[1]), "+v"(a[2]), "+v"(a[3]) : "i"(N) : "memory");
}

template <bool FINAL>
__global__ __launch_bounds__(512, 2) void ffn_kernel(
    const u16* __restrict__ xT, const u16* __restrict__ W1F, const float* __restrict__ c1,
    u16* HF, float* pst, const u16* __restrict__ W2F, unsigned* cnt,
    u16* __restrict__ out, float* __restrict__ stats) {
  extern __shared__ __attribute__((aligned(16))) char smem[];
  const int tid = threadIdx.x, lane = tid & 63;
  const int wid = __builtin_amdgcn_readfirstlane(tid >> 6);
  const int bx = blockIdx.x, xj = bx & 7, r = bx >> 3;
  const int tp = 4 * (xj >> 1) + (r & 3);
  const int fp = 7 * (xj & 1) + (r >> 2);
  if (r < 28) {
  const int h = lane >> 5, blk = (lane >> 4) & 1, q = (lane >> 2) & 3, p = lane & 3;
  const char* rbase = smem + (8 * h + q) * 64 + blk * 32 + 8 * p;
  float* st = (float*)(smem + G1_ST);
  if (wid >= 4) {
    const int hq = wid - 4;
    const char* xsrc = (const char*)xT + (size_t)tp * 131072 + hq * 1024 + lane * 16;
    const unsigned ldst = (unsigned)(size_t)smem + hq * 1024;
    auto issue_c = [&](int c) AINL {
#pragma unroll
      for (int i = 0; i < 3; ++i) glds16(xsrc + c * 4096 + i * 32768, ldst + c * 4096 + i * 32768);
    };
#pragma unroll
    for (int c = 0; c < G1_XPF; ++c) issue_c(c);
#pragma unroll 1
    for (int c = 0; c < 8 - G1_XPF; ++c) {
      wait_vm<3 * (G1_XPF - 1)>(); bar();
      issue_c(c + G1_XPF);
    }
    sfor<G1_XPF>([&](auto I) AINL {
      constexpr int i = decltype(I)::value;
      if constexpr (i == 0) wait_vm<3 * (G1_XPF - 1)>(); else wait_vm<3 * (G1_XPF - 1 - i) + 8>();
      bar();
      if constexpr (i == 0) {
#pragma unroll
        for (int k = 0; k < 8; ++k) glds16(xsrc + 3 * 32768 + k * 4096, ldst + 3 * 32768 + k * 4096);
      }
    });
    wait_vm<0>();
    bar();
#pragma unroll 1
    for (int it = 0; it < 2; ++it) {
      const int u = it == 0 ? 12 + hq : (fp - 1) * 4 + hq;
      if (it == 1 && (fp < 1 || fp > 3)) break;
      const char* rq = rbase + u * 8192;
      s4 fr[8][2];
      sfor<8>([&](auto I) AINL {
        constexpr int i = decltype(I)::value;
        fr[i][0] = tr_read(rq + i * 1024);
        fr[i][1] = tr_read(rq + i * 1024 + 256);
      });
      float sa = 0.f, sb = 0.f, qa = 0.f, qb = 0.f;
      sfor<8>([&](auto I) AINL {
        constexpr int i = decltype(I)::value;
        u4 xv = __builtin_bit_cast(u4, cat8(fr[i][0], fr[i][1]));
#pragma unroll
        for (int j = 0; j < 4; ++j) {
          float lo = bflo(xv[j]), hi = bfhi(xv[j]);
          add1(sa, lo); add1(sb, hi); sqa1(qa, lo); sqa1(qb, hi);
        }
      });
      float s0 = sa + sb, ss0 = qa + qb;
      s0 += __shfl_xor(s0, 32);
      ss0 += __shfl_xor(ss0, 32);
      if (it == 0) {
        if (lane < 32) st[128 + hq * 32 + lane] = s0;
        bar();
      }
      if (lane < 32 && (it == 1 || fp == 0))
        st8(pst + ((tp * 128 + (u >> 2) * 32 + lane) * 4 + (u & 3)) * 2, u2{__float_as_uint(s0), __float_as_uint(ss0)});
    }
  } else {
  const int ft = fp * 4 + wid;
  u4 wf[32];
  u4 cfu[4];
  const char* wsrc = (const char*)W1F + ((size_t)ft * 32 * 64 + lane) * 16;
  auto issue_w = [&](auto S) AINL {
    constexpr int s = decltype(S)::value;
    gload16<(s & 3) * 1024>(wf[s], wsrc + (s >> 2) * 4096);
  };
  sfor<G1_WPF>(issue_w);
  f16v acc[4], asum;
  s4 xa[3][3][2];
  constexpr int XD = 4, XR = XD + 1;
  s4 xc[XR][2];
  u4 ov;
  float nm[4];
  const int w3 = wid == 3 ? 0 : wid;
  int tto[3];
#pragma unroll
  for (int i = 0; i < 3; ++i) { int tt = i + w3; tto[i] = tt >= 3 ? tt - 3 : tt; }
  const char* rb[3];
#pragma unroll
  for (int i = 0; i < 3; ++i) rb[i] = rbase + tto[i] * 32768;
  u16* dst = HF + ((size_t)((tp * 8 + blk) * 56 + ft) * 64 + (lane & 15) + 16 * h) * 8;
  auto epi_pair = [&](const f16v& a, float m, int e) AINL -> unsigned {
    f4 cf = __builtin_bit_cast(f4, cfu[e >> 2]);
    unsigned pu = pack_bf(fmaf(cf[e & 3], m, a[e]), fmaf(cf[(e & 3) + 1], m, a[e + 1]));
    asm("" : "+v"(pu));
    return __builtin_bit_cast(unsigned, __builtin_elementwise_max(__builtin_bit_cast(s2v, pu), s2v{0, 0}));
  };
  f16v z;
#pragma unroll
  for (int j = 0; j < 16; ++j) z[j] = 0.f;
  const s8 ones8 = {0x3f80, 0x3f80, 0x3f80, 0x3f80, 0x3f80, 0x3f80, 0x3f80, 0x3f80};
  SCHED;
  asm volatile("s_barrier" ::: "memory");
#pragma unroll
  for (int k = 0; k < 2; ++k)
#pragma unroll
    for (int i = 0; i < 3; ++i) { xa[k][i][0] = tr_read(rb[i] + k * 1024); xa[k][i][1] = tr_read(rb[i] + k * 1024 + 256); }
  SCHED;
  sfor<32>([&](auto S) AINL {
    constexpr int s = decltype(S)::value;
    constexpr int issued = (G1_WPF + s <= 32) ? G1_WPF + s : 36;
    asum = __builtin_amdgcn_mfma_f32_32x32x16_bf16(__builtin_bit_cast(b8, ones8), __builtin_bit_cast(b8, cat8(xa[s % 3][0][0], xa[s % 3][0][1])), s == 0 ? z : asum, 0, 0, 0);
    wait_only<issued - s - 1>(wf[s]);
    if constexpr ((s & 3) == 2 && s < 30) asm volatile("s_barrier" ::: "memory");
    SCHED;
    sfor<3>([&](auto I) AINL {
      constexpr int i = decltype(I)::value;
      const b8 xv = __builtin_bit_cast(b8, cat8(xa[s % 3][i][0], xa[s % 3][i][1]));
      acc[i] = __builtin_amdgcn_mfma_f32_32x32x16_bf16(__builtin_bit_cast(b8, wf[s]), xv, s == 0 ? z : acc[i], 0, 0, 0);
      if constexpr (s + 2 < 32) {
        xa[(s + 2) % 3][i][0] = tr_read(rb[i] + (s + 2) * 1024);
        xa[(s + 2) % 3][i][1] = tr_read(rb[i] + (s + 2) * 1024 + 256);
      }
      if constexpr (i == 0) {
        if constexpr (G1_WPF + s < 32) issue_w(ic<(G1_WPF + s < 32) ? G1_WPF + s : 0>{});
        if constexpr (G1_WPF + s == 32) {
          gload16<0>(cfu[0], c1 + ft * 32 + 4 * h);
          gload16<32>(cfu[1], c1 + ft * 32 + 4 * h);
          gload16<64>(cfu[2], c1 + ft * 32 + 4 * h);
          gload16<96>(cfu[3], c1 + ft * 32 + 4 * h);
        }
      }
      SCHED;
    });
  });
  if (lane < 32) st[wid * 32 + lane] = -rbf(asum[0] * (1.0f / DM));
  asm volatile("s_waitcnt vmcnt(0) lgkmcnt(0)\n\ts_barrier" : "+v"(cfu[0]), "+v"(cfu[1]), "+v"(cfu[2]), "+v"(cfu[3]) :: "memory");
  {
    unsigned rbo = (unsigned)(rbase - smem) + 3 * 32768;
    asm volatile("" : "+v"(rbo));
    const char* rbb = smem + rbo;
    sfor<XD>([&](auto I) AINL {
      constexpr int i = decltype(I)::value;
      xc[i][0] = tr_read(rbb + i * 1024); xc[i][1] = tr_read(rbb + i * 1024 + 256);
    });
#pragma unroll
    for (int i = 0; i < 3; ++i) nm[i] = st[tto[i] * 32 + (lane & 31)];
    SCHED;
    sfor<32>([&](auto S) AINL {
      constexpr int s = decltype(S)::value;
      const b8 xv = __builtin_bit_cast(b8, cat8(xc[s % XR][0], xc[s % XR][1]));
      acc[3] = __builtin_amdgcn_mfma_f32_32x32x16_bf16(__builtin_bit_cast(b8, wf[s]), xv, s == 0 ? z : acc[3], 0, 0, 0);
      if constexpr (s + XD < 32) {
        xc[(s + XD) % XR][0] = tr_read(rbb + (s + XD) * 1024);
        xc[(s + XD) % XR][1] = tr_read(rbb + (s + XD) * 1024 + 256);
      }
      if constexpr (s < 24) {
        constexpr int i = s >> 3, pr = s & 7;
        ov[pr & 3] = epi_pair(acc[i], nm[i], 2 * pr);
        if constexpr ((pr & 3) == 3) st16(dst + tto[i] * (2 * 56 * 512) + (pr >> 2) * 256, ov);
      }
      SCHED;
    });
  }
  asm volatile("s_barrier" ::: "memory");
  {
    const float* sp = st + 128 + (lane & 31);
    nm[3] = -rbf(((sp[0] + sp[32]) + (sp[64] + sp[96])) * (1.0f / DM));
  }
#pragma unroll
  for (int c = 0; c < 2; ++c) {
#pragma unroll
    for (int j = 0; j < 4; ++j) ov[j] = epi_pair(acc[3], nm[3], 8 * c + 2 * j);
    st16(dst + 3 * (2 * 56 * 512) + c * 256, ov);
  }
  }
  }
  const int tb = 8 * (xj >> 1) + (r & 7), db = 4 * (xj & 1) + (r >> 3);
  const int g = lane >> 4, n = lane & 15;
  const int mo = wid >> 1, no = 2 * (wid & 1);
  u4 bf[7][4], af[7][4];
  {
    const char* bp[4];
#pragma unroll
    for (int j = 0; j < 4; ++j) bp[j] = (const char*)W2F + ((size_t)((db * 4 + ((j + no) & 3)) * 56 + wid * 7) * 64 + lane) * 16;
    auto issue_b = [&](auto KS) AINL {
      constexpr int ks = decltype(KS)::value;
      sfor<4>([&](auto J) AINL {
        constexpr int j = decltype(J)::value;
        gload16<(ks & 3) * 1024>(bf[ks][j], bp[j] + (ks >> 2) * 4096);
      });
    };
    wait_vm<0>();
    bar();
    if (r < 28 && tid == 0) __hip_atomic_fetch_add(cnt + tp * 32, 1u, __ATOMIC_RELAXED, __HIP_MEMORY_SCOPE_AGENT);
    if (wid == 7) {
      const unsigned* cp = cnt + (tb >> 1) * 32;
      for (int spin = 0; spin < (1 << 22); ++spin) {
        if (__hip_atomic_load(cp, __ATOMIC_RELAXED, __HIP_MEMORY_SCOPE_AGENT) >= 14u) break;
        __builtin_amdgcn_s_sleep(2);
      }
    }
    sfor<F_BPRE>(issue_b);
    bar();
    sfor<7 - F_BPRE>([&](auto I) AINL { issue_b(ic<F_BPRE + decltype(I)::value>{}); });
  }
  u2 res[2];
  u16 resf[2][4];
  if (!FINAL) {
#pragma unroll
    for (int j = 0; j < 2; ++j) res[j] = *(const u2*)(xT + xoff(db * 64 + (no + j) * 16 + n, tb * 64 + mo * 16 + 4 * g));
  } else {
#pragma unroll
    for (int j = 0; j < 2; ++j)
#pragma unroll
      for (int e = 0; e < 4; ++e) resf[j][e] = xT[xoff(db * 64 + (no + j) * 16 + 4 * g + e, tb * 64 + mo * 16 + n)];
  }
  const unsigned long long psu = __hip_atomic_load((const unsigned long long*)(pst + ((tb * 64 + mo * 16 + n) * 4 + g) * 2), __ATOMIC_RELAXED, __HIP_MEMORY_SCOPE_AGENT);
  const char* ap[4];
#pragma unroll
  for (int i = 0; i < 4; ++i) ap[i] = (const char*)HF + ((size_t)((tb * 4 + ((i + mo) & 3)) * 56 + wid * 7) * 64 + lane) * 16;
  auto issue_a = [&](auto KS) AINL {
    constexpr int ks = decltype(KS)::value;
    sfor<4>([&](auto I) AINL {
      constexpr int i = decltype(I)::value;
      gload16_sc1<(ks & 3) * 1024>(af[ks][i], ap[i] + (ks >> 2) * 4096);
    });
  };
  sfor<4>(issue_a);
  f4 acc[4][4];
#pragma unroll
  for (int i = 0; i < 4; ++i)
#pragma unroll
    for (int j = 0; j < 4; ++j) acc[i][j] = f4{0, 0, 0, 0};
  SCHED;
  sfor<7>([&](auto KS) AINL {
    constexpr int ks = decltype(KS)::value;
    wait4<(ks < 4 ? 12 : 4 * (6 - ks))>(af[ks]);
    wait4<63>(bf[ks]);
    SCHED;
#pragma unroll
    for (int i = 0; i < 4; ++i)
#pragma unroll
      for (int j = 0; j < 4; ++j) {
        if (!FINAL) acc[i][j] = __builtin_amdgcn_mfma_f32_16x16x32_bf16(__builtin_bit_cast(b8, af[ks][i]), __builtin_bit_cast(b8, bf[ks][j]), acc[i][j], 0, 0, 0);
        else acc[i][j] = __builtin_amdgcn_mfma_f32_16x16x32_bf16(__builtin_bit_cast(b8, bf[ks][j]), __builtin_bit_cast(b8, af[ks][i]), acc[i][j], 0, 0, 0);
      }
    SCHED;
    if constexpr (ks + 4 < 7) issue_a(ic<(ks + 4 < 7) ? ks + 4 : 0>{});
    SCHED;
  });
  f4* red = (f4*)smem;
#pragma unroll
  for (int i = 0; i < 4; ++i)
#pragma unroll
    for (int jp = 0; jp < 2; ++jp) {
      if (i == 0 && jp == 0) continue;
      const int owner = ((i + mo) & 3) * 2 + ((jp + wid) & 1);
      red[((owner * 7 + i * 2 + jp - 1) * 2 + 0) * 64 + lane] = acc[i][2 * jp];
      red[((owner * 7 + i * 2 + jp - 1) * 2 + 1) * 64 + lane] = acc[i][2 * jp + 1];
    }
  f4 rr;
  {
    float sx = __uint_as_float((unsigned)psu), sy = __uint_as_float((unsigned)(psu >> 32));
    sx += __shfl_xor(sx, 16); sy += __shfl_xor(sy, 16);
    sx += __shfl_xor(sx, 32); sy += __shfl_xor(sy, 32);
    float mb, ri;
    ln_consts(sx, sy, mb, ri);
    if (FINAL) rr = f4{ri, ri, ri, ri};
    else {
#pragma unroll
      for (int e = 0; e < 4; ++e) rr[e] = __shfl(ri, 4 * g + e);
    }
  }
  bar();
  float s[4] = {0, 0, 0, 0}, ss[4] = {0, 0, 0, 0};
#pragma unroll
  for (int j = 0; j < 2; ++j) {
    const f4* rp = red + (wid * 14 + j) * 64 + lane;
    f4 v = ((acc[0][j] + rp[0]) + (rp[128] + rp[256])) + ((rp[384] + rp[512]) + (rp[640] + rp[768]));
    v *= rr;
    unsigned m01 = pack_bf(v[0], v[1]), m23 = pack_bf(v[2], v[3]);
    u2 ov;
    if (!FINAL) {
      ov[0] = pack_bf(bflo(res[j][0]) + bflo(m01), bfhi(res[j][0]) + bfhi(m01));
      ov[1] = pack_bf(bflo(res[j][1]) + bflo(m23), bfhi(res[j][1]) + bfhi(m23));
      *(u2*)(out + xoff(db * 64 + (no + j) * 16 + n, tb * 64 + mo * 16 + 4 * g)) = ov;
      float o[4] = {bflo(ov[0]), bfhi(ov[0]), bflo(ov[1]), bfhi(ov[1])};
#pragma unroll
      for (int e = 0; e < 4; ++e) { s[e] += o[e]; ss[e] = fmaf(o[e], o[e], ss[e]); }
    } else {
      ov[0] = pack_bf(bf2f(resf[j][0]) + bflo(m01), bf2f(resf[j][1]) + bfhi(m01));
      ov[1] = pack_bf(bf2f(resf[j][2]) + bflo(m23), bf2f(resf[j][3]) + bfhi(m23));
      *(u2*)(out + (size_t)(tb * 64 + mo * 16 + n) * DM + db * 64 + (no + j) * 16 + 4 * g) = ov;
    }
  }
  if (!FINAL) {
#pragma unroll
    for (int e = 0; e < 4; ++e) { s[e] = row_sum16(s[e]); ss[e] = row_sum16(ss[e]); }
    if (n < 8) {
      int e = n >> 1;
      float v = (n & 1) ? (e == 0 ? ss[0] : e == 1 ? ss[1] : e == 2 ? ss[2] : ss[3]) : (e == 0 ? s[0] : e == 1 ? s[1] : e == 2 ? s[2] : s[3]);
      atomicAdd(stats + (tb * 64 + mo * 16 + 4 * g) * 2 + n, v);
    }
  }
}

extern "C" void kernel_launch(void* const* d_in, const int* in_sizes, int n_in,
                              void* d_out, int out_size, void* d_ws, size_t ws_size,
                              hipStream_t stream) {
  const u16* x = (const u16*)d_in[0];
  const u16* logA = (const u16*)d_in[1];
  const u16* Aim = (const u16*)d_in[2];
  const u16* Cre = (const u16*)d_in[3];
  const u16* Cim = (const u16*)d_in[4];
  const u16* logdt = (const u16*)d_in[5];
  const u16* dskip = (const u16*)d_in[6];
  const u16* w1 = (const u16*)d_in[8];
  const u16* w2 = (const u16*)d_in[9];
  char* ws = (char*)d_ws;
  u16* out = (u16*)d_out;
  (void)hipFuncSetAttribute((const void*)ffn_kernel<false>, hipFuncAttributeMaxDynamicSharedMemorySize, G1_SMEM);
  (void)hipFuncSetAttribute((const void*)ffn_kernel<true>, hipFuncAttributeMaxDynamicSharedMemorySize, G1_SMEM);
  (void)hipFuncSetAttribute((const void*)conv_kernel, hipFuncAttributeMaxDynamicSharedMemorySize, CV_SMEM);

  prep_kernel<<<256 + PREP_W1 + PREP_W2 + PREP_TR, 512, 0, stream>>>(x, logA, Aim, Cre, Cim, logdt, w1, w2, ws);
  conv_kernel<<<256, 512, CV_SMEM, stream>>>((const u16*)(ws + WS_XT0), (const float*)(ws + WS_ST0), (const u16*)(ws + WS_KH),
                                             (const u16*)(ws + WS_KL), (const float*)(ws + WS_KSC), dskip, (u16*)(ws + WS_XT1));
  ffn_kernel<false><<<256, 512, G1_SMEM, stream>>>((const u16*)(ws + WS_XT1), (const u16*)(ws + WS_W1F), (const float*)(ws + WS_C1),
                                                   (u16*)(ws + WS_HF), (float*)(ws + WS_PST), (const u16*)(ws + WS_W2F),
                                                   (unsigned*)(ws + WS_CNT), (u16*)(ws + WS_XT2), (float*)(ws + WS_ST2));
  conv_kernel<<<256, 512, CV_SMEM, stream>>>((const u16*)(ws + WS_XT2), (const float*)(ws + WS_ST2), (const u16*)(ws + WS_KH),
                                             (const u16*)(ws + WS_KL), (const float*)(ws + WS_KSC), dskip, (u16*)(ws + WS_XT3));
  ffn_kernel<true><<<256, 512, G1_SMEM, stream>>>((const u16*)(ws + WS_XT3), (const u16*)(ws + WS_W1F), (const float*)(ws + WS_C1),
                                                  (u16*)(ws + WS_HF2), (float*)(ws + WS_PST2), (const u16*)(ws + WS_W2F),
                                                  (unsigned*)(ws + WS_CNT) + 512, out, nullptr);
}
```

```cpp
#include <hip/hip_runtime.h>
#include <hip/hip_bf16.h>

typedef unsigned short u16;
typedef short s4 __attribute__((ext_vector_type(4)));
typedef short s2v __attribute__((ext_vector_type(2)));
typedef short s8 __attribute__((ext_vector_type(8)));
typedef _Float16 h8 __attribute__((ext_vector_type(8)));
typedef _Float16 h2 __attribute__((ext_vector_type(2)));
typedef __bf16 b2 __attribute__((ext_vector_type(2)));
typedef __bf16 b8 __attribute__((ext_vector_type(8)));
typedef float f4 __attribute__((ext_vector_type(4)));
typedef float f2 __attribute__((ext_vector_type(2)));
typedef double d2 __attribute__((ext_vector_type(2)));
typedef __bf16 b4 __attribute__((ext_vector_type(4)));
typedef float f16v __attribute__((ext_vector_type(16)));
typedef unsigned u4 __attribute__((ext_vector_type(4)));
typedef unsigned u2 __attribute__((ext_vector_type(2)));
typedef __attribute__((address_space(3))) s4* lds_s4p;

#define SCHED __builtin_amdgcn_sched_barrier(0)
template <int... Is> struct iseq {};
template <int N, int... Is> struct mk_iseq : mk_iseq<N - 1, N - 1, Is...> {};
template <int... Is> struct mk_iseq<0, Is...> { typedef iseq<Is...> type; };
template <int N> using make_iseq = typename mk_iseq<N>::type;
template <int I> struct ic { static constexpr int value = I; };
template <class F, int... Is> __device__ __forceinline__ void sfor_impl(F&& f, iseq<Is...>) { (f(ic<Is>{}), ...); }
template <int N, class F> __device__ __forceinline__ void sfor(F&& f) { sfor_impl(f, make_iseq<N>{}); }
#define AINL __attribute__((always_inline))

constexpr int SL = 2048;
constexpr int DM = 512;
constexpr int NS = 128;
constexpr int FF = 1792;
#define KTERMS 1

constexpr size_t WS_XT0 = 0;
constexpr size_t WS_XT1 = WS_XT0 + (size_t)SL * DM * 2;
constexpr size_t WS_XT2 = WS_XT1 + (size_t)SL * DM * 2;
constexpr size_t WS_XT3 = WS_XT2 + (size_t)SL * DM * 2;
constexpr size_t WS_KH  = WS_XT3 + (size_t)SL * DM * 2;
constexpr size_t WS_KL  = WS_KH + (size_t)SL * DM * 2;
constexpr size_t WS_W1F = WS_KL + (size_t)SL * DM * 2;
constexpr size_t WS_W2F = WS_W1F + (size_t)DM * FF * 2;
constexpr size_t WS_HF  = WS_W2F + (size_t)DM * FF * 2;
constexpr size_t WS_HF2 = WS_HF + (size_t)SL * FF * 2;
constexpr size_t WS_ST0 = WS_HF2 + (size_t)SL * FF * 2;
constexpr size_t WS_ST2 = WS_ST0 + (size_t)SL * 16;
constexpr size_t WS_C1  = WS_ST2 + (size_t)SL * 16;
constexpr size_t WS_KSC = WS_C1 + (size_t)FF * 4;
constexpr size_t WS_PST = WS_KSC + (size_t)DM * 4;
constexpr size_t WS_PST2 = WS_PST + (size_t)SL * 32;
constexpr size_t WS_CNT = WS_PST2 + (size_t)SL * 32;

__host__ __device__ __forceinline__ int xoff(int d, int t) { return (t >> 5) * 16384 + d * 32 + (t & 31); }
__device__ __forceinline__ float bf2f(unsigned h) { return __uint_as_float(h << 16); }
__device__ __forceinline__ float bflo(unsigned w) { return __uint_as_float(w << 16); }
__device__ __forceinline__ float bfhi(unsigned w) { return __uint_as_float(w & 0xffff0000u); }
__device__ __forceinline__ unsigned pack_bf(float a, float b) {
  b2 v = {(__bf16)a, (__bf16)b};
  return __builtin_bit_cast(unsigned, v);
}
__device__ __forceinline__ float rbf(float f) {
  return __uint_as_float(((unsigned)__builtin_bit_cast(u16, (__bf16)f)) << 16);
}
__device__ __forceinline__ unsigned pack_h(float a, float b) {
  h2 v = {(_Float16)a, (_Float16)b};
  return __builtin_bit_cast(unsigned, v);
}
__device__ __forceinline__ float dot2(unsigned a, unsigned b, float c) {
  return __builtin_amdgcn_fdot2_f32_bf16(__builtin_bit_cast(b2, a), __builtin_bit_cast(b2, b), c, false);
}
__device__ __forceinline__ s4 tr_read(const char* p) {
  return __builtin_amdgcn_ds_read_tr16_b64_v4i16((lds_s4p)p);
}
__device__ __forceinline__ s8 cat8(s4 a, s4 b) { return __builtin_shufflevector(a, b, 0, 1, 2, 3, 4, 5, 6, 7); }
template <int CTRL>
__device__ __forceinline__ float dppf(float v) {
  return __builtin_bit_cast(float, __builtin_amdgcn_update_dpp(0, __builtin_bit_cast(int, v), CTRL, 0xf, 0xf, true));
}
__device__ __forceinline__ float shr1(float v) { return dppf<0x138>(v); }
__device__ __forceinline__ float row_sum16(float v) {
  v += dppf<0x128>(v);
  v += dppf<0x124>(v);
  v += dppf<0x122>(v);
  v += dppf<0x121>(v);
  return v;
}
constexpr unsigned BF_ONES = 0x3f803f80u;
__device__ __forceinline__ void st16(void* p, u4 v) { asm volatile("global_store_dwordx4 %0, %1, off sc1\n\ts_nop 1" :: "v"(p), "v"(v) : "memory"); }
__device__ __forceinline__ void st8(void* p, u2 v) { asm volatile("global_store_dwordx2 %0, %1, off sc1\n\ts_nop 0" :: "v"(p), "v"(v) : "memory"); }

__device__ __forceinline__ void ln_consts(float s, float ss, float& mb, float& rinv) {
  float mean = s * (1.0f / DM);
  float var = fmaxf(ss * (1.0f / DM) - mean * mean, 0.0f);
  mb = rbf(mean);
  float sd = rbf(__builtin_amdgcn_sqrtf(rbf(rbf(var) + 1e-5f)));
  rinv = __builtin_amdgcn_rcpf(sd);
}

constexpr int PREP_W1 = 56, PREP_W2 = 56, PREP_TR = 64;
constexpr int PK_PAR = 0, PK_PARC = 2048, PK_RED = 3072, PK_KV = PK_RED + 12288, PK_MX = PK_KV + 3072, PK_SZ = PK_MX + 128;
constexpr int PREP_SMEM = (32768 + 1024 > 2 * PK_SZ) ? 32768 + 1024 : 2 * PK_SZ;

__device__ __forceinline__ int alpha_of(int idx) {
  if (idx < 16) return (idx >> 3) * 128 + (idx & 7);
  if (idx < 24) { int j = idx - 16; return (2 + (j >> 2)) * 128 + (j & 3) * 2; }
  if (idx < 32) { int j = idx - 24; return (4 + (j >> 1)) * 128 + (j & 1) * 4; }
  if (idx < 41) return (8 + idx - 32) * 128;
  return 0;
}
__device__ __forceinline__ float exp_acc(float x) {
  const float L2EH = 1.44269504f, L2EL = (float)(1.4426950408889634 - (double)1.44269504f);
  float t = x * L2EH;
  float e = fmaf(x, L2EH, -t) + x * L2EL;
  float r = __builtin_amdgcn_exp2f(t);
  return fmaf(r, e * 0.69314718f, r);
}
__device__ __forceinline__ float row_max16(float v) {
  v = fmaxf(v, dppf<0x128>(v));
  v = fmaxf(v, dppf<0x124>(v));
  v = fmaxf(v, dppf<0x122>(v));
  v = fmaxf(v, dppf<0x121>(v));
  return v;
}
__device__ __forceinline__ void split_h(float a, float b, unsigned& hi, unsigned& lo) {
  float ah = __uint_as_float(__float_as_uint(a) & 0xffffe000u), bh = __uint_as_float(__float_as_uint(b) & 0xffffe000u);
  hi = pack_h(ah, bh);
  lo = pack_h(a - ah, b - bh);
}

__global__ __launch_bounds__(512, 4) void prep_kernel(
    const u16* __restrict__ x, const u16* __restrict__ logA, const u16* __restrict__ Aim,
    const u16* __restrict__ Cre, const u16* __restrict__ Cim, const u16* __restrict__ logdt,
    const u16* __restrict__ w1, const u16* __restrict__ w2, char* __restrict__ ws) {
  __shared__ __attribute__((aligned(16))) char smem[PREP_SMEM];
  const int tid = threadIdx.x, lane = tid & 63;
  const int wid = __builtin_amdgcn_readfirstlane(tid >> 6);
  const int b = blockIdx.x;
  const int idx = lane & 15, g = lane >> 4;
  if (b >= 256) {
    const int bj = b - 256;
    const int job = bj < PREP_W1 ? 0 : (bj < PREP_W1 + PREP_W2 ? 1 : 2);
    const int jb = job == 0 ? bj : (job == 1 ? bj - PREP_W1 : bj - PREP_W1 - PREP_W2);
    u4 v[4];
    int lofs;
    int ls1, ls2;
    {
      const u16* src;
      int gs1, gs2;
      if (job == 0) {
        src = w1 + (size_t)(tid >> 2) * FF + jb * 32 + (tid & 3) * 8;
        gs1 = 128 * FF; gs2 = 256 * FF; lofs = tid * 16; ls1 = 8192; ls2 = 16384;
      } else if (job == 1) {
        src = w2 + (size_t)((jb >> 2) * 128 + (tid >> 4)) * DM + (jb & 3) * 128 + (tid & 15) * 8;
        gs1 = 32 * DM; gs2 = 64 * DM; lofs = (tid >> 4) * 256 + (((tid & 15) ^ (((tid >> 4) & 7) << 1)) * 16); ls1 = 8192; ls2 = 16384;
      } else {
        const int r0 = (wid >> 2) * 8 + (wid & 3);
        src = x + (size_t)(jb * 32 + r0) * DM + lane * 8;
        gs1 = 4 * DM; gs2 = 16 * DM; lofs = r0 * 1024 + ((lane ^ (wid << 1)) * 16); ls1 = 4096; ls2 = 16384;
      }
      v[0] = *(const u4*)(src); v[1] = *(const u4*)(src + gs1); v[2] = *(const u4*)(src + gs2); v[3] = *(const u4*)(src + gs1 + gs2);
    }
    *(u4*)(smem + lofs) = v[0];
    *(u4*)(smem + lofs + ls1) = v[1];
    *(u4*)(smem + lofs + ls2) = v[2];
    *(u4*)(smem + lofs + ls1 + ls2) = v[3];
    __syncthreads();
    const int q = (lane >> 2) & 3, p = lane & 3;
    float* csr = (float*)(smem + 32768);
    if (job == 0) {
      const int h = lane >> 5, blk = (lane >> 4) & 1;
      const char* rb = smem + (wid * 64 + 8 * h + q) * 64 + blk * 32 + p * 8;
      u16* W1F = (u16*)(ws + WS_W1F);
      float cs = 0.f;
#pragma unroll
      for (int i = 0; i < 4; ++i) {
        s8 o = cat8(tr_read(rb + i * 1024), tr_read(rb + i * 1024 + 256));
        u4 ou = __builtin_bit_cast(u4, o);
#pragma unroll
        for (int j = 0; j < 4; ++j) cs = dot2(ou[j], BF_ONES, cs);
        st16(W1F + ((size_t)(jb * 32 + wid * 4 + i) * 64 + lane) * 8, ou);
      }
      cs += __shfl_xor(cs, 32);
      if (lane < 32) csr[wid * 32 + lane] = cs;
    } else if (job == 1) {
      const int rl = 16 * (g >> 1) + 4 * (g & 1) + q;
      const int sw = rl & 7;
      u16* W2F = (u16*)(ws + WS_W2F);
#pragma unroll
      for (int i = 0; i < 4; ++i) {
        const int nt = (wid & 1) * 4 + i;
        const char* rb = smem + ((wid >> 1) * 32 + rl) * 256 + ((nt ^ sw) * 32) + p * 8;
        s8 o = cat8(tr_read(rb), tr_read(rb + 2048));
        st16(W2F + ((size_t)(((jb & 3) * 8 + nt) * 56 + (jb >> 2) * 4 + (wid >> 1)) * 64 + lane) * 8, __builtin_bit_cast(u4, o));
      }
    } else if (job == 2) {
      const int sw = q | ((g & 1) << 2);
      u16* xT = (u16*)(ws + WS_XT0);
#pragma unroll
      for (int i = 0; i < 4; ++i) {
        int dt_ = wid * 4 + i;
        const char* base = smem + (8 * g + q) * 1024 + ((dt_ ^ sw) * 32) + 8 * p;
        s8 o = cat8(tr_read(base), tr_read(base + 4096));
        *(s8*)(xT + xoff(dt_ * 16 + idx, jb * 32 + 8 * g)) = o;
      }
      float sv[4], ssv[4];
#pragma unroll
      for (int i = 0; i < 4; ++i) {
        float s = 0.f, ss = 0.f;
#pragma unroll
        for (int j = 0; j < 4; ++j) { s = dot2(v[i][j], BF_ONES, s); ss = dot2(v[i][j], v[i][j], ss); }
        sv[i] = row_sum16(s); ssv[i] = row_sum16(ss);
      }
      float val = sv[0];
#pragma unroll
      for (int i = 1; i < 4; ++i) val = (idx == 2 * i) ? sv[i] : val;
#pragma unroll
      for (int i = 0; i < 4; ++i) val = (idx == 2 * i + 1) ? ssv[i] : val;
      val += __shfl_xor(val, 16);
      val += __shfl_xor(val, 32);
      if (lane < 8) {
        const int i = lane >> 1;
        const int r = jb * 32 + (i >> 1) * 16 + (wid >> 2) * 8 + (i & 1) * 4 + (wid & 3);
        ((double*)(ws + WS_ST0))[r * 2 + (lane & 1)] = (double)val;
        ((double*)(ws + WS_ST2))[r * 2 + (lane & 1)] = 0.0;
      }
    }

    if (job == 0) {
      __syncthreads();
      if (tid < 32) {
        float cs = 0.f;
#pragma unroll
        for (int i = 0; i < 8; ++i) cs += csr[i * 32 + tid];
        ((float*)(ws + WS_C1))[jb * 32 + tid] = cs;
      }
    }
    if (bj == 0 && tid >= 64 && tid < 96) ((unsigned*)(ws + WS_CNT))[(tid - 64) * 32] = 0u;
    return;
  }
  const int ch = wid >> 2, w4 = wid & 3, t8 = tid & 255;
  const int d = b * 2 + ch;
  const int pn = d * NS + (t8 & (NS - 1));
  const u16 pl0 = logdt[d], pl1 = logA[pn], pl2 = Aim[pn], pl3 = Cre[pn], pl4 = Cim[pn];
  SCHED;
  int kidx[8];
#pragma unroll
  for (int j = 0; j < 8; ++j) {
    float pf = rbf((float)(t8 * 8 + j));
    int p = (int)pf;
    int e = (int)(__float_as_uint(pf) >> 23) - (127 + 7);
    e = e < 0 ? 0 : (e > 3 ? 3 : e);
    int ai_ = ((((p >> 7) << 3) + (p & 7)) >> e) + 8 * e;
    kidx[j] = ch * PK_SZ + PK_KV + (ai_ * 16 + ((p & 127) >> 3)) * 4;
    asm volatile("" : "+v"(kidx[j]));
  }
  char* ks = smem + ch * PK_SZ;
  f4* par = (f4*)(ks + PK_PAR);
  f2* parc = (f2*)(ks + PK_PARC);
  f4* red = (f4*)(ks + PK_RED);
  float* kv = (float*)(ks + PK_KV);
  float* mc = (float*)(ks + PK_MX);
  SCHED;
  {
    float dt = rbf(exp_acc(bf2f(pl0)));
    float Ar = -rbf(exp_acc(bf2f(pl1)));
    float ar = Ar * dt;
    float ai = bf2f(pl2) * dt;
    const float I2PH = 0.15915494f;
    const float I2PL = (float)(0.15915494309189535 - (double)0.15915494f);
    float fh = ai * I2PH;
    float fl = fmaf(ai, I2PH, -fh) + ai * I2PL;
    fh -= rintf(fh);
    float fhi = __uint_as_float(__float_as_uint(fh) & 0xfffff000u);
    float flo = (fh - fhi) + fl;
    float crv = 2.0f * bf2f(pl3) * dt;
    float civ = 2.0f * bf2f(pl4) * dt;
    float cm = row_sum16(fabsf(crv) + fabsf(civ));
    float p0 = ar * 1.44269504f;
    asm volatile("" : "+v"(p0), "+v"(fhi), "+v"(flo), "+v"(cm));
    if (w4 < 2) {
      par[t8] = f4{p0, fhi, flo, 0.f};
      parc[t8] = f2{crv, civ};
      if (idx == 0) mc[t8 >> 4] = cm;
    }
  }
  __syncthreads();
  int ce;
  {
    f4 m0 = *(const f4*)(mc), m1 = *(const f4*)(mc + 4);
    float m = ((m0[0] + m0[1]) + (m0[2] + m0[3])) + ((m1[0] + m1[1]) + (m1[2] + m1[3]));
    ce = (int)((__float_as_uint(m) >> 23) & 0xff) - 127;
    ce = ce < -100 ? -100 : (ce > 100 ? 100 : ce);
  }
  {
    const f2 pos01 = {(float)(8 * idx), (float)alpha_of(idx)};
    const f2 pos23 = {(float)alpha_of(16 + idx), (float)alpha_of(32 + idx)};
    const f2 eo01 = {(float)(12 - ce), 13.f}, eo23 = {13.f, 13.f};
    f4 acc0 = {0, 0, 0, 0}, acc1 = {0, 0, 0, 0}, acc2 = {0, 0, 0, 0};
#pragma unroll
    for (int k2 = 0; k2 < 2; ++k2) {
      u4 hi[4], lo[4];
#pragma unroll
      for (int e = 0; e < 4; ++e) {
        const int n = (w4 * 2 + k2) * 16 + 4 * g + e;
        const f4 pp = par[n];
        const f2 pc = parc[n];
        f2 t01 = pos01 * pp[1], t23 = pos23 * pp[1];
        f2 r01 = {__builtin_amdgcn_fractf(t01[0]), __builtin_amdgcn_fractf(t01[1])};
        f2 r23 = {__builtin_amdgcn_fractf(t23[0]), __builtin_amdgcn_fractf(t23[1])};
        r01 = __builtin_elementwise_fma(pos01, f2{pp[2], pp[2]}, r01);
        r23 = __builtin_elementwise_fma(pos23, f2{pp[2], pp[2]}, r23);
        f2 a01 = __builtin_elementwise_fma(pos01, f2{pp[0], pp[0]}, eo01);
        f2 a23 = __builtin_elementwise_fma(pos23, f2{pp[0], pp[0]}, eo23);
        float rv[4] = {r01[0], r01[1], r23[0], r23[1]};
        float av[4] = {a01[0], a01[1], a23[0], a23[1]};
#pragma unroll
        for (int qq = 0; qq < 4; ++qq) {
          float ex = __builtin_amdgcn_exp2f(av[qq]);
          float cs = __builtin_amdgcn_cosf(rv[qq]), sn = __builtin_amdgcn_sinf(rv[qq]);
          f2 z;
          if (qq == 0) {
            f2 t = f2{cs, sn} * pc[0];
            t = __builtin_elementwise_fma(f2{-sn, cs}, f2{pc[1], pc[1]}, t);
            z = t * ex;
          } else {
            z = f2{cs, -sn} * ex;
          }
          unsigned hh, ll;
          split_h(z[0], z[1], hh, ll);
          hi[qq][e] = hh; lo[qq][e] = ll;
        }
      }
      const h8 bh = __builtin_bit_cast(h8, hi[0]), bl = __builtin_bit_cast(h8, lo[0]);
      acc0 = __builtin_amdgcn_mfma_f32_16x16x32_f16(__builtin_bit_cast(h8, lo[1]), bh, acc0, 0, 0, 0);
      acc1 = __builtin_amdgcn_mfma_f32_16x16x32_f16(__builtin_bit_cast(h8, lo[2]), bh, acc1, 0, 0, 0);
      acc2 = __builtin_amdgcn_mfma_f32_16x16x32_f16(__builtin_bit_cast(h8, lo[3]), bh, acc2, 0, 0, 0);
      acc0 = __builtin_amdgcn_mfma_f32_16x16x32_f16(__builtin_bit_cast(h8, hi[1]), bl, acc0, 0, 0, 0);
      acc1 = __builtin_amdgcn_mfma_f32_16x16x32_f16(__builtin_bit_cast(h8, hi[2]), bl, acc1, 0, 0, 0);
      acc2 = __builtin_amdgcn_mfma_f32_16x16x32_f16(__builtin_bit_cast(h8, hi[3]), bl, acc2, 0, 0, 0);
      acc0 = __builtin_amdgcn_mfma_f32_16x16x32_f16(__builtin_bit_cast(h8, hi[1]), bh, acc0, 0, 0, 0);
      acc1 = __builtin_amdgcn_mfma_f32_16x16x32_f16(__builtin_bit_cast(h8, hi[2]), bh, acc1, 0, 0, 0);
      acc2 = __builtin_amdgcn_mfma_f32_16x16x32_f16(__builtin_bit_cast(h8, hi[3]), bh, acc2, 0, 0, 0);
    }
    red[(w4 * 3 + 0) * 64 + lane] = acc0;
    red[(w4 * 3 + 1) * 64 + lane] = acc1;
    red[(w4 * 3 + 2) * 64 + lane] = acc2;
  }
  __syncthreads();
  if (w4 < 3) {
    f4 vv = red[(0 * 3 + w4) * 64 + lane] + red[(1 * 3 + w4) * 64 + lane] +
            (red[(2 * 3 + w4) * 64 + lane] + red[(3 * 3 + w4) * 64 + lane]);
#pragma unroll
    for (int r = 0; r < 4; ++r) kv[(16 * w4 + 4 * g + r) * 16 + idx] = vv[r] * (1.0f / 2048.0f);
  }
  if (t8 == 0) ((float*)(ws + WS_KSC))[d] = __uint_as_float((unsigned)(127 + ce - 14) << 23);
  __syncthreads();
  u4 hi, lo;
#pragma unroll
  for (int j = 0; j < 4; ++j) {
    if (KTERMS > 1) {
      unsigned hh, ll;
      split_h(*(const float*)(smem + kidx[2 * j]), *(const float*)(smem + kidx[2 * j + 1]), hh, ll);
      hi[j] = hh; lo[j] = ll;
    } else {
      hi[j] = pack_h(*(const float*)(smem + kidx[2 * j]), *(const float*)(smem + kidx[2 * j + 1]));
    }
  }
  st16(ws + WS_KH + ((size_t)d * SL + t8 * 8) * 2, hi);
  if (KTERMS > 1) st16(ws + WS_KL + ((size_t)d * SL + t8 * 8) * 2, lo);
}

constexpr int CV_U = 2064 * 2;
constexpr int CV_K = 2160 * 2;
constexpr int CV_OFF_K = 2 * CV_U;
constexpr int CV_OFF_Y = CV_OFF_K + 2 * KTERMS * CV_K;
constexpr int CV_Q = (SL + 4) * 4;
constexpr int CV_SMEM = CV_OFF_Y + 2 * 4 * CV_Q;

constexpr int CV_RPF = 2 + 2 * KTERMS;
constexpr int CV_R0 = (KTERMS == 2 ? 6 : 10) * CV_RPF;
constexpr int CV_NM = 68 * KTERMS;
struct CvStep { int a, n, k, first, op, arg, arg2; };
template <int SUB>
struct CvSched {
  CvStep s[CV_NM];
  int wt, wc, slack;
  constexpr CvSched() : s{}, wt(-1), wc(4), slack(1000) {
    const int AMAX = SUB == 0 ? 15 : 14;
    const int NR = (AMAX + 1) * CV_RPF;
    int nr = CV_R0, mi = 0;
    int endat[16] = {};
    for (int a = 0; a < 16; ++a) endat[a] = 1 << 20;
    for (int a = 0; a <= AMAX; ++a) {
      if ((((a + 1) >> 1) & 1) != SUB) continue;
      for (int nn = 0; nn <= a; ++nn) {
        const int n = (a == 0) ? 0 : (nn + 1 < a ? nn + 1 : (nn + 1 == a ? 0 : a));
        for (int k = 0; k < KTERMS; ++k) {
          CvStep st{a, n, k, (nn == 0 && k == 0) ? 1 : 0, 0, 0, 0};
          { const int need = ((n > a - n ? n : a - n) + 1) * CV_RPF; if (nr < NR && nr - need < slack) slack = nr - need; }
          ++mi;
          if (wc == 4) {
            for (int t = wt + 1; t < 16; ++t)
              if (endat[t] < (1 << 20)) { if (endat[t] + 8 <= mi) { wt = t; wc = 0; } break; }
          }
          if (wc < 4) { st.op = 2; st.arg = wt; st.arg2 = wc; ++wc; }
          else if (nr < NR) { st.op = 1; st.arg = nr; ++nr; }
          s[mi - 1] = st;
        }
      }
      endat[a] = mi;
    }
  }
};
template <int R>
__device__ __forceinline__ void cv_rd(s4 (&A)[16][KTERMS][2], s4 (&B)[16][2], const char* ub, const char* kb) {
  constexpr int i = R / CV_RPF, part = R % CV_RPF;
  if constexpr (part < 2) B[i][part] = tr_read(ub + 256 * i + 32 * part);
  else A[i][(part - 2) >> 1][part & 1] = tr_read(kb + ((part - 2) >> 1) * CV_K + 256 * i + 32 * (1 - (part & 1)));
}
template <int SUB, int I>
__device__ __forceinline__ void cv_step(s4 (&A)[16][KTERMS][2], s4 (&B)[16][2], f4 (&acc)[16], const char* ub, const char* kb, char* qb) {
  constexpr CvSched<SUB> S{};
  constexpr CvStep st = S.s[I];
  const s8 av = cat8(A[st.a - st.n][st.k][0], A[st.a - st.n][st.k][1]), bv = cat8(B[st.n][0], B[st.n][1]);
  if constexpr (st.first) asm volatile("v_mfma_f32_16x16x32_f16 %0, %1, %2, 0" : "=&v"(acc[st.a]) : "v"(av), "v"(bv));
  else asm volatile("v_mfma_f32_16x16x32_f16 %0, %1, %2, %0" : "+v"(acc[st.a]) : "v"(av), "v"(bv));
  if constexpr (st.op == 2) *(float*)(qb + st.arg2 * (CV_Q + 4) + st.arg * 512) = acc[st.arg][st.arg2];
  else if constexpr (st.op == 1) cv_rd<st.arg>(A, B, ub, kb);
  SCHED;
}
template <int SUB, int... Rs, int... Is>
__device__ __forceinline__ void conv_mfma_impl(const char* ub, const char* kb, char* qb, iseq<Rs...>, iseq<Is...>) {
  s4 A[16][KTERMS][2];
  s4 B[16][2];
  f4 acc[16];
  (cv_rd<Rs>(A, B, ub, kb), ...);
  SCHED;
  (cv_step<SUB, Is>(A, B, acc, ub, kb, qb), ...);
  asm volatile("s_nop 7");
  SCHED;
  constexpr CvSched<SUB> S{};
  static_assert(S.slack >= 4, "fragment read scheduled too late");
#pragma unroll
  for (int t = 0; t < 16; ++t) {
    if ((((t + 1) >> 1) & 1) != SUB) continue;
#pragma unroll
    for (int r = 0; r < 4; ++r)
      if (t > S.wt || (t == S.wt && r >= S.wc)) *(float*)(qb + r * (CV_Q + 4) + t * 512) = acc[t][r];
  }
}
template <int SUB>
__device__ __forceinline__ void conv_mfma(const char* ub, const char* kb, char* qb) {
  conv_mfma_impl<SUB>(ub, kb, qb, make_iseq<CV_R0>{}, make_iseq<CV_NM>{});
}

__global__ __launch_bounds__(512, 2) void conv_kernel(
    const u16* __restrict__ xT, const double* __restrict__ stats, const u16* __restrict__ Kh,
    const u16* __restrict__ Kl, const float* __restrict__ ksc, const u16* __restrict__ dskip,
    u16* __restrict__ outT) {
  extern __shared__ __attribute__((aligned(16))) char smem[];
  const int tid = threadIdx.x, lane = tid & 63;
  const int wid = __builtin_amdgcn_readfirstlane(tid >> 6);
  const int d0 = blockIdx.x * 2;
  const int t0 = tid * 4;
  u2 xr[2];
  u4 kr[KTERMS];
  d2 sr[4];
#pragma unroll
  for (int i = 0; i < 4; ++i) sr[i] = *(const d2*)(stats + t0 * 2 + i * 2);
#pragma unroll
  for (int c = 0; c < 2; ++c) xr[c] = *(const u2*)(xT + xoff(d0 + c, t0));
  kr[0] = *(const u4*)(Kh + (size_t)d0 * SL + tid * 8);
  if (KTERMS > 1) kr[KTERMS - 1] = *(const u4*)(Kl + (size_t)d0 * SL + tid * 8);
  const float2 isc2 = *(const float2*)(ksc + d0);
  const unsigned ds2 = *(const unsigned*)(dskip + d0);
  if (tid < 14 * 2 * KTERMS) {
    int which = tid / 14, j = tid % 14;
    *(u4*)(smem + CV_OFF_K + which * CV_K + j * 16) = u4{0, 0, 0, 0};
  } else if (tid >= 64 && tid < 70) {
    int c = (tid - 64) / 3, j = (tid - 64) % 3;
    *(u2*)(smem + c * CV_U + j * 8) = u2{0, 0};
  } else if (tid >= 128 && tid < 136) {
    *(f4*)(smem + CV_OFF_Y + (tid - 128) * CV_Q) = f4{0, 0, 0, 0};
  }
  float mb[4], ri[4];
#pragma unroll
  for (int j = 0; j < 4; ++j) ln_consts((float)sr[j][0], (float)sr[j][1], mb[j], ri[j]);
  u2 ub16[2];
#pragma unroll
  for (int c = 0; c < 2; ++c) {
    unsigned d01 = pack_bf(bflo(xr[c][0]) - mb[0], bfhi(xr[c][0]) - mb[1]);
    unsigned d23 = pack_bf(bflo(xr[c][1]) - mb[2], bfhi(xr[c][1]) - mb[3]);
    ub16[c][0] = pack_bf(bflo(d01) * ri[0], bfhi(d01) * ri[1]);
    ub16[c][1] = pack_bf(bflo(d23) * ri[2], bfhi(d23) * ri[3]);
    u2 a = {pack_h(bflo(ub16[c][0]), bfhi(ub16[c][0])), pack_h(bflo(ub16[c][1]), bfhi(ub16[c][1]))};
    *(u2*)(smem + c * CV_U + 24 + tid * 8) = a;
  }
#pragma unroll
  for (int k = 0; k < KTERMS; ++k)
    *(u4*)(smem + CV_OFF_K + ((tid >> 8) * KTERMS + k) * CV_K + 224 + (tid & 255) * 16) = kr[k];
  __syncthreads();
  {
    const int ch = wid >> 2, par = (wid >> 1) & 1;
    const int g = lane >> 4, q = (lane >> 2) & 3, p = lane & 3;
    const char* ub = smem + ch * CV_U + 8 * (p + q + 8 * g);
    const char* kb = smem + CV_OFF_K + ch * KTERMS * CV_K + 8 * (31 + 16 * par + 4 * p - 8 * g - q) - 32;
    char* qb = smem + CV_OFF_Y + ch * 4 * CV_Q + par * 256 + lane * 4;
    if (wid & 1) conv_mfma<1>(ub, kb, qb);
    else conv_mfma<0>(ub, kb, qb);
  }
  __syncthreads();
#pragma unroll
  for (int c = 0; c < 2; ++c) {
    const char* Q = smem + CV_OFF_Y + c * 4 * CV_Q + t0 * 4;
    f4 y0 = (*(const f4*)(Q) + *(const f4*)(Q + CV_Q)) + (*(const f4*)(Q + 2 * CV_Q) + *(const f4*)(Q + 3 * CV_Q));
    float isc = c ? isc2.y : isc2.x;
    float ds = c ? bfhi(ds2) : bflo(ds2);
    unsigned y01 = pack_bf(y0[0] * isc, y0[1] * isc), y23 = pack_bf(y0[2] * isc, y0[3] * isc);
    unsigned s01 = pack_bf(bflo(ub16[c][0]) * ds, bfhi(ub16[c][0]) * ds), s23 = pack_bf(bflo(ub16[c][1]) * ds, bfhi(ub16[c][1]) * ds);
    unsigned a01 = pack_bf(bflo(y01) + bflo(s01), bfhi(y01) + bfhi(s01)), a23 = pack_bf(bflo(y23) + bflo(s23), bfhi(y23) + bfhi(s23));
    u2 ov = {pack_bf(bflo(xr[c][0]) + bflo(a01), bfhi(xr[c][0]) + bfhi(a01)), pack_bf(bflo(xr[c][1]) + bflo(a23), bfhi(xr[c][1]) + bfhi(a23))};
    st8(outT + xoff(d0 + c, t0), ov);
  }
}

constexpr int G1_SMEM = 512 * 256 + 512 * 8;

__device__ __forceinline__ void glds16(const void* gsrc, unsigned lds_dst) {
  unsigned keep;
  asm volatile("s_mov_b32 %0, m0\n\ts_mov_b32 m0, %2\n\ts_nop 0\n\tglobal_load_lds_dwordx4 %1, off\n\ts_mov_b32 m0, %0"
               : "=&s"(keep) : "v"(gsrc), "s"(lds_dst) : "memory");
}
template <int OFF>
__device__ __forceinline__ void gload16(u4& dst, const void* p) {
  asm volatile("global_load_dwordx4 %0, %1, off offset:%2" : "=v"(dst) : "v"(p), "i"(OFF) : "memory");
}
template <int N>
__device__ __forceinline__ void wait_only(u4& a) {
  asm volatile("s_waitcnt vmcnt(%1)" : "+v"(a) : "i"(N) : "memory");
}
template <int N>
__device__ __forceinline__ void wait_vm() { asm volatile("s_waitcnt vmcnt(%0)" :: "i"(N) : "memory"); }
__device__ __forceinline__ void add1(float& a, float b) { asm("v_add_f32 %0, %0, %1" : "+v"(a) : "v"(b)); }
__device__ __forceinline__ void sqa1(float& a, float b) { asm("v_fmac_f32 %0, %1, %1" : "+v"(a) : "v"(b)); }
__device__ __forceinline__ void bar() { asm volatile("s_waitcnt lgkmcnt(0)\n\ts_barrier" ::: "memory"); }
constexpr int G1_ST = 4 * 32768;
constexpr int F_BPRE = 5;
constexpr int G1_XPF = 3;
constexpr int G1_WPF = 8;

template <int OFF>
__device__ __forceinline__ void gload16_sc1(u4& dst, const void* p) {
  asm volatile("global_load_dwordx4 %0, %1, off offset:%2 sc1" : "=v"(dst) : "v"(p), "i"(OFF) : "memory");
}
template <int N>
__device__ __forceinline__ void wait4(u4 (&a)[4]) {
  asm volatile("s_waitcnt vmcnt(%4)" : "+v"(a[0]), "+v"(a[1]), "+v"(a[2]), "+v"(a[3]) : "i"(N) : "memory");
}

template <bool FINAL>
__global__ __launch_bounds__(512, 2) void ffn_kernel(
    const u16* __restrict__ xT, const u16* __restrict__ W1F, const float* __restrict__ c1,
    u16* HF, float* pst, const u16* __restrict__ W2F, unsigned* cnt,
    u16* __restrict__ out, double* __restrict__ stats) {
  extern __shared__ __attribute__((aligned(16))) char smem[];
  const int tid = threadIdx.x, lane = tid & 63;
  const int wid = __builtin_amdgcn_readfirstlane(tid >> 6);
  const int bx = blockIdx.x, xj = bx & 7, r = bx >> 3;
  const int tp = 4 * (xj >> 1) + (r & 3);
  const int fp = 7 * (xj & 1) + (r >> 2);
  if (r < 28) {
  const int h = lane >> 5, blk = (lane >> 4) & 1, q = (lane >> 2) & 3, p = lane & 3;
  const char* rbase = smem + (8 * h + q) * 64 + blk * 32 + 8 * p;
  float* st = (float*)(smem + G1_ST);
  if (wid >= 4) {
    const int hq = wid - 4;
    const char* xsrc = (const char*)xT + (size_t)tp * 131072 + hq * 1024 + lane * 16;
    const unsigned ldst = (unsigned)(size_t)smem + hq * 1024;
    auto issue_c = [&](int c) AINL {
#pragma unroll
      for (int i = 0; i < 3; ++i) glds16(xsrc + c * 4096 + i * 32768, ldst + c * 4096 + i * 32768);
    };
#pragma unroll
    for (int c = 0; c < G1_XPF; ++c) issue_c(c);
#pragma unroll 1
    for (int c = 0; c < 8 - G1_XPF; ++c) {
      wait_vm<3 * (G1_XPF - 1)>(); bar();
      issue_c(c + G1_XPF);
    }
    sfor<G1_XPF>([&](auto I) AINL {
      constexpr int i = decltype(I)::value;
      if constexpr (i == 0) wait_vm<3 * (G1_XPF - 1)>(); else wait_vm<3 * (G1_XPF - 1 - i) + 8>();
      bar();
      if constexpr (i == 0) {
#pragma unroll
        for (int k = 0; k < 8; ++k) glds16(xsrc + 3 * 32768 + k * 4096, ldst + 3 * 32768 + k * 4096);
      }
    });
    wait_vm<0>();
    bar();
#pragma unroll 1
    for (int it = 0; it < 2; ++it) {
      const int u = it == 0 ? 12 + hq : (fp - 1) * 4 + hq;
      if (it == 1 && (fp < 1 || fp > 3)) break;
      const char* rq = rbase + u * 8192;
      s4 fr[8][2];
      sfor<8>([&](auto I) AINL {
        constexpr int i = decltype(I)::value;
        fr[i][0] = tr_read(rq + i * 1024);
        fr[i][1] = tr_read(rq + i * 1024 + 256);
      });
      float sa = 0.f, sb = 0.f, qa = 0.f, qb = 0.f;
      sfor<8>([&](auto I) AINL {
        constexpr int i = decltype(I)::value;
        u4 xv = __builtin_bit_cast(u4, cat8(fr[i][0], fr[i][1]));
#pragma unroll
        for (int j = 0; j < 4; ++j) {
          float lo = bflo(xv[j]), hi = bfhi(xv[j]);
          add1(sa, lo); add1(sb, hi); sqa1(qa, lo); sqa1(qb, hi);
        }
      });
      float s0 = sa + sb, ss0 = qa + qb;
      s0 += __shfl_xor(s0, 32);
      ss0 += __shfl_xor(ss0, 32);
      if (it == 0) {
        if (lane < 32) st[128 + hq * 32 + lane] = s0;
        bar();
      }
      if (lane < 32 && (it == 1 || fp == 0))
        st8(pst + ((tp * 128 + (u >> 2) * 32 + lane) * 4 + (u & 3)) * 2, u2{__float_as_uint(s0), __float_as_uint(ss0)});
    }
  } else {
  const int ft = fp * 4 + wid;
  u4 wf[32];
  u4 cfu[4];
  const char* wsrc = (const char*)W1F + ((size_t)ft * 32 * 64 + lane) * 16;
  auto issue_w = [&](auto S) AINL {
    constexpr int s = decltype(S)::value;
    gload16<(s & 3) * 1024>(wf[s], wsrc + (s >> 2) * 4096);
  };
  sfor<G1_WPF>(issue_w);
  f16v acc[4], asum;
  s4 xa[3][3][2];
  constexpr int XD = 4, XR = XD + 1;
  s4 xc[XR][2];
  u4 ov;
  float nm[4];
  const int w3 = wid == 3 ? 0 : wid;
  int tto[3];
#pragma unroll
  for (int i = 0; i < 3; ++i) { int tt = i + w3; tto[i] = tt >= 3 ? tt - 3 : tt; }
  const char* rb[3];
#pragma unroll
  for (int i = 0; i < 3; ++i) rb[i] = rbase + tto[i] * 32768;
  u16* dst = HF + ((size_t)((tp * 8 + blk) * 56 + ft) * 64 + (lane & 15) + 16 * h) * 8;
  auto epi_pair = [&](const f16v& a, float m, int e) AINL -> unsigned {
    f4 cf = __builtin_bit_cast(f4, cfu[e >> 2]);
    unsigned pu = pack_bf(fmaf(cf[e & 3], m, a[e]), fmaf(cf[(e & 3) + 1], m, a[e + 1]));
    asm("" : "+v"(pu));
    return __builtin_bit_cast(unsigned, __builtin_elementwise_max(__builtin_bit_cast(s2v, pu), s2v{0, 0}));
  };
  f16v z;
#pragma unroll
  for (int j = 0; j < 16; ++j) z[j] = 0.f;
  const s8 ones8 = {0x3f80, 0x3f80, 0x3f80, 0x3f80, 0x3f80, 0x3f80, 0x3f80, 0x3f80};
  SCHED;
  asm volatile("s_barrier" ::: "memory");
#pragma unroll
  for (int k = 0; k < 2; ++k)
#pragma unroll
    for (int i = 0; i < 3; ++i) { xa[k][i][0] = tr_read(rb[i] + k * 1024); xa[k][i][1] = tr_read(rb[i] + k * 1024 + 256); }
  SCHED;
  sfor<32>([&](auto S) AINL {
    constexpr int s = decltype(S)::value;
    constexpr int issued = (G1_WPF + s <= 32) ? G1_WPF + s : 36;
    asum = __builtin_amdgcn_mfma_f32_32x32x16_bf16(__builtin_bit_cast(b8, ones8), __builtin_bit_cast(b8, cat8(xa[s % 3][0][0], xa[s % 3][0][1])), s == 0 ? z : asum, 0, 0, 0);
    wait_only<issued - s - 1>(wf[s]);
    if constexpr ((s & 3) == 2 && s < 30) asm volatile("s_barrier" ::: "memory");
    SCHED;
    sfor<3>([&](auto I) AINL {
      constexpr int i = decltype(I)::value;
      const b8 xv = __builtin_bit_cast(b8, cat8(xa[s % 3][i][0], xa[s % 3][i][1]));
      acc[i] = __builtin_amdgcn_mfma_f32_32x32x16_bf16(__builtin_bit_cast(b8, wf[s]), xv, s == 0 ? z : acc[i], 0, 0, 0);
      if constexpr (s + 2 < 32) {
        xa[(s + 2) % 3][i][0] = tr_read(rb[i] + (s + 2) * 1024);
        xa[(s + 2) % 3][i][1] = tr_read(rb[i] + (s + 2) * 1024 + 256);
      }
      if constexpr (i == 0) {
        if constexpr (G1_WPF + s < 32) issue_w(ic<(G1_WPF + s < 32) ? G1_WPF + s : 0>{});
        if constexpr (G1_WPF + s == 32) {
          gload16<0>(cfu[0], c1 + ft * 32 + 4 * h);
          gload16<32>(cfu[1], c1 + ft * 32 + 4 * h);
          gload16<64>(cfu[2], c1 + ft * 32 + 4 * h);
          gload16<96>(cfu[3], c1 + ft * 32 + 4 * h);
        }
      }
      SCHED;
    });
  });
  if (lane < 32) st[wid * 32 + lane] = -rbf(asum[0] * (1.0f / DM));
  asm volatile("s_waitcnt vmcnt(0) lgkmcnt(0)\n\ts_barrier" : "+v"(cfu[0]), "+v"(cfu[1]), "+v"(cfu[2]), "+v"(cfu[3]) :: "memory");
  {
    unsigned rbo = (unsigned)(rbase - smem) + 3 * 32768;
    asm volatile("" : "+v"(rbo));
    const char* rbb = smem + rbo;
    sfor<XD>([&](auto I) AINL {
      constexpr int i = decltype(I)::value;
      xc[i][0] = tr_read(rbb + i * 1024); xc[i][1] = tr_read(rbb + i * 1024 + 256);
    });
#pragma unroll
    for (int i = 0; i < 3; ++i) nm[i] = st[tto[i] * 32 + (lane & 31)];
    SCHED;
    sfor<32>([&](auto S) AINL {
      constexpr int s = decltype(S)::value;
      const b8 xv = __builtin_bit_cast(b8, cat8(xc[s % XR][0], xc[s % XR][1]));
      acc[3] = __builtin_amdgcn_mfma_f32_32x32x16_bf16(__builtin_bit_cast(b8, wf[s]), xv, s == 0 ? z : acc[3], 0, 0, 0);
      if constexpr (s + XD < 32) {
        xc[(s + XD) % XR][0] = tr_read(rbb + (s + XD) * 1024);
        xc[(s + XD) % XR][1] = tr_read(rbb + (s + XD) * 1024 + 256);
      }
      if constexpr (s < 24) {
        constexpr int i = s >> 3, pr = s & 7;
        ov[pr & 3] = epi_pair(acc[i], nm[i], 2 * pr);
        if constexpr ((pr & 3) == 3) st16(dst + tto[i] * (2 * 56 * 512) + (pr >> 2) * 256, ov);
      }
      SCHED;
    });
  }
  asm volatile("s_barrier" ::: "memory");
  {
    const float* sp = st + 128 + (lane & 31);
    nm[3] = -rbf(((sp[0] + sp[32]) + (sp[64] + sp[96])) * (1.0f / DM));
  }
#pragma unroll
  for (int c = 0; c < 2; ++c) {
#pragma unroll
    for (int j = 0; j < 4; ++j) ov[j] = epi_pair(acc[3], nm[3], 8 * c + 2 * j);
    st16(dst + 3 * (2 * 56 * 512) + c * 256, ov);
  }
  }
  }
  const int tb = 8 * (xj >> 1) + (r & 7), db = 4 * (xj & 1) + (r >> 3);
  const int g = lane >> 4, n = lane & 15;
  const int mo = wid >> 1, no = 2 * (wid & 1);
  u4 bf[7][4], af[7][4];
  {
    const char* bp[4];
#pragma unroll
    for (int j = 0; j < 4; ++j) bp[j] = (const char*)W2F + ((size_t)((db * 4 + ((j + no) & 3)) * 56 + wid * 7) * 64 + lane) * 16;
    auto issue_b = [&](auto KS) AINL {
      constexpr int ks = decltype(KS)::value;
      sfor<4>([&](auto J) AINL {
        constexpr int j = decltype(J)::value;
        gload16<(ks & 3) * 1024>(bf[ks][j], bp[j] + (ks >> 2) * 4096);
      });
    };
    wait_vm<0>();
    bar();
    if (r < 28 && tid == 0) __hip_atomic_fetch_add(cnt + tp * 32, 1u, __ATOMIC_RELAXED, __HIP_MEMORY_SCOPE_AGENT);
    if (wid == 7) {
      const unsigned* cp = cnt + (tb >> 1) * 32;
      for (int spin = 0; spin < (1 << 22); ++spin) {
        if (__hip_atomic_load(cp, __ATOMIC_RELAXED, __HIP_MEMORY_SCOPE_AGENT) >= 14u) break;
        __builtin_amdgcn_s_sleep(2);
      }
    }
    sfor<F_BPRE>(issue_b);
    bar();
    sfor<7 - F_BPRE>([&](auto I) AINL { issue_b(ic<F_BPRE + decltype(I)::value>{}); });
  }
  u2 res[2];
  u16 resf[2][4];
  if (!FINAL) {
#pragma unroll
    for (int j = 0; j < 2; ++j) res[j] = *(const u2*)(xT + xoff(db * 64 + (no + j) * 16 + n, tb * 64 + mo * 16 + 4 * g));
  } else {
#pragma unroll
    for (int j = 0; j < 2; ++j)
#pragma unroll
      for (int e = 0; e < 4; ++e) resf[j][e] = xT[xoff(db * 64 + (no + j) * 16 + 4 * g + e, tb * 64 + mo * 16 + n)];
  }
  const unsigned long long psu = __hip_atomic_load((const unsigned long long*)(pst + ((tb * 64 + mo * 16 + n) * 4 + g) * 2), __ATOMIC_RELAXED, __HIP_MEMORY_SCOPE_AGENT);
  const char* ap[4];
#pragma unroll
  for (int i = 0; i < 4; ++i) ap[i] = (const char*)HF + ((size_t)((tb * 4 + ((i + mo) & 3)) * 56 + wid * 7) * 64 + lane) * 16;
  auto issue_a = [&](auto KS) AINL {
    constexpr int ks = decltype(KS)::value;
    sfor<4>([&](auto I) AINL {
      constexpr int i = decltype(I)::value;
      gload16_sc1<(ks & 3) * 1024>(af[ks][i], ap[i] + (ks >> 2) * 4096);
    });
  };
  sfor<4>(issue_a);
  f4 acc[4][4];
#pragma unroll
  for (int i = 0; i < 4; ++i)
#pragma unroll
    for (int j = 0; j < 4; ++j) acc[i][j] = f4{0, 0, 0, 0};
  SCHED;
  sfor<7>([&](auto KS) AINL {
    constexpr int ks = decltype(KS)::value;
    wait4<(ks < 4 ? 12 : 4 * (6 - ks))>(af[ks]);
    wait4<63>(bf[ks]);
    SCHED;
#pragma unroll
    for (int i = 0; i < 4; ++i)
#pragma unroll
      for (int j = 0; j < 4; ++j) {
        if (!FINAL) acc[i][j] = __builtin_amdgcn_mfma_f32_16x16x32_bf16(__builtin_bit_cast(b8, af[ks][i]), __builtin_bit_cast(b8, bf[ks][j]), acc[i][j], 0, 0, 0);
        else acc[i][j] = __builtin_amdgcn_mfma_f32_16x16x32_bf16(__builtin_bit_cast(b8, bf[ks][j]), __builtin_bit_cast(b8, af[ks][i]), acc[i][j], 0, 0, 0);
      }
    SCHED;
    if constexpr (ks + 4 < 7) issue_a(ic<(ks + 4 < 7) ? ks + 4 : 0>{});
    SCHED;
  });
  f4* red = (f4*)smem;
#pragma unroll
  for (int i = 0; i < 4; ++i)
#pragma unroll
    for (int jp = 0; jp < 2; ++jp) {
      if (i == 0 && jp == 0) continue;
      const int owner = ((i + mo) & 3) * 2 + ((jp + wid) & 1);
      red[((owner * 7 + i * 2 + jp - 1) * 2 + 0) * 64 + lane] = acc[i][2 * jp];
      red[((owner * 7 + i * 2 + jp - 1) * 2 + 1) * 64 + lane] = acc[i][2 * jp + 1];
    }
  f4 rr;
  {
    float sx = __uint_as_float((unsigned)psu), sy = __uint_as_float((unsigned)(psu >> 32));
    sx += __shfl_xor(sx, 16); sy += __shfl_xor(sy, 16);
    sx += __shfl_xor(sx, 32); sy += __shfl_xor(sy, 32);
    float mb, ri;
    ln_consts(sx, sy, mb, ri);
    if (FINAL) rr = f4{ri, ri, ri, ri};
    else {
#pragma unroll
      for (int e = 0; e < 4; ++e) rr[e] = __shfl(ri, 4 * g + e);
    }
  }
  bar();
  float s[4] = {0, 0, 0, 0}, ss[4] = {0, 0, 0, 0};
#pragma unroll
  for (int j = 0; j < 2; ++j) {
    const f4* rp = red + (wid * 14 + j) * 64 + lane;
    f4 v = ((acc[0][j] + rp[0]) + (rp[128] + rp[256])) + ((rp[384] + rp[512]) + (rp[640] + rp[768]));
    v *= rr;
    unsigned m01 = pack_bf(v[0], v[1]), m23 = pack_bf(v[2], v[3]);
    u2 ov;
    if (!FINAL) {
      ov[0] = pack_bf(bflo(res[j][0]) + bflo(m01), bfhi(res[j][0]) + bfhi(m01));
      ov[1] = pack_bf(bflo(res[j][1]) + bflo(m23), bfhi(res[j][1]) + bfhi(m23));
      *(u2*)(out + xoff(db * 64 + (no + j) * 16 + n, tb * 64 + mo * 16 + 4 * g)) = ov;
      float o[4] = {bflo(ov[0]), bfhi(ov[0]), bflo(ov[1]), bfhi(ov[1])};
#pragma unroll
      for (int e = 0; e < 4; ++e) { s[e] += o[e]; ss[e] = fmaf(o[e], o[e], ss[e]); }
    } else {
      ov[0] = pack_bf(bf2f(resf[j][0]) + bflo(m01), bf2f(resf[j][1]) + bfhi(m01));
      ov[1] = pack_bf(bf2f(resf[j][2]) + bflo(m23), bf2f(resf[j][3]) + bfhi(m23));
      *(u2*)(out + (size_t)(tb * 64 + mo * 16 + n) * DM + db * 64 + (no + j) * 16 + 4 * g) = ov;
    }
  }
  if (!FINAL) {
#pragma unroll
    for (int e = 0; e < 4; ++e) { s[e] = row_sum16(s[e]); ss[e] = row_sum16(ss[e]); }
    if (n < 8) {
      int e = n >> 1;
      float v = (n & 1) ? (e == 0 ? ss[0] : e == 1 ? ss[1] : e == 2 ? ss[2] : ss[3]) : (e == 0 ? s[0] : e == 1 ? s[1] : e == 2 ? s[2] : s[3]);
      unsafeAtomicAdd(stats + (tb * 64 + mo * 16 + 4 * g) * 2 + n, (double)v);
    }
  }
}

extern "C" void kernel_launch(void* const* d_in, const int* in_sizes, int n_in,
                              void* d_out, int out_size, void* d_ws, size_t ws_size,
                              hipStream_t stream) {
  const u16* x = (const u16*)d_in[0];
  const u16* logA = (const u16*)d_in[1];
  const u16* Aim = (const u16*)d_in[2];
  const u16* Cre = (const u16*)d_in[3];
  const u16* Cim = (const u16*)d_in[4];
  const u16* logdt = (const u16*)d_in[5];
  const u16* dskip = (const u16*)d_in[6];
  const u16* w1 = (const u16*)d_in[8];
  const u16* w2 = (const u16*)d_in[9];
  char* ws = (char*)d_ws;
  u16* out = (u16*)d_out;
  (void)hipFuncSetAttribute((const void*)ffn_kernel<false>, hipFuncAttributeMaxDynamicSharedMemorySize, G1_SMEM);
  (void)hipFuncSetAttribute((const void*)ffn_kernel<true>, hipFuncAttributeMaxDynamicSharedMemorySize, G1_SMEM);
  (void)hipFuncSetAttribute((const void*)conv_kernel, hipFuncAttributeMaxDynamicSharedMemorySize, CV_SMEM);

  prep_kernel<<<256 + PREP_W1 + PREP_W2 + PREP_TR, 512, 0, stream>>>(x, logA, Aim, Cre, Cim, logdt, w1, w2, ws);
  conv_kernel<<<256, 512, CV_SMEM, stream>>>((const u16*)(ws + WS_XT0), (const double*)(ws + WS_ST0), (const u16*)(ws + WS_KH),
                                             (const u16*)(ws + WS_KL), (const float*)(ws + WS_KSC), dskip, (u16*)(ws + WS_XT1));
  ffn_kernel<false><<<256, 512, G1_SMEM, stream>>>((const u16*)(ws + WS_XT1), (const u16*)(ws + WS_W1F), (const float*)(ws + WS_C1),
                                                   (u16*)(ws + WS_HF), (float*)(ws + WS_PST), (const u16*)(ws + WS_W2F),
                                                   (unsigned*)(ws + WS_CNT), (u16*)(ws + WS_XT2), (double*)(ws + WS_ST2));
  conv_kernel<<<256, 512, CV_SMEM, stream>>>((const u16*)(ws + WS_XT2), (const double*)(ws + WS_ST2), (const u16*)(ws + WS_KH),
                                             (const u16*)(ws + WS_KL), (const float*)(ws + WS_KSC), dskip, (u16*)(ws + WS_XT3));
  ffn_kernel<true><<<256, 512, G1_SMEM, stream>>>((const u16*)(ws + WS_XT3), (const u16*)(ws + WS_W1F), (const float*)(ws + WS_C1),
                                                  (u16*)(ws + WS_HF2), (float*)(ws + WS_PST2), (const u16*)(ws + WS_W2F),
                                                  (unsigned*)(ws + WS_CNT) + 512, out, nullptr);
}
```

```cpp
#include <hip/hip_runtime.h>
#include <hip/hip_bf16.h>

typedef unsigned short u16;
typedef short s4 __attribute__((ext_vector_type(4)));
typedef short s2v __attribute__((ext_vector_type(2)));
typedef short s8 __attribute__((ext_vector_type(8)));
typedef _Float16 h8 __attribute__((ext_vector_type(8)));
typedef _Float16 h2 __attribute__((ext_vector_type(2)));
typedef __bf16 b2 __attribute__((ext_vector_type(2)));
typedef __bf16 b8 __attribute__((ext_vector_type(8)));
typedef float f4 __attribute__((ext_vector_type(4)));
typedef float f2 __attribute__((ext_vector_type(2)));
typedef double d2 __attribute__((ext_vector_type(2)));
typedef __bf16 b4 __attribute__((ext_vector_type(4)));
typedef float f16v __attribute__((ext_vector_type(16)));
typedef unsigned u4 __attribute__((ext_vector_type(4)));
typedef unsigned u2 __attribute__((ext_vector_type(2)));
typedef __attribute__((address_space(3))) s4* lds_s4p;

#define SCHED __builtin_amdgcn_sched_barrier(0)
template <int... Is> struct iseq {};
template <int N, int... Is> struct mk_iseq : mk_iseq<N - 1, N - 1, Is...> {};
template <int... Is> struct mk_iseq<0, Is...> { typedef iseq<Is...> type; };
template <int N> using make_iseq = typename mk_iseq<N>::type;
template <int I> struct ic { static constexpr int value = I; };
template <class F, int... Is> __device__ __forceinline__ void sfor_impl(F&& f, iseq<Is...>) { (f(ic<Is>{}), ...); }
template <int N, class F> __device__ __forceinline__ void sfor(F&& f) { sfor_impl(f, make_iseq<N>{}); }
#define AINL __attribute__((always_inline))

constexpr int SL = 2048;
constexpr int DM = 512;
constexpr int NS = 128;
constexpr int FF = 1792;
#define KTERMS 1

constexpr size_t WS_XT0 = 0;
constexpr size_t WS_XT1 = WS_XT0 + (size_t)SL * DM * 2;
constexpr size_t WS_XT2 = WS_XT1 + (size_t)SL * DM * 2;
constexpr size_t WS_XT3 = WS_XT2 + (size_t)SL * DM * 2;
constexpr size_t WS_KH  = WS_XT3 + (size_t)SL * DM * 2;
constexpr size_t WS_KL  = WS_KH + (size_t)SL * DM * 2;
constexpr size_t WS_W1F = WS_KL + (size_t)SL * DM * 2;
constexpr size_t WS_W2F = WS_W1F + (size_t)DM * FF * 2;
constexpr size_t WS_HF  = WS_W2F + (size_t)DM * FF * 2;
constexpr size_t WS_HF2 = WS_HF + (size_t)SL * FF * 2;
constexpr size_t WS_ST0 = WS_HF2 + (size_t)SL * FF * 2;
constexpr size_t WS_ST2 = WS_ST0 + (size_t)SL * 16;
constexpr size_t WS_C1  = WS_ST2 + (size_t)SL * 16;
constexpr size_t WS_KSC = WS_C1 + (size_t)FF * 4;
constexpr size_t WS_PST = WS_KSC + (size_t)DM * 4;
constexpr size_t WS_PST2 = WS_PST + (size_t)SL * 32;
constexpr size_t WS_CNT = WS_PST2 + (size_t)SL * 32;

__host__ __device__ __forceinline__ int xoff(int d, int t) { return (t >> 5) * 16384 + d * 32 + (t & 31); }
__device__ __forceinline__ float bf2f(unsigned h) { return __uint_as_float(h << 16); }
__device__ __forceinline__ float bflo(unsigned w) { return __uint_as_float(w << 16); }
__device__ __forceinline__ float bfhi(unsigned w) { return __uint_as_float(w & 0xffff0000u); }
__device__ __forceinline__ unsigned pack_bf(float a, float b) {
  b2 v = {(__bf16)a, (__bf16)b};
  return __builtin_bit_cast(unsigned, v);
}
__device__ __forceinline__ float rbf(float f) {
  return __uint_as_float(((unsigned)__builtin_bit_cast(u16, (__bf16)f)) << 16);
}
__device__ __forceinline__ unsigned pack_h(float a, float b) {
  h2 v = {(_Float16)a, (_Float16)b};
  return __builtin_bit_cast(unsigned, v);
}
__device__ __forceinline__ float dot2(unsigned a, unsigned b, float c) {
  return __builtin_amdgcn_fdot2_f32_bf16(__builtin_bit_cast(b2, a), __builtin_bit_cast(b2, b), c, false);
}
__device__ __forceinline__ s4 tr_read(const char* p) {
  return __builtin_amdgcn_ds_read_tr16_b64_v4i16((lds_s4p)p);
}
__device__ __forceinline__ s8 cat8(s4 a, s4 b) { return __builtin_shufflevector(a, b, 0, 1, 2, 3, 4, 5, 6, 7); }
template <int CTRL>
__device__ __forceinline__ float dppf(float v) {
  return __builtin_bit_cast(float, __builtin_amdgcn_update_dpp(0, __builtin_bit_cast(int, v), CTRL, 0xf, 0xf, true));
}
__device__ __forceinline__ float shr1(float v) { return dppf<0x138>(v); }
__device__ __forceinline__ float row_sum16(float v) {
  v += dppf<0x128>(v);
  v += dppf<0x124>(v);
  v += dppf<0x122>(v);
  v += dppf<0x121>(v);
  return v;
}
constexpr unsigned BF_ONES = 0x3f803f80u;
__device__ __forceinline__ void st16(void* p, u4 v) { asm volatile("global_store_dwordx4 %0, %1, off sc1\n\ts_nop 1" :: "v"(p), "v"(v) : "memory"); }
__device__ __forceinline__ void st8(void* p, u2 v) { asm volatile("global_store_dwordx2 %0, %1, off sc1\n\ts_nop 0" :: "v"(p), "v"(v) : "memory"); }

__device__ __forceinline__ void ln_consts(float s, float ss, float& mb, float& rinv) {
  float mean = s * (1.0f / DM);
  float var = fmaxf(ss * (1.0f / DM) - mean * mean, 0.0f);
  mb = rbf(mean);
  float sd = rbf(__builtin_amdgcn_sqrtf(rbf(rbf(var) + 1e-5f)));
  const float rc = __builtin_amdgcn_rcpf(sd);
  asm("s_nop 0\n\tv_mov_b32 %0, %1" : "=v"(rinv) : "v"(rc), "v"(sd));
}

constexpr int PREP_W1 = 56, PREP_W2 = 56, PREP_TR = 64;
constexpr int PK_PAR = 0, PK_PARC = 2048, PK_RED = 3072, PK_KV = PK_RED + 12288, PK_MX = PK_KV + 3072, PK_SZ = PK_MX + 128;
constexpr int PREP_SMEM = (32768 + 1024 > 2 * PK_SZ) ? 32768 + 1024 : 2 * PK_SZ;

__device__ __forceinline__ int alpha_of(int idx) {
  if (idx < 16) return (idx >> 3) * 128 + (idx & 7);
  if (idx < 24) { int j = idx - 16; return (2 + (j >> 2)) * 128 + (j & 3) * 2; }
  if (idx < 32) { int j = idx - 24; return (4 + (j >> 1)) * 128 + (j & 1) * 4; }
  if (idx < 41) return (8 + idx - 32) * 128;
  return 0;
}
__device__ __forceinline__ float exp_acc(float x) {
  const float L2EH = 1.44269504f, L2EL = (float)(1.4426950408889634 - (double)1.44269504f);
  float t = x * L2EH;
  float e = fmaf(x, L2EH, -t) + x * L2EL;
  float r = __builtin_amdgcn_exp2f(t);
  float o = fmaf(r, e * 0.69314718f, r);
  asm("" : "+v"(o) : "v"(t));
  return o;
}
__device__ __forceinline__ float row_max16(float v) {
  v = fmaxf(v, dppf<0x128>(v));
  v = fmaxf(v, dppf<0x124>(v));
  v = fmaxf(v, dppf<0x122>(v));
  v = fmaxf(v, dppf<0x121>(v));
  return v;
}
__device__ __forceinline__ void split_h(float a, float b, unsigned& hi, unsigned& lo) {
  float ah = __uint_as_float(__float_as_uint(a) & 0xffffe000u), bh = __uint_as_float(__float_as_uint(b) & 0xffffe000u);
  hi = pack_h(ah, bh);
  lo = pack_h(a - ah, b - bh);
}

__global__ __launch_bounds__(512, 4) void prep_kernel(
    const u16* __restrict__ x, const u16* __restrict__ logA, const u16* __restrict__ Aim,
    const u16* __restrict__ Cre, const u16* __restrict__ Cim, const u16* __restrict__ logdt,
    const u16* __restrict__ w1, const u16* __restrict__ w2, char* __restrict__ ws) {
  __shared__ __attribute__((aligned(16))) char smem[PREP_SMEM];
  const int tid = threadIdx.x, lane = tid & 63;
  const int wid = __builtin_amdgcn_readfirstlane(tid >> 6);
  const int b = blockIdx.x;
  const int idx = lane & 15, g = lane >> 4;
  if (b >= 256) {
    const int bj = b - 256;
    const int job = bj < PREP_W1 ? 0 : (bj < PREP_W1 + PREP_W2 ? 1 : 2);
    const int jb = job == 0 ? bj : (job == 1 ? bj - PREP_W1 : bj - PREP_W1 - PREP_W2);
    u4 v[4];
    int lofs;
    int ls1, ls2;
    {
      const u16* src;
      int gs1, gs2;
      if (job == 0) {
        src = w1 + (size_t)(tid >> 2) * FF + jb * 32 + (tid & 3) * 8;
        gs1 = 128 * FF; gs2 = 256 * FF; lofs = tid * 16; ls1 = 8192; ls2 = 16384;
      } else if (job == 1) {
        src = w2 + (size_t)((jb >> 2) * 128 + (tid >> 4)) * DM + (jb & 3) * 128 + (tid & 15) * 8;
        gs1 = 32 * DM; gs2 = 64 * DM; lofs = (tid >> 4) * 256 + (((tid & 15) ^ (((tid >> 4) & 7) << 1)) * 16); ls1 = 8192; ls2 = 16384;
      } else {
        const int r0 = (wid >> 2) * 8 + (wid & 3);
        src = x + (size_t)(jb * 32 + r0) * DM + lane * 8;
        gs1 = 4 * DM; gs2 = 16 * DM; lofs = r0 * 1024 + ((lane ^ (wid << 1)) * 16); ls1 = 4096; ls2 = 16384;
      }
      v[0] = *(const u4*)(src); v[1] = *(const u4*)(src + gs1); v[2] = *(const u4*)(src + gs2); v[3] = *(const u4*)(src + gs1 + gs2);
    }
    *(u4*)(smem + lofs) = v[0];
    *(u4*)(smem + lofs + ls1) = v[1];
    *(u4*)(smem + lofs + ls2) = v[2];
    *(u4*)(smem + lofs + ls1 + ls2) = v[3];
    __syncthreads();
    const int q = (lane >> 2) & 3, p = lane & 3;
    float* csr = (float*)(smem + 32768);
    if (job == 0) {
      const int h = lane >> 5, blk = (lane >> 4) & 1;
      const char* rb = smem + (wid * 64 + 8 * h + q) * 64 + blk * 32 + p * 8;
      u16* W1F = (u16*)(ws + WS_W1F);
      float cs = 0.f;
#pragma unroll
      for (int i = 0; i < 4; ++i) {
        s8 o = cat8(tr_read(rb + i * 1024), tr_read(rb + i * 1024 + 256));
        u4 ou = __builtin_bit_cast(u4, o);
#pragma unroll
        for (int j = 0; j < 4; ++j) cs = dot2(ou[j], BF_ONES, cs);
        st16(W1F + ((size_t)(jb * 32 + wid * 4 + i) * 64 + lane) * 8, ou);
      }
      cs += __shfl_xor(cs, 32);
      if (lane < 32) csr[wid * 32 + lane] = cs;
    } else if (job == 1) {
      const int rl = 16 * (g >> 1) + 4 * (g & 1) + q;
      const int sw = rl & 7;
      u16* W2F = (u16*)(ws + WS_W2F);
#pragma unroll
      for (int i = 0; i < 4; ++i) {
        const int nt = (wid & 1) * 4 + i;
        const char* rb = smem + ((wid >> 1) * 32 + rl) * 256 + ((nt ^ sw) * 32) + p * 8;
        s8 o = cat8(tr_read(rb), tr_read(rb + 2048));
        st16(W2F + ((size_t)(((jb & 3) * 8 + nt) * 56 + (jb >> 2) * 4 + (wid >> 1)) * 64 + lane) * 8, __builtin_bit_cast(u4, o));
      }
    } else if (job == 2) {
      const int sw = q | ((g & 1) << 2);
      u16* xT = (u16*)(ws + WS_XT0);
#pragma unroll
      for (int i = 0; i < 4; ++i) {
        int dt_ = wid * 4 + i;
        const char* base = smem + (8 * g + q) * 1024 + ((dt_ ^ sw) * 32) + 8 * p;
        s8 o = cat8(tr_read(base), tr_read(base + 4096));
        *(s8*)(xT + xoff(dt_ * 16 + idx, jb * 32 + 8 * g)) = o;
      }
      float sv[4], ssv[4];
#pragma unroll
      for (int i = 0; i < 4; ++i) {
        float s = 0.f, ss = 0.f;
#pragma unroll
        for (int j = 0; j < 4; ++j) { s = dot2(v[i][j], BF_ONES, s); ss = dot2(v[i][j], v[i][j], ss); }
        sv[i] = row_sum16(s); ssv[i] = row_sum16(ss);
      }
      float val = sv[0];
#pragma unroll
      for (int i = 1; i < 4; ++i) val = (idx == 2 * i) ? sv[i] : val;
#pragma unroll
      for (int i = 0; i < 4; ++i) val = (idx == 2 * i + 1) ? ssv[i] : val;
      val += __shfl_xor(val, 16);
      val += __shfl_xor(val, 32);
      if (lane < 8) {
        const int i = lane >> 1;
        const int r = jb * 32 + (i >> 1) * 16 + (wid >> 2) * 8 + (i & 1) * 4 + (wid & 3);
        ((double*)(ws + WS_ST0))[r * 2 + (lane & 1)] = (double)val;
        ((double*)(ws + WS_ST2))[r * 2 + (lane & 1)] = 0.0;
      }
    }

    if (job == 0) {
      __syncthreads();
      if (tid < 32) {
        float cs = 0.f;
#pragma unroll
        for (int i = 0; i < 8; ++i) cs += csr[i * 32 + tid];
        ((float*)(ws + WS_C1))[jb * 32 + tid] = cs;
      }
    }
    if (bj == 0 && tid >= 64 && tid < 96) ((unsigned*)(ws + WS_CNT))[(tid - 64) * 32] = 0u;
    return;
  }
  const int ch = wid >> 2, w4 = wid & 3, t8 = tid & 255;
  const int d = b * 2 + ch;
  const int pn = d * NS + (t8 & (NS - 1));
  const u16 pl0 = logdt[d], pl1 = logA[pn], pl2 = Aim[pn], pl3 = Cre[pn], pl4 = Cim[pn];
  SCHED;
  int kidx[8];
#pragma unroll
  for (int j = 0; j < 8; ++j) {
    float pf = rbf((float)(t8 * 8 + j));
    int p = (int)pf;
    int e = (int)(__float_as_uint(pf) >> 23) - (127 + 7);
    e = e < 0 ? 0 : (e > 3 ? 3 : e);
    int ai_ = ((((p >> 7) << 3) + (p & 7)) >> e) + 8 * e;
    kidx[j] = ch * PK_SZ + PK_KV + (ai_ * 16 + ((p & 127) >> 3)) * 4;
    asm volatile("" : "+v"(kidx[j]));
  }
  char* ks = smem + ch * PK_SZ;
  f4* par = (f4*)(ks + PK_PAR);
  f2* parc = (f2*)(ks + PK_PARC);
  f4* red = (f4*)(ks + PK_RED);
  float* kv = (float*)(ks + PK_KV);
  float* mc = (float*)(ks + PK_MX);
  SCHED;
  {
    float dt = rbf(exp_acc(bf2f(pl0)));
    float Ar = -rbf(exp_acc(bf2f(pl1)));
    float ar = Ar * dt;
    float ai = bf2f(pl2) * dt;
    const float I2PH = 0.15915494f;
    const float I2PL = (float)(0.15915494309189535 - (double)0.15915494f);
    float fh = ai * I2PH;
    float fl = fmaf(ai, I2PH, -fh) + ai * I2PL;
    fh -= rintf(fh);
    float fhi = __uint_as_float(__float_as_uint(fh) & 0xfffff000u);
    float flo = (fh - fhi) + fl;
    float crv = 2.0f * bf2f(pl3) * dt;
    float civ = 2.0f * bf2f(pl4) * dt;
    float cm = row_sum16(fabsf(crv) + fabsf(civ));
    float p0 = ar * 1.44269504f;
    asm volatile("" : "+v"(p0), "+v"(fhi), "+v"(flo), "+v"(cm));
    if (w4 < 2) {
      par[t8] = f4{p0, fhi, flo, 0.f};
      parc[t8] = f2{crv, civ};
      if (idx == 0) mc[t8 >> 4] = cm;
    }
  }
  __syncthreads();
  int ce;
  {
    f4 m0 = *(const f4*)(mc), m1 = *(const f4*)(mc + 4);
    float m = ((m0[0] + m0[1]) + (m0[2] + m0[3])) + ((m1[0] + m1[1]) + (m1[2] + m1[3]));
    ce = (int)((__float_as_uint(m) >> 23) & 0xff) - 127;
    ce = ce < -100 ? -100 : (ce > 100 ? 100 : ce);
  }
  {
    const f2 pos01 = {(float)(8 * idx), (float)alpha_of(idx)};
    const f2 pos23 = {(float)alpha_of(16 + idx), (float)alpha_of(32 + idx)};
    const f2 eo01 = {(float)(12 - ce), 13.f}, eo23 = {13.f, 13.f};
    f4 acc0 = {0, 0, 0, 0}, acc1 = {0, 0, 0, 0}, acc2 = {0, 0, 0, 0};
#pragma unroll
    for (int k2 = 0; k2 < 2; ++k2) {
      u4 hi[4], lo[4];
#pragma unroll
      for (int e = 0; e < 4; ++e) {
        const int n = (w4 * 2 + k2) * 16 + 4 * g + e;
        const f4 pp = par[n];
        const f2 pc = parc[n];
        f2 t01 = pos01 * pp[1], t23 = pos23 * pp[1];
        f2 r01 = {__builtin_amdgcn_fractf(t01[0]), __builtin_amdgcn_fractf(t01[1])};
        f2 r23 = {__builtin_amdgcn_fractf(t23[0]), __builtin_amdgcn_fractf(t23[1])};
        r01 = __builtin_elementwise_fma(pos01, f2{pp[2], pp[2]}, r01);
        r23 = __builtin_elementwise_fma(pos23, f2{pp[2], pp[2]}, r23);
        f2 a01 = __builtin_elementwise_fma(pos01, f2{pp[0], pp[0]}, eo01);
        f2 a23 = __builtin_elementwise_fma(pos23, f2{pp[0], pp[0]}, eo23);
        float rv[4] = {r01[0], r01[1], r23[0], r23[1]};
        float av[4] = {a01[0], a01[1], a23[0], a23[1]};
#pragma unroll
        for (int qq = 0; qq < 4; ++qq) {
          float ex = __builtin_amdgcn_exp2f(av[qq]);
          float cs = __builtin_amdgcn_cosf(rv[qq]), sn = __builtin_amdgcn_sinf(rv[qq]);
          f2 z;
          if (qq == 0) {
            f2 t = f2{cs, sn} * pc[0];
            t = __builtin_elementwise_fma(f2{-sn, cs}, f2{pc[1], pc[1]}, t);
            z = t * ex;
          } else {
            z = f2{cs, -sn} * ex;
          }
          asm("" : "+v"(z) : "v"(rv[qq]), "v"(av[qq]));
          unsigned hh, ll;
          split_h(z[0], z[1], hh, ll);
          hi[qq][e] = hh; lo[qq][e] = ll;
        }
      }
      const h8 bh = __builtin_bit_cast(h8, hi[0]), bl = __builtin_bit_cast(h8, lo[0]);
      acc0 = __builtin_amdgcn_mfma_f32_16x16x32_f16(__builtin_bit_cast(h8, lo[1]), bh, acc0, 0, 0, 0);
      acc1 = __builtin_amdgcn_mfma_f32_16x16x32_f16(__builtin_bit_cast(h8, lo[2]), bh, acc1, 0, 0, 0);
      acc2 = __builtin_amdgcn_mfma_f32_16x16x32_f16(__builtin_bit_cast(h8, lo[3]), bh, acc2, 0, 0, 0);
      acc0 = __builtin_amdgcn_mfma_f32_16x16x32_f16(__builtin_bit_cast(h8, hi[1]), bl, acc0, 0, 0, 0);
      acc1 = __builtin_amdgcn_mfma_f32_16x16x32_f16(__builtin_bit_cast(h8, hi[2]), bl, acc1, 0, 0, 0);
      acc2 = __builtin_amdgcn_mfma_f32_16x16x32_f16(__builtin_bit_cast(h8, hi[3]), bl, acc2, 0, 0, 0);
      acc0 = __builtin_amdgcn_mfma_f32_16x16x32_f16(__builtin_bit_cast(h8, hi[1]), bh, acc0, 0, 0, 0);
      acc1 = __builtin_amdgcn_mfma_f32_16x16x32_f16(__builtin_bit_cast(h8, hi[2]), bh, acc1, 0, 0, 0);
      acc2 = __builtin_amdgcn_mfma_f32_16x16x32_f16(__builtin_bit_cast(h8, hi[3]), bh, acc2, 0, 0, 0);
    }
    red[(w4 * 3 + 0) * 64 + lane] = acc0;
    red[(w4 * 3 + 1) * 64 + lane] = acc1;
    red[(w4 * 3 + 2) * 64 + lane] = acc2;
  }
  __syncthreads();
  if (w4 < 3) {
    f4 vv = red[(0 * 3 + w4) * 64 + lane] + red[(1 * 3 + w4) * 64 + lane] +
            (red[(2 * 3 + w4) * 64 + lane] + red[(3 * 3 + w4) * 64 + lane]);
#pragma unroll
    for (int r = 0; r < 4; ++r) kv[(16 * w4 + 4 * g + r) * 16 + idx] = vv[r] * (1.0f / 2048.0f);
  }
  if (t8 == 0) ((float*)(ws + WS_KSC))[d] = __uint_as_float((unsigned)(127 + ce - 14) << 23);
  __syncthreads();
  u4 hi, lo;
#pragma unroll
  for (int j = 0; j < 4; ++j) {
    if (KTERMS > 1) {
      unsigned hh, ll;
      split_h(*(const float*)(smem + kidx[2 * j]), *(const float*)(smem + kidx[2 * j + 1]), hh, ll);
      hi[j] = hh; lo[j] = ll;
    } else {
      hi[j] = pack_h(*(const float*)(smem + kidx[2 * j]), *(const float*)(smem + kidx[2 * j + 1]));
    }
  }
  st16(ws + WS_KH + ((size_t)d * SL + t8 * 8) * 2, hi);
  if (KTERMS > 1) st16(ws + WS_KL + ((size_t)d * SL + t8 * 8) * 2, lo);
}

constexpr int CV_U = 2064 * 2;
constexpr int CV_K = 2160 * 2;
constexpr int CV_OFF_K = 2 * CV_U;
constexpr int CV_OFF_Y = CV_OFF_K + 2 * KTERMS * CV_K;
constexpr int CV_Q = (SL + 4) * 4;
constexpr int CV_SMEM = CV_OFF_Y + 2 * 4 * CV_Q;

constexpr int CV_RPF = 2 + 2 * KTERMS;
constexpr int CV_R0 = (KTERMS == 2 ? 6 : 10) * CV_RPF;
constexpr int CV_NM = 68 * KTERMS;
struct CvStep { int a, n, k, first, op, arg, arg2; };
template <int SUB>
struct CvSched {
  CvStep s[CV_NM];
  int wt, wc, slack;
  constexpr CvSched() : s{}, wt(-1), wc(4), slack(1000) {
    const int AMAX = SUB == 0 ? 15 : 14;
    const int NR = (AMAX + 1) * CV_RPF;
    int nr = CV_R0, mi = 0;
    int endat[16] = {};
    for (int a = 0; a < 16; ++a) endat[a] = 1 << 20;
    for (int a = 0; a <= AMAX; ++a) {
      if ((((a + 1) >> 1) & 1) != SUB) continue;
      for (int nn = 0; nn <= a; ++nn) {
        const int n = (a == 0) ? 0 : (nn + 1 < a ? nn + 1 : (nn + 1 == a ? 0 : a));
        for (int k = 0; k < KTERMS; ++k) {
          CvStep st{a, n, k, (nn == 0 && k == 0) ? 1 : 0, 0, 0, 0};
          { const int need = ((n > a - n ? n : a - n) + 1) * CV_RPF; if (nr < NR && nr - need < slack) slack = nr - need; }
          ++mi;
          if (wc == 4) {
            for (int t = wt + 1; t < 16; ++t)
              if (endat[t] < (1 << 20)) { if (endat[t] + 8 <= mi) { wt = t; wc = 0; } break; }
          }
          if (wc < 4) { st.op = 2; st.arg = wt; st.arg2 = wc; ++wc; }
          else if (nr < NR) { st.op = 1; st.arg = nr; ++nr; }
          s[mi - 1] = st;
        }
      }
      endat[a] = mi;
    }
  }
};
template <int R>
__device__ __forceinline__ void cv_rd(s4 (&A)[16][KTERMS][2], s4 (&B)[16][2], const char* ub, const char* kb) {
  constexpr int i = R / CV_RPF, part = R % CV_RPF;
  if constexpr (part < 2) B[i][part] = tr_read(ub + 256 * i + 32 * part);
  else A[i][(part - 2) >> 1][part & 1] = tr_read(kb + ((part - 2) >> 1) * CV_K + 256 * i + 32 * (1 - (part & 1)));
}
template <int SUB, int I>
__device__ __forceinline__ void cv_step(s4 (&A)[16][KTERMS][2], s4 (&B)[16][2], f4 (&acc)[16], const char* ub, const char* kb, char* qb) {
  constexpr CvSched<SUB> S{};
  constexpr CvStep st = S.s[I];
  const s8 av = cat8(A[st.a - st.n][st.k][0], A[st.a - st.n][st.k][1]), bv = cat8(B[st.n][0], B[st.n][1]);
  if constexpr (st.first) asm volatile("v_mfma_f32_16x16x32_f16 %0, %1, %2, 0" : "=&v"(acc[st.a]) : "v"(av), "v"(bv));
  else asm volatile("v_mfma_f32_16x16x32_f16 %0, %1, %2, %0" : "+v"(acc[st.a]) : "v"(av), "v"(bv));
  if constexpr (st.op == 2) *(float*)(qb + st.arg2 * (CV_Q + 4) + st.arg * 512) = acc[st.arg][st.arg2];
  else if constexpr (st.op == 1) cv_rd<st.arg>(A, B, ub, kb);
  SCHED;
}
template <int SUB, int... Rs, int... Is>
__device__ __forceinline__ void conv_mfma_impl(const char* ub, const char* kb, char* qb, iseq<Rs...>, iseq<Is...>) {
  s4 A[16][KTERMS][2];
  s4 B[16][2];
  f4 acc[16];
  (cv_rd<Rs>(A, B, ub, kb), ...);
  SCHED;
  (cv_step<SUB, Is>(A, B, acc, ub, kb, qb), ...);
  asm volatile("s_nop 7");
  SCHED;
  constexpr CvSched<SUB> S{};
  static_assert(S.slack >= 4, "fragment read scheduled too late");
#pragma unroll
  for (int t = 0; t < 16; ++t) {
    if ((((t + 1) >> 1) & 1) != SUB) continue;
#pragma unroll
    for (int r = 0; r < 4; ++r)
      if (t > S.wt || (t == S.wt && r >= S.wc)) *(float*)(qb + r * (CV_Q + 4) + t * 512) = acc[t][r];
  }
}
template <int SUB>
__device__ __forceinline__ void conv_mfma(const char* ub, const char* kb, char* qb) {
  conv_mfma_impl<SUB>(ub, kb, qb, make_iseq<CV_R0>{}, make_iseq<CV_NM>{});
}

__global__ __launch_bounds__(512, 2) void conv_kernel(
    const u16* __restrict__ xT, const double* __restrict__ stats, const u16* __restrict__ Kh,
    const u16* __restrict__ Kl, const float* __restrict__ ksc, const u16* __restrict__ dskip,
    u16* __restrict__ outT) {
  extern __shared__ __attribute__((aligned(16))) char smem[];
  const int tid = threadIdx.x, lane = tid & 63;
  const int wid = __builtin_amdgcn_readfirstlane(tid >> 6);
  const int d0 = blockIdx.x * 2;
  const int t0 = tid * 4;
  u2 xr[2];
  u4 kr[KTERMS];
  d2 sr[4];
#pragma unroll
  for (int i = 0; i < 4; ++i) sr[i] = *(const d2*)(stats + t0 * 2 + i * 2);
#pragma unroll
  for (int c = 0; c < 2; ++c) xr[c] = *(const u2*)(xT + xoff(d0 + c, t0));
  kr[0] = *(const u4*)(Kh + (size_t)d0 * SL + tid * 8);
  if (KTERMS > 1) kr[KTERMS - 1] = *(const u4*)(Kl + (size_t)d0 * SL + tid * 8);
  const float2 isc2 = *(const float2*)(ksc + d0);
  const unsigned ds2 = *(const unsigned*)(dskip + d0);
  if (tid < 14 * 2 * KTERMS) {
    int which = tid / 14, j = tid % 14;
    *(u4*)(smem + CV_OFF_K + which * CV_K + j * 16) = u4{0, 0, 0, 0};
  } else if (tid >= 64 && tid < 70) {
    int c = (tid - 64) / 3, j = (tid - 64) % 3;
    *(u2*)(smem + c * CV_U + j * 8) = u2{0, 0};
  } else if (tid >= 128 && tid < 136) {
    *(f4*)(smem + CV_OFF_Y + (tid - 128) * CV_Q) = f4{0, 0, 0, 0};
  }
  float mb[4], ri[4];
#pragma unroll
  for (int j = 0; j < 4; ++j) ln_consts((float)sr[j][0], (float)sr[j][1], mb[j], ri[j]);
  u2 ub16[2];
#pragma unroll
  for (int c = 0; c < 2; ++c) {
    unsigned d01 = pack_bf(bflo(xr[c][0]) - mb[0], bfhi(xr[c][0]) - mb[1]);
    unsigned d23 = pack_bf(bflo(xr[c][1]) - mb[2], bfhi(xr[c][1]) - mb[3]);
    ub16[c][0] = pack_bf(bflo(d01) * ri[0], bfhi(d01) * ri[1]);
    ub16[c][1] = pack_bf(bflo(d23) * ri[2], bfhi(d23) * ri[3]);
    u2 a = {pack_h(bflo(ub16[c][0]), bfhi(ub16[c][0])), pack_h(bflo(ub16[c][1]), bfhi(ub16[c][1]))};
    *(u2*)(smem + c * CV_U + 24 + tid * 8) = a;
  }
#pragma unroll
  for (int k = 0; k < KTERMS; ++k)
    *(u4*)(smem + CV_OFF_K + ((tid >> 8) * KTERMS + k) * CV_K + 224 + (tid & 255) * 16) = kr[k];
  __syncthreads();
  {
    const int ch = wid >> 2, par = (wid >> 1) & 1;
    const int g = lane >> 4, q = (lane >> 2) & 3, p = lane & 3;
    const char* ub = smem + ch * CV_U + 8 * (p + q + 8 * g);
    const char* kb = smem + CV_OFF_K + ch * KTERMS * CV_K + 8 * (31 + 16 * par + 4 * p - 8 * g - q) - 32;
    char* qb = smem + CV_OFF_Y + ch * 4 * CV_Q + par * 256 + lane * 4;
    if (wid & 1) conv_mfma<1>(ub, kb, qb);
    else conv_mfma<0>(ub, kb, qb);
  }
  __syncthreads();
#pragma unroll
  for (int c = 0; c < 2; ++c) {
    const char* Q = smem + CV_OFF_Y + c * 4 * CV_Q + t0 * 4;
    f4 y0 = (*(const f4*)(Q) + *(const f4*)(Q + CV_Q)) + (*(const f4*)(Q + 2 * CV_Q) + *(const f4*)(Q + 3 * CV_Q));
    float isc = c ? isc2.y : isc2.x;
    float ds = c ? bfhi(ds2) : bflo(ds2);
    unsigned y01 = pack_bf(y0[0] * isc, y0[1] * isc), y23 = pack_bf(y0[2] * isc, y0[3] * isc);
    unsigned s01 = pack_bf(bflo(ub16[c][0]) * ds, bfhi(ub16[c][0]) * ds), s23 = pack_bf(bflo(ub16[c][1]) * ds, bfhi(ub16[c][1]) * ds);
    unsigned a01 = pack_bf(bflo(y01) + bflo(s01), bfhi(y01) + bfhi(s01)), a23 = pack_bf(bflo(y23) + bflo(s23), bfhi(y23) + bfhi(s23));
    u2 ov = {pack_bf(bflo(xr[c][0]) + bflo(a01), bfhi(xr[c][0]) + bfhi(a01)), pack_bf(bflo(xr[c][1]) + bflo(a23), bfhi(xr[c][1]) + bfhi(a23))};
    st8(outT + xoff(d0 + c, t0), ov);
  }
}

constexpr int G1_SMEM = 512 * 256 + 512 * 8;

__device__ __forceinline__ void glds16(const void* gsrc, unsigned lds_dst) {
  unsigned keep;
  asm volatile("s_mov_b32 %0, m0\n\ts_mov_b32 m0, %2\n\ts_nop 0\n\tglobal_load_lds_dwordx4 %1, off\n\ts_mov_b32 m0, %0"
               : "=&s"(keep) : "v"(gsrc), "s"(lds_dst) : "memory");
}
template <int OFF>
__device__ __forceinline__ void gload16(u4& dst, const void* p) {
  asm volatile("global_load_dwordx4 %0, %1, off offset:%2" : "=v"(dst) : "v"(p), "i"(OFF) : "memory");
}
template <int N>
__device__ __forceinline__ void wait_only(u4& a) {
  asm volatile("s_waitcnt vmcnt(%1)" : "+v"(a) : "i"(N) : "memory");
}
template <int N>
__device__ __forceinline__ void wait_vm() { asm volatile("s_waitcnt vmcnt(%0)" :: "i"(N) : "memory"); }
__device__ __forceinline__ void add1(float& a, float b) { asm("v_add_f32 %0, %0, %1" : "+v"(a) : "v"(b)); }
__device__ __forceinline__ void sqa1(float& a, float b) { asm("v_fmac_f32 %0, %1, %1" : "+v"(a) : "v"(b)); }
__device__ __forceinline__ void bar() { asm volatile("s_waitcnt lgkmcnt(0)\n\ts_barrier" ::: "memory"); }
constexpr int G1_ST = 4 * 32768;
constexpr int F_BPRE = 5;
constexpr int G1_XPF = 3;
constexpr int G1_WPF = 8;

template <int OFF>
__device__ __forceinline__ void gload16_sc1(u4& dst, const void* p) {
  asm volatile("global_load_dwordx4 %0, %1, off offset:%2 sc1" : "=v"(dst) : "v"(p), "i"(OFF) : "memory");
}
template <int N>
__device__ __forceinline__ void wait4(u4 (&a)[4]) {
  asm volatile("s_waitcnt vmcnt(%4)" : "+v"(a[0]), "+v"(a[1]), "+v"(a[2]), "+v"(a[3]) : "i"(N) : "memory");
}

template <bool FINAL>
__global__ __launch_bounds__(512, 2) void ffn_kernel(
    const u16* __restrict__ xT, const u16* __restrict__ W1F, const float* __restrict__ c1,
    u16* HF, float* pst, const u16* __restrict__ W2F, unsigned* cnt,
    u16* __restrict__ out, double* __restrict__ stats) {
  extern __shared__ __attribute__((aligned(16))) char smem[];
  const int tid = threadIdx.x, lane = tid & 63;
  const int wid = __builtin_amdgcn_readfirstlane(tid >> 6);
  const int bx = blockIdx.x, xj = bx & 7, r = bx >> 3;
  const int tp = 4 * (xj >> 1) + (r & 3);
  const int fp = 7 * (xj & 1) + (r >> 2);
  if (r < 28) {
  const int h = lane >> 5, blk = (lane >> 4) & 1, q = (lane >> 2) & 3, p = lane & 3;
  const char* rbase = smem + (8 * h + q) * 64 + blk * 32 + 8 * p;
  float* st = (float*)(smem + G1_ST);
  if (wid >= 4) {
    const int hq = wid - 4;
    const char* xsrc = (const char*)xT + (size_t)tp * 131072 + hq * 1024 + lane * 16;
    const unsigned ldst = (unsigned)(size_t)smem + hq * 1024;
    auto issue_c = [&](int c) AINL {
#pragma unroll
      for (int i = 0; i < 3; ++i) glds16(xsrc + c * 4096 + i * 32768, ldst + c * 4096 + i * 32768);
    };
#pragma unroll
    for (int c = 0; c < G1_XPF; ++c) issue_c(c);
#pragma unroll 1
    for (int c = 0; c < 8 - G1_XPF; ++c) {
      wait_vm<3 * (G1_XPF - 1)>(); bar();
      issue_c(c + G1_XPF);
    }
    sfor<G1_XPF>([&](auto I) AINL {
      constexpr int i = decltype(I)::value;
      if constexpr (i == 0) wait_vm<3 * (G1_XPF - 1)>(); else wait_vm<3 * (G1_XPF - 1 - i) + 8>();
      bar();
      if constexpr (i == 0) {
#pragma unroll
        for (int k = 0; k < 8; ++k) glds16(xsrc + 3 * 32768 + k * 4096, ldst + 3 * 32768 + k * 4096);
      }
    });
    wait_vm<0>();
    bar();
#pragma unroll 1
    for (int it = 0; it < 2; ++it) {
      const int u = it == 0 ? 12 + hq : (fp - 1) * 4 + hq;
      if (it == 1 && (fp < 1 || fp > 3)) break;
      const char* rq = rbase + u * 8192;
      s4 fr[8][2];
      sfor<8>([&](auto I) AINL {
        constexpr int i = decltype(I)::value;
        fr[i][0] = tr_read(rq + i * 1024);
        fr[i][1] = tr_read(rq + i * 1024 + 256);
      });
      float sa = 0.f, sb = 0.f, qa = 0.f, qb = 0.f;
      sfor<8>([&](auto I) AINL {
        constexpr int i = decltype(I)::value;
        u4 xv = __builtin_bit_cast(u4, cat8(fr[i][0], fr[i][1]));
#pragma unroll
        for (int j = 0; j < 4; ++j) {
          float lo = bflo(xv[j]), hi = bfhi(xv[j]);
          add1(sa, lo); add1(sb, hi); sqa1(qa, lo); sqa1(qb, hi);
        }
      });
      float s0 = sa + sb, ss0 = qa + qb;
      s0 += __shfl_xor(s0, 32);
      ss0 += __shfl_xor(ss0, 32);
      if (it == 0) {
        if (lane < 32) st[128 + hq * 32 + lane] = s0;
        bar();
      }
      if (lane < 32 && (it == 1 || fp == 0))
        st8(pst + ((tp * 128 + (u >> 2) * 32 + lane) * 4 + (u & 3)) * 2, u2{__float_as_uint(s0), __float_as_uint(ss0)});
    }
  } else {
  const int ft = fp * 4 + wid;
  u4 wf[32];
  u4 cfu[4];
  const char* wsrc = (const char*)W1F + ((size_t)ft * 32 * 64 + lane) * 16;
  auto issue_w = [&](auto S) AINL {
    constexpr int s = decltype(S)::value;
    gload16<(s & 3) * 1024>(wf[s], wsrc + (s >> 2) * 4096);
  };
  sfor<G1_WPF>(issue_w);
  f16v acc[4], asum;
  s4 xa[3][3][2];
  constexpr int XD = 4, XR = XD + 1;
  s4 xc[XR][2];
  u4 ov;
  float nm[4];
  const int w3 = wid == 3 ? 0 : wid;
  int tto[3];
#pragma unroll
  for (int i = 0; i < 3; ++i) { int tt = i + w3; tto[i] = tt >= 3 ? tt - 3 : tt; }
  const char* rb[3];
#pragma unroll
  for (int i = 0; i < 3; ++i) rb[i] = rbase + tto[i] * 32768;
  u16* dst = HF + ((size_t)((tp * 8 + blk) * 56 + ft) * 64 + (lane & 15) + 16 * h) * 8;
  auto epi_pair = [&](const f16v& a, float m, int e) AINL -> unsigned {
    f4 cf = __builtin_bit_cast(f4, cfu[e >> 2]);
    unsigned pu = pack_bf(fmaf(cf[e & 3], m, a[e]), fmaf(cf[(e & 3) + 1], m, a[e + 1]));
    asm("" : "+v"(pu));
    return __builtin_bit_cast(unsigned, __builtin_elementwise_max(__builtin_bit_cast(s2v, pu), s2v{0, 0}));
  };
  f16v z;
#pragma unroll
  for (int j = 0; j < 16; ++j) z[j] = 0.f;
  const s8 ones8 = {0x3f80, 0x3f80, 0x3f80, 0x3f80, 0x3f80, 0x3f80, 0x3f80, 0x3f80};
  SCHED;
  asm volatile("s_barrier" ::: "memory");
#pragma unroll
  for (int k = 0; k < 2; ++k)
#pragma unroll
    for (int i = 0; i < 3; ++i) { xa[k][i][0] = tr_read(rb[i] + k * 1024); xa[k][i][1] = tr_read(rb[i] + k * 1024 + 256); }
  SCHED;
  sfor<32>([&](auto S) AINL {
    constexpr int s = decltype(S)::value;
    constexpr int issued = (G1_WPF + s <= 32) ? G1_WPF + s : 36;
    asum = __builtin_amdgcn_mfma_f32_32x32x16_bf16(__builtin_bit_cast(b8, ones8), __builtin_bit_cast(b8, cat8(xa[s % 3][0][0], xa[s % 3][0][1])), s == 0 ? z : asum, 0, 0, 0);
    wait_only<issued - s - 1>(wf[s]);
    if constexpr ((s & 3) == 2 && s < 30) asm volatile("s_barrier" ::: "memory");
    SCHED;
    sfor<3>([&](auto I) AINL {
      constexpr int i = decltype(I)::value;
      const b8 xv = __builtin_bit_cast(b8, cat8(xa[s % 3][i][0], xa[s % 3][i][1]));
      acc[i] = __builtin_amdgcn_mfma_f32_32x32x16_bf16(__builtin_bit_cast(b8, wf[s]), xv, s == 0 ? z : acc[i], 0, 0, 0);
      if constexpr (s + 2 < 32) {
        xa[(s + 2) % 3][i][0] = tr_read(rb[i] + (s + 2) * 1024);
        xa[(s + 2) % 3][i][1] = tr_read(rb[i] + (s + 2) * 1024 + 256);
      }
      if constexpr (i == 0) {
        if constexpr (G1_WPF + s < 32) issue_w(ic<(G1_WPF + s < 32) ? G1_WPF + s : 0>{});
        if constexpr (G1_WPF + s == 32) {
          gload16<0>(cfu[0], c1 + ft * 32 + 4 * h);
          gload16<32>(cfu[1], c1 + ft * 32 + 4 * h);
          gload16<64>(cfu[2], c1 + ft * 32 + 4 * h);
          gload16<96>(cfu[3], c1 + ft * 32 + 4 * h);
        }
      }
      SCHED;
    });
  });
  if (lane < 32) st[wid * 32 + lane] = -rbf(asum[0] * (1.0f / DM));
  asm volatile("s_waitcnt vmcnt(0) lgkmcnt(0)\n\ts_barrier" : "+v"(cfu[0]), "+v"(cfu[1]), "+v"(cfu[2]), "+v"(cfu[3]) :: "memory");
  {
    unsigned rbo = (unsigned)(rbase - smem) + 3 * 32768;
    asm volatile("" : "+v"(rbo));
    const char* rbb = smem + rbo;
    sfor<XD>([&](auto I) AINL {
      constexpr int i = decltype(I)::value;
      xc[i][0] = tr_read(rbb + i * 1024); xc[i][1] = tr_read(rbb + i * 1024 + 256);
    });
#pragma unroll
    for (int i = 0; i < 3; ++i) nm[i] = st[tto[i] * 32 + (lane & 31)];
    SCHED;
    sfor<32>([&](auto S) AINL {
      constexpr int s = decltype(S)::value;
      const b8 xv = __builtin_bit_cast(b8, cat8(xc[s % XR][0], xc[s % XR][1]));
      acc[3] = __builtin_amdgcn_mfma_f32_32x32x16_bf16(__builtin_bit_cast(b8, wf[s]), xv, s == 0 ? z : acc[3], 0, 0, 0);
      if constexpr (s + XD < 32) {
        xc[(s + XD) % XR][0] = tr_read(rbb + (s + XD) * 1024);
        xc[(s + XD) % XR][1] = tr_read(rbb + (s + XD) * 1024 + 256);
      }
      if constexpr (s < 24) {
        constexpr int i = s >> 3, pr = s & 7;
        ov[pr & 3] = epi_pair(acc[i], nm[i], 2 * pr);
        if constexpr ((pr & 3) == 3) st16(dst + tto[i] * (2 * 56 * 512) + (pr >> 2) * 256, ov);
      }
      SCHED;
    });
  }
  asm volatile("s_barrier" ::: "memory");
  {
    const float* sp = st + 128 + (lane & 31);
    nm[3] = -rbf(((sp[0] + sp[32]) + (sp[64] + sp[96])) * (1.0f / DM));
  }
#pragma unroll
  for (int c = 0; c < 2; ++c) {
#pragma unroll
    for (int j = 0; j < 4; ++j) ov[j] = epi_pair(acc[3], nm[3], 8 * c + 2 * j);
    st16(dst + 3 * (2 * 56 * 512) + c * 256, ov);
  }
  }
  }
  const int tb = 8 * (xj >> 1) + (r & 7), db = 4 * (xj & 1) + (r >> 3);
  const int g = lane >> 4, n = lane & 15;
  const int mo = wid >> 1, no = 2 * (wid & 1);
  u4 bf[7][4], af[7][4];
  {
    const char* bp[4];
#pragma unroll
    for (int j = 0; j < 4; ++j) bp[j] = (const char*)W2F + ((size_t)((db * 4 + ((j + no) & 3)) * 56 + wid * 7) * 64 + lane) * 16;
    auto issue_b = [&](auto KS) AINL {
      constexpr int ks = decltype(KS)::value;
      sfor<4>([&](auto J) AINL {
        constexpr int j = decltype(J)::value;
        gload16<(ks & 3) * 1024>(bf[ks][j], bp[j] + (ks >> 2) * 4096);
      });
    };
    wait_vm<0>();
    bar();
    if (r < 28 && tid == 0) __hip_atomic_fetch_add(cnt + tp * 32, 1u, __ATOMIC_RELAXED, __HIP_MEMORY_SCOPE_AGENT);
    if (wid == 7) {
      const unsigned* cp = cnt + (tb >> 1) * 32;
      for (int spin = 0; spin < (1 << 22); ++spin) {
        if (__hip_atomic_load(cp, __ATOMIC_RELAXED, __HIP_MEMORY_SCOPE_AGENT) >= 14u) break;
        __builtin_amdgcn_s_sleep(2);
      }
    }
    sfor<F_BPRE>(issue_b);
    bar();
    sfor<7 - F_BPRE>([&](auto I) AINL { issue_b(ic<F_BPRE + decltype(I)::value>{}); });
  }
  u2 res[2];
  u16 resf[2][4];
  if (!FINAL) {
#pragma unroll
    for (int j = 0; j < 2; ++j) res[j] = *(const u2*)(xT + xoff(db * 64 + (no + j) * 16 + n, tb * 64 + mo * 16 + 4 * g));
  } else {
#pragma unroll
    for (int j = 0; j < 2; ++j)
#pragma unroll
      for (int e = 0; e < 4; ++e) resf[j][e] = xT[xoff(db * 64 + (no + j) * 16 + 4 * g + e, tb * 64 + mo * 16 + n)];
  }
  const unsigned long long psu = __hip_atomic_load((const unsigned long long*)(pst + ((tb * 64 + mo * 16 + n) * 4 + g) * 2), __ATOMIC_RELAXED, __HIP_MEMORY_SCOPE_AGENT);
  const char* ap[4];
#pragma unroll
  for (int i = 0; i < 4; ++i) ap[i] = (const char*)HF + ((size_t)((tb * 4 + ((i + mo) & 3)) * 56 + wid * 7) * 64 + lane) * 16;
  auto issue_a = [&](auto KS) AINL {
    constexpr int ks = decltype(KS)::value;
    sfor<4>([&](auto I) AINL {
      constexpr int i = decltype(I)::value;
      gload16_sc1<(ks & 3) * 1024>(af[ks][i], ap[i] + (ks >> 2) * 4096);
    });
  };
  sfor<4>(issue_a);
  f4 acc[4][4];
#pragma unroll
  for (int i = 0; i < 4; ++i)
#pragma unroll
    for (int j = 0; j < 4; ++j) acc[i][j] = f4{0, 0, 0, 0};
  SCHED;
  sfor<7>([&](auto KS) AINL {
    constexpr int ks = decltype(KS)::value;
    wait4<(ks < 4 ? 12 : 4 * (6 - ks))>(af[ks]);
    wait4<63>(bf[ks]);
    SCHED;
#pragma unroll
    for (int i = 0; i < 4; ++i)
#pragma unroll
      for (int j = 0; j < 4; ++j) {
        if (!FINAL) acc[i][j] = __builtin_amdgcn_mfma_f32_16x16x32_bf16(__builtin_bit_cast(b8, af[ks][i]), __builtin_bit_cast(b8, bf[ks][j]), acc[i][j], 0, 0, 0);
        else acc[i][j] = __builtin_amdgcn_mfma_f32_16x16x32_bf16(__builtin_bit_cast(b8, bf[ks][j]), __builtin_bit_cast(b8, af[ks][i]), acc[i][j], 0, 0, 0);
      }
    SCHED;
    if constexpr (ks + 4 < 7) issue_a(ic<(ks + 4 < 7) ? ks + 4 : 0>{});
    SCHED;
  });
  f4* red = (f4*)smem;
#pragma unroll
  for (int i = 0; i < 4; ++i)
#pragma unroll
    for (int jp = 0; jp < 2; ++jp) {
      if (i == 0 && jp == 0) continue;
      const int owner = ((i + mo) & 3) * 2 + ((jp + wid) & 1);
      red[((owner * 7 + i * 2 + jp - 1) * 2 + 0) * 64 + lane] = acc[i][2 * jp];
      red[((owner * 7 + i * 2 + jp - 1) * 2 + 1) * 64 + lane] = acc[i][2 * jp + 1];
    }
  f4 rr;
  {
    float sx = __uint_as_float((unsigned)psu), sy = __uint_as_float((unsigned)(psu >> 32));
    sx += __shfl_xor(sx, 16); sy += __shfl_xor(sy, 16);
    sx += __shfl_xor(sx, 32); sy += __shfl_xor(sy, 32);
    float mb, ri;
    ln_consts(sx, sy, mb, ri);
    if (FINAL) rr = f4{ri, ri, ri, ri};
    else {
#pragma unroll
      for (int e = 0; e < 4; ++e) rr[e] = __shfl(ri, 4 * g + e);
    }
  }
  bar();
  float s[4] = {0, 0, 0, 0}, ss[4] = {0, 0, 0, 0};
#pragma unroll
  for (int j = 0; j < 2; ++j) {
    const f4* rp = red + (wid * 14 + j) * 64 + lane;
    f4 v = ((acc[0][j] + rp[0]) + (rp[128] + rp[256])) + ((rp[384] + rp[512]) + (rp[640] + rp[768]));
    v *= rr;
    unsigned m01 = pack_bf(v[0], v[1]), m23 = pack_bf(v[2], v[3]);
    u2 ov;
    if (!FINAL) {
      ov[0] = pack_bf(bflo(res[j][0]) + bflo(m01), bfhi(res[j][0]) + bfhi(m01));
      ov[1] = pack_bf(bflo(res[j][1]) + bflo(m23), bfhi(res[j][1]) + bfhi(m23));
      *(u2*)(out + xoff(db * 64 + (no + j) * 16 + n, tb * 64 + mo * 16 + 4 * g)) = ov;
      float o[4] = {bflo(ov[0]), bfhi(ov[0]), bflo(ov[1]), bfhi(ov[1])};
#pragma unroll
      for (int e = 0; e < 4; ++e) { s[e] += o[e]; ss[e] = fmaf(o[e], o[e], ss[e]); }
    } else {
      ov[0] = pack_bf(bf2f(resf[j][0]) + bflo(m01), bf2f(resf[j][1]) + bfhi(m01));
      ov[1] = pack_bf(bf2f(resf[j][2]) + bflo(m23), bf2f(resf[j][3]) + bfhi(m23));
      *(u2*)(out + (size_t)(tb * 64 + mo * 16 + n) * DM + db * 64 + (no + j) * 16 + 4 * g) = ov;
    }
  }
  if (!FINAL) {
#pragma unroll
    for (int e = 0; e < 4; ++e) { s[e] = row_sum16(s[e]); ss[e] = row_sum16(ss[e]); }
    if (n < 8) {
      int e = n >> 1;
      float v = (n & 1) ? (e == 0 ? ss[0] : e == 1 ? ss[1] : e == 2 ? ss[2] : ss[3]) : (e == 0 ? s[0] : e == 1 ? s[1] : e == 2 ? s[2] : s[3]);
      unsafeAtomicAdd(stats + (tb * 64 + mo * 16 + 4 * g) * 2 + n, (double)v);
    }
  }
}

extern "C" void kernel_launch(void* const* d_in, const int* in_sizes, int n_in,
                              void* d_out, int out_size, void* d_ws, size_t ws_size,
                              hipStream_t stream) {
  const u16* x = (const u16*)d_in[0];
  const u16* logA = (const u16*)d_in[1];
  const u16* Aim = (const u16*)d_in[2];
  const u16* Cre = (const u16*)d_in[3];
  const u16* Cim = (const u16*)d_in[4];
  const u16* logdt = (const u16*)d_in[5];
  const u16* dskip = (const u16*)d_in[6];
  const u16* w1 = (const u16*)d_in[8];
  const u16* w2 = (const u16*)d_in[9];
  char* ws = (char*)d_ws;
  u16* out = (u16*)d_out;
  (void)hipFuncSetAttribute((const void*)ffn_kernel<false>, hipFuncAttributeMaxDynamicSharedMemorySize, G1_SMEM);
  (void)hipFuncSetAttribute((const void*)ffn_kernel<true>, hipFuncAttributeMaxDynamicSharedMemorySize, G1_SMEM);
  (void)hipFuncSetAttribute((const void*)conv_kernel, hipFuncAttributeMaxDynamicSharedMemorySize, CV_SMEM);

  prep_kernel<<<256 + PREP_W1 + PREP_W2 + PREP_TR, 512, 0, stream>>>(x, logA, Aim, Cre, Cim, logdt, w1, w2, ws);
  conv_kernel<<<256, 512, CV_SMEM, stream>>>((const u16*)(ws + WS_XT0), (const double*)(ws + WS_ST0), (const u16*)(ws + WS_KH),
                                             (const u16*)(ws + WS_KL), (const float*)(ws + WS_KSC), dskip, (u16*)(ws + WS_XT1));
  ffn_kernel<false><<<256, 512, G1_SMEM, stream>>>((const u16*)(ws + WS_XT1), (const u16*)(ws + WS_W1F), (const float*)(ws + WS_C1),
                                                   (u16*)(ws + WS_HF), (float*)(ws + WS_PST), (const u16*)(ws + WS_W2F),
                                                   (unsigned*)(ws + WS_CNT), (u16*)(ws + WS_XT2), (double*)(ws + WS_ST2));
  conv_kernel<<<256, 512, CV_SMEM, stream>>>((const u16*)(ws + WS_XT2), (const double*)(ws + WS_ST2), (const u16*)(ws + WS_KH),
                                             (const u16*)(ws + WS_KL), (const float*)(ws + WS_KSC), dskip, (u16*)(ws + WS_XT3));
  ffn_kernel<true><<<256, 512, G1_SMEM, stream>>>((const u16*)(ws + WS_XT3), (const u16*)(ws + WS_W1F), (const float*)(ws + WS_C1),
                                                  (u16*)(ws + WS_HF2), (float*)(ws + WS_PST2), (const u16*)(ws + WS_W2F),
                                                  (unsigned*)(ws + WS_CNT) + 512, out, nullptr);
}
```

```cpp
#include <hip/hip_runtime.h>
#include <hip/hip_bf16.h>

typedef unsigned short u16;
typedef short s4 __attribute__((ext_vector_type(4)));
typedef short s2v __attribute__((ext_vector_type(2)));
typedef short s8 __attribute__((ext_vector_type(8)));
typedef _Float16 h8 __attribute__((ext_vector_type(8)));
typedef _Float16 h2 __attribute__((ext_vector_type(2)));
typedef __bf16 b2 __attribute__((ext_vector_type(2)));
typedef __bf16 b8 __attribute__((ext_vector_type(8)));
typedef float f4 __attribute__((ext_vector_type(4)));
typedef float f2 __attribute__((ext_vector_type(2)));
typedef double d2 __attribute__((ext_vector_type(2)));
typedef __bf16 b4 __attribute__((ext_vector_type(4)));
typedef float f16v __attribute__((ext_vector_type(16)));
typedef unsigned u4 __attribute__((ext_vector_type(4)));
typedef unsigned u2 __attribute__((ext_vector_type(2)));
typedef __attribute__((address_space(3))) s4* lds_s4p;

#define SCHED __builtin_amdgcn_sched_barrier(0)
template <int... Is> struct iseq {};
template <int N, int... Is> struct mk_iseq : mk_iseq<N - 1, N - 1, Is...> {};
template <int... Is> struct mk_iseq<0, Is...> { typedef iseq<Is...> type; };
template <int N> using make_iseq = typename mk_iseq<N>::type;
template <int I> struct ic { static constexpr int value = I; };
template <class F, int... Is> __device__ __forceinline__ void sfor_impl(F&& f, iseq<Is...>) { (f(ic<Is>{}), ...); }
template <int N, class F> __device__ __forceinline__ void sfor(F&& f) { sfor_impl(f, make_iseq<N>{}); }
#define AINL __attribute__((always_inline))

constexpr int SL = 2048;
constexpr int DM = 512;
constexpr int NS = 128;
constexpr int FF = 1792;
#define KTERMS 1

constexpr size_t WS_XT0 = 0;
constexpr size_t WS_XT1 = WS_XT0 + (size_t)SL * DM * 2;
constexpr size_t WS_XT2 = WS_XT1 + (size_t)SL * DM * 2;
constexpr size_t WS_XT3 = WS_XT2 + (size_t)SL * DM * 2;
constexpr size_t WS_KH  = WS_XT3 + (size_t)SL * DM * 2;
constexpr size_t WS_KL  = WS_KH + (size_t)SL * DM * 2;
constexpr size_t WS_W1F = WS_KL + (size_t)SL * DM * 2;
constexpr size_t WS_W2F = WS_W1F + (size_t)DM * FF * 2;
constexpr size_t WS_HF  = WS_W2F + (size_t)DM * FF * 2;
constexpr size_t WS_HF2 = WS_HF + (size_t)SL * FF * 2;
constexpr size_t WS_ST0 = WS_HF2 + (size_t)SL * FF * 2;
constexpr size_t WS_ST2 = WS_ST0 + (size_t)SL * 16;
constexpr size_t WS_C1  = WS_ST2 + (size_t)SL * 16;
constexpr size_t WS_KSC = WS_C1 + (size_t)FF * 4;
constexpr size_t WS_PST = WS_KSC + (size_t)DM * 4;
constexpr size_t WS_PST2 = WS_PST + (size_t)SL * 32;
constexpr size_t WS_CNT = WS_PST2 + (size_t)SL * 32;

__host__ __device__ __forceinline__ int xoff(int d, int t) { return (t >> 5) * 16384 + d * 32 + (t & 31); }
__device__ __forceinline__ float bf2f(unsigned h) { return __uint_as_float(h << 16); }
__device__ __forceinline__ float bflo(unsigned w) { return __uint_as_float(w << 16); }
__device__ __forceinline__ float bfhi(unsigned w) { return __uint_as_float(w & 0xffff0000u); }
__device__ __forceinline__ unsigned pack_bf(float a, float b) {
  b2 v = {(__bf16)a, (__bf16)b};
  return __builtin_bit_cast(unsigned, v);
}
__device__ __forceinline__ float rbf(float f) {
  return __uint_as_float(((unsigned)__builtin_bit_cast(u16, (__bf16)f)) << 16);
}
__device__ __forceinline__ unsigned pack_h(float a, float b) {
  h2 v = {(_Float16)a, (_Float16)b};
  return __builtin_bit_cast(unsigned, v);
}
__device__ __forceinline__ float dot2(unsigned a, unsigned b, float c) {
  return __builtin_amdgcn_fdot2_f32_bf16(__builtin_bit_cast(b2, a), __builtin_bit_cast(b2, b), c, false);
}
__device__ __forceinline__ s4 tr_read(const char* p) {
  return __builtin_amdgcn_ds_read_tr16_b64_v4i16((lds_s4p)p);
}
__device__ __forceinline__ s8 cat8(s4 a, s4 b) { return __builtin_shufflevector(a, b, 0, 1, 2, 3, 4, 5, 6, 7); }
template <int CTRL>
__device__ __forceinline__ float dppf(float v) {
  return __builtin_bit_cast(float, __builtin_amdgcn_update_dpp(0, __builtin_bit_cast(int, v), CTRL, 0xf, 0xf, true));
}
__device__ __forceinline__ float shr1(float v) { return dppf<0x138>(v); }
__device__ __forceinline__ float row_sum16(float v) {
  v += dppf<0x128>(v);
  v += dppf<0x124>(v);
  v += dppf<0x122>(v);
  v += dppf<0x121>(v);
  return v;
}
constexpr unsigned BF_ONES = 0x3f803f80u;
__device__ __forceinline__ void st16(void* p, u4 v) { asm volatile("global_store_dwordx4 %0, %1, off sc1\n\ts_nop 1" :: "v"(p), "v"(v) : "memory"); }
__device__ __forceinline__ void st8(void* p, u2 v) { asm volatile("global_store_dwordx2 %0, %1, off sc1\n\ts_nop 0" :: "v"(p), "v"(v) : "memory"); }

__device__ __forceinline__ void ln_consts(float s, float ss, float& mb, float& rinv) {
  float mean = s * (1.0f / DM);
  float var = fmaxf(ss * (1.0f / DM) - mean * mean, 0.0f);
  mb = rbf(mean);
  float sd = rbf(__builtin_amdgcn_sqrtf(rbf(rbf(var) + 1e-5f)));
  const float rc = __builtin_amdgcn_rcpf(sd);
  asm("s_nop 0\n\tv_mov_b32 %0, %1" : "=v"(rinv) : "v"(rc), "v"(sd));
}

constexpr int PREP_W1 = 56, PREP_W2 = 56, PREP_TR = 64;
constexpr int PK_PAR = 0, PK_PARC = 2048, PK_RED = 3072, PK_KV = PK_RED + 12288, PK_MX = PK_KV + 3072, PK_SZ = PK_MX + 128;
constexpr int PREP_SMEM = (32768 + 1024 > 2 * PK_SZ) ? 32768 + 1024 : 2 * PK_SZ;

__device__ __forceinline__ int alpha_of(int idx) {
  if (idx < 16) return (idx >> 3) * 128 + (idx & 7);
  if (idx < 24) { int j = idx - 16; return (2 + (j >> 2)) * 128 + (j & 3) * 2; }
  if (idx < 32) { int j = idx - 24; return (4 + (j >> 1)) * 128 + (j & 1) * 4; }
  if (idx < 41) return (8 + idx - 32) * 128;
  return 0;
}
__device__ __forceinline__ float exp_acc(float x) {
  const float L2EH = 1.44269504f, L2EL = (float)(1.4426950408889634 - (double)1.44269504f);
  float t = x * L2EH;
  float e = fmaf(x, L2EH, -t) + x * L2EL;
  float r = __builtin_amdgcn_exp2f(t);
  float o = fmaf(r, e * 0.69314718f, r);
  asm("" : "+v"(o) : "v"(t));
  return o;
}
__device__ __forceinline__ float row_max16(float v) {
  v = fmaxf(v, dppf<0x128>(v));
  v = fmaxf(v, dppf<0x124>(v));
  v = fmaxf(v, dppf<0x122>(v));
  v = fmaxf(v, dppf<0x121>(v));
  return v;
}
__device__ __forceinline__ void split_h(float a, float b, unsigned& hi, unsigned& lo) {
  float ah = __uint_as_float(__float_as_uint(a) & 0xffffe000u), bh = __uint_as_float(__float_as_uint(b) & 0xffffe000u);
  hi = pack_h(ah, bh);
  lo = pack_h(a - ah, b - bh);
}

__global__ __launch_bounds__(512, 4) void prep_kernel(
    const u16* __restrict__ x, const u16* __restrict__ logA, const u16* __restrict__ Aim,
    const u16* __restrict__ Cre, const u16* __restrict__ Cim, const u16* __restrict__ logdt,
    const u16* __restrict__ w1, const u16* __restrict__ w2, char* __restrict__ ws) {
  __shared__ __attribute__((aligned(16))) char smem[PREP_SMEM];
  const int tid = threadIdx.x, lane = tid & 63;
  const int wid = __builtin_amdgcn_readfirstlane(tid >> 6);
  const int b = blockIdx.x;
  const int idx = lane & 15, g = lane >> 4;
  if (b >= 256) {
    const int bj = b - 256;
    const int job = bj < PREP_W1 ? 0 : (bj < PREP_W1 + PREP_W2 ? 1 : 2);
    const int jb = job == 0 ? bj : (job == 1 ? bj - PREP_W1 : bj - PREP_W1 - PREP_W2);
    u4 v[4];
    int lofs;
    int ls1, ls2;
    {
      const u16* src;
      int gs1, gs2;
      if (job == 0) {
        src = w1 + (size_t)(tid >> 2) * FF + jb * 32 + (tid & 3) * 8;
        gs1 = 128 * FF; gs2 = 256 * FF; lofs = tid * 16; ls1 = 8192; ls2 = 16384;
      } else if (job == 1) {
        src = w2 + (size_t)((jb >> 2) * 128 + (tid >> 4)) * DM + (jb & 3) * 128 + (tid & 15) * 8;
        gs1 = 32 * DM; gs2 = 64 * DM; lofs = (tid >> 4) * 256 + (((tid & 15) ^ (((tid >> 4) & 7) << 1)) * 16); ls1 = 8192; ls2 = 16384;
      } else {
        const int r0 = (wid >> 2) * 8 + (wid & 3);
        src = x + (size_t)(jb * 32 + r0) * DM + lane * 8;
        gs1 = 4 * DM; gs2 = 16 * DM; lofs = r0 * 1024 + ((lane ^ (wid << 1)) * 16); ls1 = 4096; ls2 = 16384;
      }
      v[0] = *(const u4*)(src); v[1] = *(const u4*)(src + gs1); v[2] = *(const u4*)(src + gs2); v[3] = *(const u4*)(src + gs1 + gs2);
    }
    *(u4*)(smem + lofs) = v[0];
    *(u4*)(smem + lofs + ls1) = v[1];
    *(u4*)(smem + lofs + ls2) = v[2];
    *(u4*)(smem + lofs + ls1 + ls2) = v[3];
    __syncthreads();
    const int q = (lane >> 2) & 3, p = lane & 3;
    float* csr = (float*)(smem + 32768);
    if (job == 0) {
      const int h = lane >> 5, blk = (lane >> 4) & 1;
      const char* rb = smem + (wid * 64 + 8 * h + q) * 64 + blk * 32 + p * 8;
      u16* W1F = (u16*)(ws + WS_W1F);
      float cs = 0.f;
#pragma unroll
      for (int i = 0; i < 4; ++i) {
        s8 o = cat8(tr_read(rb + i * 1024), tr_read(rb + i * 1024 + 256));
        u4 ou = __builtin_bit_cast(u4, o);
#pragma unroll
        for (int j = 0; j < 4; ++j) cs = dot2(ou[j], BF_ONES, cs);
        st16(W1F + ((size_t)(jb * 32 + wid * 4 + i) * 64 + lane) * 8, ou);
      }
      cs += __shfl_xor(cs, 32);
      if (lane < 32) csr[wid * 32 + lane] = cs;
    } else if (job == 1) {
      const int rl = 16 * (g >> 1) + 4 * (g & 1) + q;
      const int sw = rl & 7;
      u16* W2F = (u16*)(ws + WS_W2F);
#pragma unroll
      for (int i = 0; i < 4; ++i) {
        const int nt = (wid & 1) * 4 + i;
        const char* rb = smem + ((wid >> 1) * 32 + rl) * 256 + ((nt ^ sw) * 32) + p * 8;
        s8 o = cat8(tr_read(rb), tr_read(rb + 2048));
        st16(W2F + ((size_t)(((jb & 3) * 8 + nt) * 56 + (jb >> 2) * 4 + (wid >> 1)) * 64 + lane) * 8, __builtin_bit_cast(u4, o));
      }
    } else if (job == 2) {
      const int sw = q | ((g & 1) << 2);
      u16* xT = (u16*)(ws + WS_XT0);
#pragma unroll
      for (int i = 0; i < 4; ++i) {
        int dt_ = wid * 4 + i;
        const char* base = smem + (8 * g + q) * 1024 + ((dt_ ^ sw) * 32) + 8 * p;
        s8 o = cat8(tr_read(base), tr_read(base + 4096));
        *(s8*)(xT + xoff(dt_ * 16 + idx, jb * 32 + 8 * g)) = o;
      }
      float sv[4], ssv[4];
#pragma unroll
      for (int i = 0; i < 4; ++i) {
        float s = 0.f, ss = 0.f;
#pragma unroll
        for (int j = 0; j < 4; ++j) { s = dot2(v[i][j], BF_ONES, s); ss = dot2(v[i][j], v[i][j], ss); }
        sv[i] = row_sum16(s); ssv[i] = row_sum16(ss);
      }
      float val = sv[0];
#pragma unroll
      for (int i = 1; i < 4; ++i) val = (idx == 2 * i) ? sv[i] : val;
#pragma unroll
      for (int i = 0; i < 4; ++i) val = (idx == 2 * i + 1) ? ssv[i] : val;
      val += __shfl_xor(val, 16);
      val += __shfl_xor(val, 32);
      if (lane < 8) {
        const int i = lane >> 1;
        const int r = jb * 32 + (i >> 1) * 16 + (wid >> 2) * 8 + (i & 1) * 4 + (wid & 3);
        ((double*)(ws + WS_ST0))[r * 2 + (lane & 1)] = (double)val;
        ((double*)(ws + WS_ST2))[r * 2 + (lane & 1)] = 0.0;
      }
    }

    if (job == 0) {
      __syncthreads();
      if (tid < 32) {
        float cs = 0.f;
#pragma unroll
        for (int i = 0; i < 8; ++i) cs += csr[i * 32 + tid];
        ((float*)(ws + WS_C1))[jb * 32 + tid] = cs;
      }
    }
    if (bj == 0 && tid >= 64 && tid < 96) ((unsigned*)(ws + WS_CNT))[(tid - 64) * 32] = 0u;
    if (bj < 16) {
      unsigned long long a0, a1, a2;
      asm volatile("s_getpc_b64 s[20:21]\n\ts_add_u32 s20, s20, _Z11conv_kernelPKtPKdS0_S0_PKfS0_Pt@rel32@lo+4\n\ts_addc_u32 s21, s21, _Z11conv_kernelPKtPKdS0_S0_PKfS0_Pt@rel32@hi+12\n\ts_mov_b64 %0, s[20:21]" : "=s"(a0) :: "s20", "s21", "scc");
      asm volatile("s_getpc_b64 s[20:21]\n\ts_add_u32 s20, s20, _Z10ffn_kernelILb0EEvPKtS1_PKfPtPfS1_PjS4_Pd@rel32@lo+4\n\ts_addc_u32 s21, s21, _Z10ffn_kernelILb0EEvPKtS1_PKfPtPfS1_PjS4_Pd@rel32@hi+12\n\ts_mov_b64 %0, s[20:21]" : "=s"(a1) :: "s20", "s21", "scc");
      asm volatile("s_getpc_b64 s[20:21]\n\ts_add_u32 s20, s20, _Z10ffn_kernelILb1EEvPKtS1_PKfPtPfS1_PjS4_Pd@rel32@lo+4\n\ts_addc_u32 s21, s21, _Z10ffn_kernelILb1EEvPKtS1_PKfPtPfS1_PjS4_Pd@rel32@hi+12\n\ts_mov_b64 %0, s[20:21]" : "=s"(a2) :: "s20", "s21", "scc");
      const char* p0 = tid < 76 ? (const char*)a0 + tid * 64 : (tid < 276 ? (const char*)a1 + (tid - 76) * 64 : (const char*)a2 + (tid < 468 ? tid - 276 : 0) * 64);
      unsigned t0_;
      asm volatile("global_load_dword %0, %1, off\n\ts_waitcnt vmcnt(0)" : "=&v"(t0_) : "v"(p0) : "memory");
    }
    return;
  }
  const int ch = wid >> 2, w4 = wid & 3, t8 = tid & 255;
  const int d = b * 2 + ch;
  const int pn = d * NS + (t8 & (NS - 1));
  const u16 pl0 = logdt[d], pl1 = logA[pn], pl2 = Aim[pn], pl3 = Cre[pn], pl4 = Cim[pn];
  SCHED;
  int kidx[8];
#pragma unroll
  for (int j = 0; j < 8; ++j) {
    float pf = rbf((float)(t8 * 8 + j));
    int p = (int)pf;
    int e = (int)(__float_as_uint(pf) >> 23) - (127 + 7);
    e = e < 0 ? 0 : (e > 3 ? 3 : e);
    int ai_ = ((((p >> 7) << 3) + (p & 7)) >> e) + 8 * e;
    kidx[j] = ch * PK_SZ + PK_KV + (ai_ * 16 + ((p & 127) >> 3)) * 4;
    asm volatile("" : "+v"(kidx[j]));
  }
  char* ks = smem + ch * PK_SZ;
  f4* par = (f4*)(ks + PK_PAR);
  f2* parc = (f2*)(ks + PK_PARC);
  f4* red = (f4*)(ks + PK_RED);
  float* kv = (float*)(ks + PK_KV);
  float* mc = (float*)(ks + PK_MX);
  SCHED;
  {
    float dt = rbf(exp_acc(bf2f(pl0)));
    float Ar = -rbf(exp_acc(bf2f(pl1)));
    float ar = Ar * dt;
    float ai = bf2f(pl2) * dt;
    const float I2PH = 0.15915494f;
    const float I2PL = (float)(0.15915494309189535 - (double)0.15915494f);
    float fh = ai * I2PH;
    float fl = fmaf(ai, I2PH, -fh) + ai * I2PL;
    fh -= rintf(fh);
    float fhi = __uint_as_float(__float_as_uint(fh) & 0xfffff000u);
    float flo = (fh - fhi) + fl;
    float crv = 2.0f * bf2f(pl3) * dt;
    float civ = 2.0f * bf2f(pl4) * dt;
    float cm = row_sum16(fabsf(crv) + fabsf(civ));
    float p0 = ar * 1.44269504f;
    asm volatile("" : "+v"(p0), "+v"(fhi), "+v"(flo), "+v"(cm));
    if (w4 < 2) {
      par[t8] = f4{p0, fhi, flo, 0.f};
      parc[t8] = f2{crv, civ};
      if (idx == 0) mc[t8 >> 4] = cm;
    }
  }
  __syncthreads();
  int ce;
  {
    f4 m0 = *(const f4*)(mc), m1 = *(const f4*)(mc + 4);
    float m = ((m0[0] + m0[1]) + (m0[2] + m0[3])) + ((m1[0] + m1[1]) + (m1[2] + m1[3]));
    ce = (int)((__float_as_uint(m) >> 23) & 0xff) - 127;
    ce = ce < -100 ? -100 : (ce > 100 ? 100 : ce);
  }
  {
    const f2 pos01 = {(float)(8 * idx), (float)alpha_of(idx)};
    const f2 pos23 = {(float)alpha_of(16 + idx), (float)alpha_of(32 + idx)};
    const f2 eo01 = {(float)(12 - ce), 13.f}, eo23 = {13.f, 13.f};
    f4 acc0 = {0, 0, 0, 0}, acc1 = {0, 0, 0, 0}, acc2 = {0, 0, 0, 0};
#pragma unroll
    for (int k2 = 0; k2 < 2; ++k2) {
      u4 hi[4], lo[4];
#pragma unroll
      for (int e = 0; e < 4; ++e) {
        const int n = (w4 * 2 + k2) * 16 + 4 * g + e;
        const f4 pp = par[n];
        const f2 pc = parc[n];
        f2 t01 = pos01 * pp[1], t23 = pos23 * pp[1];
        f2 r01 = {__builtin_amdgcn_fractf(t01[0]), __builtin_amdgcn_fractf(t01[1])};
        f2 r23 = {__builtin_amdgcn_fractf(t23[0]), __builtin_amdgcn_fractf(t23[1])};
        r01 = __builtin_elementwise_fma(pos01, f2{pp[2], pp[2]}, r01);
        r23 = __builtin_elementwise_fma(pos23, f2{pp[2], pp[2]}, r23);
        f2 a01 = __builtin_elementwise_fma(pos01, f2{pp[0], pp[0]}, eo01);
        f2 a23 = __builtin_elementwise_fma(pos23, f2{pp[0], pp[0]}, eo23);
        float rv[4] = {r01[0], r01[1], r23[0], r23[1]};
        float av[4] = {a01[0], a01[1], a23[0], a23[1]};
#pragma unroll
        for (int qq = 0; qq < 4; ++qq) {
          float ex = __builtin_amdgcn_exp2f(av[qq]);
          float cs = __builtin_amdgcn_cosf(rv[qq]), sn = __builtin_amdgcn_sinf(rv[qq]);
          f2 z;
          if (qq == 0) {
            f2 t = f2{cs, sn} * pc[0];
            t = __builtin_elementwise_fma(f2{-sn, cs}, f2{pc[1], pc[1]}, t);
            z = t * ex;
          } else {
            z = f2{cs, -sn} * ex;
          }
          asm("" : "+v"(z) : "v"(rv[qq]), "v"(av[qq]));
          unsigned hh, ll;
          split_h(z[0], z[1], hh, ll);
          hi[qq][e] = hh; lo[qq][e] = ll;
        }
      }
      const h8 bh = __builtin_bit_cast(h8, hi[0]), bl = __builtin_bit_cast(h8, lo[0]);
      acc0 = __builtin_amdgcn_mfma_f32_16x16x32_f16(__builtin_bit_cast(h8, lo[1]), bh, acc0, 0, 0, 0);
      acc1 = __builtin_amdgcn_mfma_f32_16x16x32_f16(__builtin_bit_cast(h8, lo[2]), bh, acc1, 0, 0, 0);
      acc2 = __builtin_amdgcn_mfma_f32_16x16x32_f16(__builtin_bit_cast(h8, lo[3]), bh, acc2, 0, 0, 0);
      acc0 = __builtin_amdgcn_mfma_f32_16x16x32_f16(__builtin_bit_cast(h8, hi[1]), bl, acc0, 0, 0, 0);
      acc1 = __builtin_amdgcn_mfma_f32_16x16x32_f16(__builtin_bit_cast(h8, hi[2]), bl, acc1, 0, 0, 0);
      acc2 = __builtin_amdgcn_mfma_f32_16x16x32_f16(__builtin_bit_cast(h8, hi[3]), bl, acc2, 0, 0, 0);
      acc0 = __builtin_amdgcn_mfma_f32_16x16x32_f16(__builtin_bit_cast(h8, hi[1]), bh, acc0, 0, 0, 0);
      acc1 = __builtin_amdgcn_mfma_f32_16x16x32_f16(__builtin_bit_cast(h8, hi[2]), bh, acc1, 0, 0, 0);
      acc2 = __builtin_amdgcn_mfma_f32_16x16x32_f16(__builtin_bit_cast(h8, hi[3]), bh, acc2, 0, 0, 0);
    }
    red[(w4 * 3 + 0) * 64 + lane] = acc0;
    red[(w4 * 3 + 1) * 64 + lane] = acc1;
    red[(w4 * 3 + 2) * 64 + lane] = acc2;
  }
  __syncthreads();
  if (w4 < 3) {
    f4 vv = red[(0 * 3 + w4) * 64 + lane] + red[(1 * 3 + w4) * 64 + lane] +
            (red[(2 * 3 + w4) * 64 + lane] + red[(3 * 3 + w4) * 64 + lane]);
#pragma unroll
    for (int r = 0; r < 4; ++r) kv[(16 * w4 + 4 * g + r) * 16 + idx] = vv[r] * (1.0f / 2048.0f);
  }
  if (t8 == 0) ((float*)(ws + WS_KSC))[d] = __uint_as_float((unsigned)(127 + ce - 14) << 23);
  __syncthreads();
  u4 hi, lo;
#pragma unroll
  for (int j = 0; j < 4; ++j) {
    if (KTERMS > 1) {
      unsigned hh, ll;
      split_h(*(const float*)(smem + kidx[2 * j]), *(const float*)(smem + kidx[2 * j + 1]), hh, ll);
      hi[j] = hh; lo[j] = ll;
    } else {
      hi[j] = pack_h(*(const float*)(smem + kidx[2 * j]), *(const float*)(smem + kidx[2 * j + 1]));
    }
  }
  st16(ws + WS_KH + ((size_t)d * SL + t8 * 8) * 2, hi);
  if (KTERMS > 1) st16(ws + WS_KL + ((size_t)d * SL + t8 * 8) * 2, lo);
}

constexpr int CV_U = 2064 * 2;
constexpr int CV_K = 2160 * 2;
constexpr int CV_OFF_K = 2 * CV_U;
constexpr int CV_OFF_Y = CV_OFF_K + 2 * KTERMS * CV_K;
constexpr int CV_Q = (SL + 4) * 4;
constexpr int CV_SMEM = CV_OFF_Y + 2 * 4 * CV_Q;

constexpr int CV_RPF = 2 + 2 * KTERMS;
constexpr int CV_R0 = (KTERMS == 2 ? 6 : 10) * CV_RPF;
constexpr int CV_NM = 68 * KTERMS;
struct CvStep { int a, n, k, first, op, arg, arg2; };
template <int SUB>
struct CvSched {
  CvStep s[CV_NM];
  int wt, wc, slack;
  constexpr CvSched() : s{}, wt(-1), wc(4), slack(1000) {
    const int AMAX = SUB == 0 ? 15 : 14;
    const int NR = (AMAX + 1) * CV_RPF;
    int nr = CV_R0, mi = 0;
    int endat[16] = {};
    for (int a = 0; a < 16; ++a) endat[a] = 1 << 20;
    for (int a = 0; a <= AMAX; ++a) {
      if ((((a + 1) >> 1) & 1) != SUB) continue;
      for (int nn = 0; nn <= a; ++nn) {
        const int n = (a == 0) ? 0 : (nn + 1 < a ? nn + 1 : (nn + 1 == a ? 0 : a));
        for (int k = 0; k < KTERMS; ++k) {
          CvStep st{a, n, k, (nn == 0 && k == 0) ? 1 : 0, 0, 0, 0};
          { const int need = ((n > a - n ? n : a - n) + 1) * CV_RPF; if (nr < NR && nr - need < slack) slack = nr - need; }
          ++mi;
          if (wc == 4) {
            for (int t = wt + 1; t < 16; ++t)
              if (endat[t] < (1 << 20)) { if (endat[t] + 8 <= mi) { wt = t; wc = 0; } break; }
          }
          if (wc < 4) { st.op = 2; st.arg = wt; st.arg2 = wc; ++wc; }
          else if (nr < NR) { st.op = 1; st.arg = nr; ++nr; }
          s[mi - 1] = st;
        }
      }
      endat[a] = mi;
    }
  }
};
template <int R>
__device__ __forceinline__ void cv_rd(s4 (&A)[16][KTERMS][2], s4 (&B)[16][2], const char* ub, const char* kb) {
  constexpr int i = R / CV_RPF, part = R % CV_RPF;
  if constexpr (part < 2) B[i][part] = tr_read(ub + 256 * i + 32 * part);
  else A[i][(part - 2) >> 1][part & 1] = tr_read(kb + ((part - 2) >> 1) * CV_K + 256 * i + 32 * (1 - (part & 1)));
}
template <int SUB, int I>
__device__ __forceinline__ void cv_step(s4 (&A)[16][KTERMS][2], s4 (&B)[16][2], f4 (&acc)[16], const char* ub, const char* kb, char* qb) {
  constexpr CvSched<SUB> S{};
  constexpr CvStep st = S.s[I];
  const s8 av = cat8(A[st.a - st.n][st.k][0], A[st.a - st.n][st.k][1]), bv = cat8(B[st.n][0], B[st.n][1]);
  if constexpr (st.first) asm volatile("v_mfma_f32_16x16x32_f16 %0, %1, %2, 0" : "=&v"(acc[st.a]) : "v"(av), "v"(bv));
  else asm volatile("v_mfma_f32_16x16x32_f16 %0, %1, %2, %0" : "+v"(acc[st.a]) : "v"(av), "v"(bv));
  if constexpr (st.op == 2) *(float*)(qb + st.arg2 * (CV_Q + 4) + st.arg * 512) = acc[st.arg][st.arg2];
  else if constexpr (st.op == 1) cv_rd<st.arg>(A, B, ub, kb);
  SCHED;
}
template <int SUB, int... Rs, int... Is>
__device__ __forceinline__ void conv_mfma_impl(const char* ub, const char* kb, char* qb, iseq<Rs...>, iseq<Is...>) {
  s4 A[16][KTERMS][2];
  s4 B[16][2];
  f4 acc[16];
  (cv_rd<Rs>(A, B, ub, kb), ...);
  SCHED;
  (cv_step<SUB, Is>(A, B, acc, ub, kb, qb), ...);
  asm volatile("s_nop 7");
  SCHED;
  constexpr CvSched<SUB> S{};
  static_assert(S.slack >= 4, "fragment read scheduled too late");
#pragma unroll
  for (int t = 0; t < 16; ++t) {
    if ((((t + 1) >> 1) & 1) != SUB) continue;
#pragma unroll
    for (int r = 0; r < 4; ++r)
      if (t > S.wt || (t == S.wt && r >= S.wc)) *(float*)(qb + r * (CV_Q + 4) + t * 512) = acc[t][r];
  }
}
template <int SUB>
__device__ __forceinline__ void conv_mfma(const char* ub, const char* kb, char* qb) {
  conv_mfma_impl<SUB>(ub, kb, qb, make_iseq<CV_R0>{}, make_iseq<CV_NM>{});
}

__global__ __launch_bounds__(512, 2) void conv_kernel(
    const u16* __restrict__ xT, const double* __restrict__ stats, const u16* __restrict__ Kh,
    const u16* __restrict__ Kl, const float* __restrict__ ksc, const u16* __restrict__ dskip,
    u16* __restrict__ outT) {
  extern __shared__ __attribute__((aligned(16))) char smem[];
  const int tid = threadIdx.x, lane = tid & 63;
  const int wid = __builtin_amdgcn_readfirstlane(tid >> 6);
  const int d0 = blockIdx.x * 2;
  const int t0 = tid * 4;
  u2 xr[2];
  u4 kr[KTERMS];
  d2 sr[4];
#pragma unroll
  for (int i = 0; i < 4; ++i) sr[i] = *(const d2*)(stats + t0 * 2 + i * 2);
#pragma unroll
  for (int c = 0; c < 2; ++c) xr[c] = *(const u2*)(xT + xoff(d0 + c, t0));
  kr[0] = *(const u4*)(Kh + (size_t)d0 * SL + tid * 8);
  if (KTERMS > 1) kr[KTERMS - 1] = *(const u4*)(Kl + (size_t)d0 * SL + tid * 8);
  const float2 isc2 = *(const float2*)(ksc + d0);
  const unsigned ds2 = *(const unsigned*)(dskip + d0);
  if (tid < 14 * 2 * KTERMS) {
    int which = tid / 14, j = tid % 14;
    *(u4*)(smem + CV_OFF_K + which * CV_K + j * 16) = u4{0, 0, 0, 0};
  } else if (tid >= 64 && tid < 70) {
    int c = (tid - 64) / 3, j = (tid - 64) % 3;
    *(u2*)(smem + c * CV_U + j * 8) = u2{0, 0};
  } else if (tid >= 128 && tid < 136) {
    *(f4*)(smem + CV_OFF_Y + (tid - 128) * CV_Q) = f4{0, 0, 0, 0};
  }
  float mb[4], ri[4];
#pragma unroll
  for (int j = 0; j < 4; ++j) ln_consts((float)sr[j][0], (float)sr[j][1], mb[j], ri[j]);
  u2 ub16[2];
#pragma unroll
  for (int c = 0; c < 2; ++c) {
    unsigned d01 = pack_bf(bflo(xr[c][0]) - mb[0], bfhi(xr[c][0]) - mb[1]);
    unsigned d23 = pack_bf(bflo(xr[c][1]) - mb[2], bfhi(xr[c][1]) - mb[3]);
    ub16[c][0] = pack_bf(bflo(d01) * ri[0], bfhi(d01) * ri[1]);
    ub16[c][1] = pack_bf(bflo(d23) * ri[2], bfhi(d23) * ri[3]);
    u2 a = {pack_h(bflo(ub16[c][0]), bfhi(ub16[c][0])), pack_h(bflo(ub16[c][1]), bfhi(ub16[c][1]))};
    *(u2*)(smem + c * CV_U + 24 + tid * 8) = a;
  }
#pragma unroll
  for (int k = 0; k < KTERMS; ++k)
    *(u4*)(smem + CV_OFF_K + ((tid >> 8) * KTERMS + k) * CV_K + 224 + (tid & 255) * 16) = kr[k];
  __syncthreads();
  {
    const int ch = wid >> 2, par = (wid >> 1) & 1;
    const int g = lane >> 4, q = (lane >> 2) & 3, p = lane & 3;
    const char* ub = smem + ch * CV_U + 8 * (p + q + 8 * g);
    const char* kb = smem + CV_OFF_K + ch * KTERMS * CV_K + 8 * (31 + 16 * par + 4 * p - 8 * g - q) - 32;
    char* qb = smem + CV_OFF_Y + ch * 4 * CV_Q + par * 256 + lane * 4;
    if (wid & 1) conv_mfma<1>(ub, kb, qb);
    else conv_mfma<0>(ub, kb, qb);
  }
  __syncthreads();
#pragma unroll
  for (int c = 0; c < 2; ++c) {
    const char* Q = smem + CV_OFF_Y + c * 4 * CV_Q + t0 * 4;
    f4 y0 = (*(const f4*)(Q) + *(const f4*)(Q + CV_Q)) + (*(const f4*)(Q + 2 * CV_Q) + *(const f4*)(Q + 3 * CV_Q));
    float isc = c ? isc2.y : isc2.x;
    float ds = c ? bfhi(ds2) : bflo(ds2);
    unsigned y01 = pack_bf(y0[0] * isc, y0[1] * isc), y23 = pack_bf(y0[2] * isc, y0[3] * isc);
    unsigned s01 = pack_bf(bflo(ub16[c][0]) * ds, bfhi(ub16[c][0]) * ds), s23 = pack_bf(bflo(ub16[c][1]) * ds, bfhi(ub16[c][1]) * ds);
    unsigned a01 = pack_bf(bflo(y01) + bflo(s01), bfhi(y01) + bfhi(s01)), a23 = pack_bf(bflo(y23) + bflo(s23), bfhi(y23) + bfhi(s23));
    u2 ov = {pack_bf(bflo(xr[c][0]) + bflo(a01), bfhi(xr[c][0]) + bfhi(a01)), pack_bf(bflo(xr[c][1]) + bflo(a23), bfhi(xr[c][1]) + bfhi(a23))};
    st8(outT + xoff(d0 + c, t0), ov);
  }
}

constexpr int G1_SMEM = 512 * 256 + 512 * 8;

__device__ __forceinline__ void glds16(const void* gsrc, unsigned lds_dst) {
  unsigned keep;
  asm volatile("s_mov_b32 %0, m0\n\ts_mov_b32 m0, %2\n\ts_nop 0\n\tglobal_load_lds_dwordx4 %1, off\n\ts_mov_b32 m0, %0"
               : "=&s"(keep) : "v"(gsrc), "s"(lds_dst) : "memory");
}
template <int OFF>
__device__ __forceinline__ void gload16(u4& dst, const void* p) {
  asm volatile("global_load_dwordx4 %0, %1, off offset:%2" : "=v"(dst) : "v"(p), "i"(OFF) : "memory");
}
template <int N>
__device__ __forceinline__ void wait_only(u4& a) {
  asm volatile("s_waitcnt vmcnt(%1)" : "+v"(a) : "i"(N) : "memory");
}
template <int N>
__device__ __forceinline__ void wait_vm() { asm volatile("s_waitcnt vmcnt(%0)" :: "i"(N) : "memory"); }
__device__ __forceinline__ void add1(float& a, float b) { asm("v_add_f32 %0, %0, %1" : "+v"(a) : "v"(b)); }
__device__ __forceinline__ void sqa1(float& a, float b) { asm("v_fmac_f32 %0, %1, %1" : "+v"(a) : "v"(b)); }
__device__ __forceinline__ void bar() { asm volatile("s_waitcnt lgkmcnt(0)\n\ts_barrier" ::: "memory"); }
constexpr int G1_ST = 4 * 32768;
constexpr int F_BPRE = 5;
constexpr int G1_XPF = 3;
constexpr int G1_WPF = 8;

template <int OFF>
__device__ __forceinline__ void gload16_sc1(u4& dst, const void* p) {
  asm volatile("global_load_dwordx4 %0, %1, off offset:%2 sc1" : "=v"(dst) : "v"(p), "i"(OFF) : "memory");
}
template <int N>
__device__ __forceinline__ void wait4(u4 (&a)[4]) {
  asm volatile("s_waitcnt vmcnt(%4)" : "+v"(a[0]), "+v"(a[1]), "+v"(a[2]), "+v"(a[3]) : "i"(N) : "memory");
}

template <bool FINAL>
__global__ __launch_bounds__(512, 2) void ffn_kernel(
    const u16* __restrict__ xT, const u16* __restrict__ W1F, const float* __restrict__ c1,
    u16* HF, float* pst, const u16* __restrict__ W2F, unsigned* cnt,
    u16* __restrict__ out, double* __restrict__ stats) {
  extern __shared__ __attribute__((aligned(16))) char smem[];
  const int tid = threadIdx.x, lane = tid & 63;
  const int wid = __builtin_amdgcn_readfirstlane(tid >> 6);
  const int bx = blockIdx.x, xj = bx & 7, r = bx >> 3;
  const int tp = 4 * (xj >> 1) + (r & 3);
  const int fp = 7 * (xj & 1) + (r >> 2);
  if (r < 28) {
  const int h = lane >> 5, blk = (lane >> 4) & 1, q = (lane >> 2) & 3, p = lane & 3;
  const char* rbase = smem + (8 * h + q) * 64 + blk * 32 + 8 * p;
  float* st = (float*)(smem + G1_ST);
  if (wid >= 4) {
    const int hq = wid - 4;
    const char* xsrc = (const char*)xT + (size_t)tp * 131072 + hq * 1024 + lane * 16;
    const unsigned ldst = (unsigned)(size_t)smem + hq * 1024;
    auto issue_c = [&](int c) AINL {
#pragma unroll
      for (int i = 0; i < 3; ++i) glds16(xsrc + c * 4096 + i * 32768, ldst + c * 4096 + i * 32768);
    };
#pragma unroll
    for (int c = 0; c < G1_XPF; ++c) issue_c(c);
#pragma unroll 1
    for (int c = 0; c < 8 - G1_XPF; ++c) {
      wait_vm<3 * (G1_XPF - 1)>(); bar();
      issue_c(c + G1_XPF);
    }
    sfor<G1_XPF>([&](auto I) AINL {
      constexpr int i = decltype(I)::value;
      if constexpr (i == 0) wait_vm<3 * (G1_XPF - 1)>(); else wait_vm<3 * (G1_XPF - 1 - i) + 8>();
      bar();
      if constexpr (i == 0) {
#pragma unroll
        for (int k = 0; k < 8; ++k) glds16(xsrc + 3 * 32768 + k * 4096, ldst + 3 * 32768 + k * 4096);
      }
    });
    wait_vm<0>();
    bar();
#pragma unroll 1
    for (int it = 0; it < 2; ++it) {
      const int u = it == 0 ? 12 + hq : (fp - 1) * 4 + hq;
      if (it == 1 && (fp < 1 || fp > 3)) break;
      const char* rq = rbase + u * 8192;
      s4 fr[8][2];
      sfor<8>([&](auto I) AINL {
        constexpr int i = decltype(I)::value;
        fr[i][0] = tr_read(rq + i * 1024);
        fr[i][1] = tr_read(rq + i * 1024 + 256);
      });
      float sa = 0.f, sb = 0.f, qa = 0.f, qb = 0.f;
      sfor<8>([&](auto I) AINL {
        constexpr int i = decltype(I)::value;
        u4 xv = __builtin_bit_cast(u4, cat8(fr[i][0], fr[i][1]));
#pragma unroll
        for (int j = 0; j < 4; ++j) {
          float lo = bflo(xv[j]), hi = bfhi(xv[j]);
          add1(sa, lo); add1(sb, hi); sqa1(qa, lo); sqa1(qb, hi);
        }
      });
      float s0 = sa + sb, ss0 = qa + qb;
      s0 += __shfl_xor(s0, 32);
      ss0 += __shfl_xor(ss0, 32);
      if (it == 0) {
        if (lane < 32) st[128 + hq * 32 + lane] = s0;
        bar();
      }
      if (lane < 32 && (it == 1 || fp == 0))
        st8(pst + ((tp * 128 + (u >> 2) * 32 + lane) * 4 + (u & 3)) * 2, u2{__float_as_uint(s0), __float_as_uint(ss0)});
    }
  } else {
  const int ft = fp * 4 + wid;
  u4 wf[32];
  u4 cfu[4];
  const char* wsrc = (const char*)W1F + ((size_t)ft * 32 * 64 + lane) * 16;
  auto issue_w = [&](auto S) AINL {
    constexpr int s = decltype(S)::value;
    gload16<(s & 3) * 1024>(wf[s], wsrc + (s >> 2) * 4096);
  };
  sfor<G1_WPF>(issue_w);
  f16v acc[4], asum;
  s4 xa[3][3][2];
  constexpr int XD = 4, XR = XD + 1;
  s4 xc[XR][2];
  u4 ov;
  float nm[4];
  const int w3 = wid == 3 ? 0 : wid;
  int tto[3];
#pragma unroll
  for (int i = 0; i < 3; ++i) { int tt = i + w3; tto[i] = tt >= 3 ? tt - 3 : tt; }
  const char* rb[3];
#pragma unroll
  for (int i = 0; i < 3; ++i) rb[i] = rbase + tto[i] * 32768;
  u16* dst = HF + ((size_t)((tp * 8 + blk) * 56 + ft) * 64 + (lane & 15) + 16 * h) * 8;
  auto epi_pair = [&](const f16v& a, float m, int e) AINL -> unsigned {
    f4 cf = __builtin_bit_cast(f4, cfu[e >> 2]);
    unsigned pu = pack_bf(fmaf(cf[e & 3], m, a[e]), fmaf(cf[(e & 3) + 1], m, a[e + 1]));
    asm("" : "+v"(pu));
    return __builtin_bit_cast(unsigned, __builtin_elementwise_max(__builtin_bit_cast(s2v, pu), s2v{0, 0}));
  };
  f16v z;
#pragma unroll
  for (int j = 0; j < 16; ++j) z[j] = 0.f;
  const s8 ones8 = {0x3f80, 0x3f80, 0x3f80, 0x3f80, 0x3f80, 0x3f80, 0x3f80, 0x3f80};
  SCHED;
  asm volatile("s_barrier" ::: "memory");
#pragma unroll
  for (int k = 0; k < 2; ++k)
#pragma unroll
    for (int i = 0; i < 3; ++i) { xa[k][i][0] = tr_read(rb[i] + k * 1024); xa[k][i][1] = tr_read(rb[i] + k * 1024 + 256); }
  SCHED;
  sfor<32>([&](auto S) AINL {
    constexpr int s = decltype(S)::value;
    constexpr int issued = (G1_WPF + s <= 32) ? G1_WPF + s : 36;
    asum = __builtin_amdgcn_mfma_f32_32x32x16_bf16(__builtin_bit_cast(b8, ones8), __builtin_bit_cast(b8, cat8(xa[s % 3][0][0], xa[s % 3][0][1])), s == 0 ? z : asum, 0, 0, 0);
    wait_only<issued - s - 1>(wf[s]);
    if constexpr ((s & 3) == 2 && s < 30) asm volatile("s_barrier" ::: "memory");
    SCHED;
    sfor<3>([&](auto I) AINL {
      constexpr int i = decltype(I)::value;
      const b8 xv = __builtin_bit_cast(b8, cat8(xa[s % 3][i][0], xa[s % 3][i][1]));
      acc[i] = __builtin_amdgcn_mfma_f32_32x32x16_bf16(__builtin_bit_cast(b8, wf[s]), xv, s == 0 ? z : acc[i], 0, 0, 0);
      if constexpr (s + 2 < 32) {
        xa[(s + 2) % 3][i][0] = tr_read(rb[i] + (s + 2) * 1024);
        xa[(s + 2) % 3][i][1] = tr_read(rb[i] + (s + 2) * 1024 + 256);
      }
      if constexpr (i == 0) {
        if constexpr (G1_WPF + s < 32) issue_w(ic<(G1_WPF + s < 32) ? G1_WPF + s : 0>{});
        if constexpr (G1_WPF + s == 32) {
          gload16<0>(cfu[0], c1 + ft * 32 + 4 * h);
          gload16<32>(cfu[1], c1 + ft * 32 + 4 * h);
          gload16<64>(cfu[2], c1 + ft * 32 + 4 * h);
          gload16<96>(cfu[3], c1 + ft * 32 + 4 * h);
        }
      }
      SCHED;
    });
  });
  if (lane < 32) st[wid * 32 + lane] = -rbf(asum[0] * (1.0f / DM));
  asm volatile("s_waitcnt vmcnt(0) lgkmcnt(0)\n\ts_barrier" : "+v"(cfu[0]), "+v"(cfu[1]), "+v"(cfu[2]), "+v"(cfu[3]) :: "memory");
  {
    unsigned rbo = (unsigned)(rbase - smem) + 3 * 32768;
    asm volatile("" : "+v"(rbo));
    const char* rbb = smem + rbo;
    sfor<XD>([&](auto I) AINL {
      constexpr int i = decltype(I)::value;
      xc[i][0] = tr_read(rbb + i * 1024); xc[i][1] = tr_read(rbb + i * 1024 + 256);
    });
#pragma unroll
    for (int i = 0; i < 3; ++i) nm[i] = st[tto[i] * 32 + (lane & 31)];
    SCHED;
    sfor<32>([&](auto S) AINL {
      constexpr int s = decltype(S)::value;
      const b8 xv = __builtin_bit_cast(b8, cat8(xc[s % XR][0], xc[s % XR][1]));
      acc[3] = __builtin_amdgcn_mfma_f32_32x32x16_bf16(__builtin_bit_cast(b8, wf[s]), xv, s == 0 ? z : acc[3], 0, 0, 0);
      if constexpr (s + XD < 32) {
        xc[(s + XD) % XR][0] = tr_read(rbb + (s + XD) * 1024);
        xc[(s + XD) % XR][1] = tr_read(rbb + (s + XD) * 1024 + 256);
      }
      if constexpr (s < 24) {
        constexpr int i = s >> 3, pr = s & 7;
        ov[pr & 3] = epi_pair(acc[i], nm[i], 2 * pr);
        if constexpr ((pr & 3) == 3) st16(dst + tto[i] * (2 * 56 * 512) + (pr >> 2) * 256, ov);
      }
      SCHED;
    });
  }
  asm volatile("s_barrier" ::: "memory");
  {
    const float* sp = st + 128 + (lane & 31);
    nm[3] = -rbf(((sp[0] + sp[32]) + (sp[64] + sp[96])) * (1.0f / DM));
  }
#pragma unroll
  for (int c = 0; c < 2; ++c) {
#pragma unroll
    for (int j = 0; j < 4; ++j) ov[j] = epi_pair(acc[3], nm[3], 8 * c + 2 * j);
    st16(dst + 3 * (2 * 56 * 512) + c * 256, ov);
  }
  }
  }
  const int tb = 8 * (xj >> 1) + (r & 7), db = 4 * (xj & 1) + (r >> 3);
  const int g = lane >> 4, n = lane & 15;
  const int mo = wid >> 1, no = 2 * (wid & 1);
  u4 bf[7][4], af[7][4];
  {
    const char* bp[4];
#pragma unroll
    for (int j = 0; j < 4; ++j) bp[j] = (const char*)W2F + ((size_t)((db * 4 + ((j + no) & 3)) * 56 + wid * 7) * 64 + lane) * 16;
    auto issue_b = [&](auto KS) AINL {
      constexpr int ks = decltype(KS)::value;
      sfor<4>([&](auto J) AINL {
        constexpr int j = decltype(J)::value;
        gload16<(ks & 3) * 1024>(bf[ks][j], bp[j] + (ks >> 2) * 4096);
      });
    };
    wait_vm<0>();
    bar();
    if (r < 28 && tid == 0) __hip_atomic_fetch_add(cnt + tp * 32, 1u, __ATOMIC_RELAXED, __HIP_MEMORY_SCOPE_AGENT);
    if (wid == 7) {
      const unsigned* cp = cnt + (tb >> 1) * 32;
      for (int spin = 0; spin < (1 << 22); ++spin) {
        if (__hip_atomic_load(cp, __ATOMIC_RELAXED, __HIP_MEMORY_SCOPE_AGENT) >= 14u) break;
        __builtin_amdgcn_s_sleep(2);
      }
    }
    sfor<F_BPRE>(issue_b);
    bar();
    sfor<7 - F_BPRE>([&](auto I) AINL { issue_b(ic<F_BPRE + decltype(I)::value>{}); });
  }
  u2 res[2];
  u16 resf[2][4];
  if (!FINAL) {
#pragma unroll
    for (int j = 0; j < 2; ++j) res[j] = *(const u2*)(xT + xoff(db * 64 + (no + j) * 16 + n, tb * 64 + mo * 16 + 4 * g));
  } else {
#pragma unroll
    for (int j = 0; j < 2; ++j)
#pragma unroll
      for (int e = 0; e < 4; ++e) resf[j][e] = xT[xoff(db * 64 + (no + j) * 16 + 4 * g + e, tb * 64 + mo * 16 + n)];
  }
  const unsigned long long psu = __hip_atomic_load((const unsigned long long*)(pst + ((tb * 64 + mo * 16 + n) * 4 + g) * 2), __ATOMIC_RELAXED, __HIP_MEMORY_SCOPE_AGENT);
  const char* ap[4];
#pragma unroll
  for (int i = 0; i < 4; ++i) ap[i] = (const char*)HF + ((size_t)((tb * 4 + ((i + mo) & 3)) * 56 + wid * 7) * 64 + lane) * 16;
  auto issue_a = [&](auto KS) AINL {
    constexpr int ks = decltype(KS)::value;
    sfor<4>([&](auto I) AINL {
      constexpr int i = decltype(I)::value;
      gload16_sc1<(ks & 3) * 1024>(af[ks][i], ap[i] + (ks >> 2) * 4096);
    });
  };
  sfor<4>(issue_a);
  f4 acc[4][4];
#pragma unroll
  for (int i = 0; i < 4; ++i)
#pragma unroll
    for (int j = 0; j < 4; ++j) acc[i][j] = f4{0, 0, 0, 0};
  SCHED;
  sfor<7>([&](auto KS) AINL {
    constexpr int ks = decltype(KS)::value;
    wait4<(ks < 4 ? 12 : 4 * (6 - ks))>(af[ks]);
    wait4<63>(bf[ks]);
    SCHED;
#pragma unroll
    for (int i = 0; i < 4; ++i)
#pragma unroll
      for (int j = 0; j < 4; ++j) {
        if (!FINAL) acc[i][j] = __builtin_amdgcn_mfma_f32_16x16x32_bf16(__builtin_bit_cast(b8, af[ks][i]), __builtin_bit_cast(b8, bf[ks][j]), acc[i][j], 0, 0, 0);
        else acc[i][j] = __builtin_amdgcn_mfma_f32_16x16x32_bf16(__builtin_bit_cast(b8, bf[ks][j]), __builtin_bit_cast(b8, af[ks][i]), acc[i][j], 0, 0, 0);
      }
    SCHED;
    if constexpr (ks + 4 < 7) issue_a(ic<(ks + 4 < 7) ? ks + 4 : 0>{});
    SCHED;
  });
  f4* red = (f4*)smem;
#pragma unroll
  for (int i = 0; i < 4; ++i)
#pragma unroll
    for (int jp = 0; jp < 2; ++jp) {
      if (i == 0 && jp == 0) continue;
      const int owner = ((i + mo) & 3) * 2 + ((jp + wid) & 1);
      red[((owner * 7 + i * 2 + jp - 1) * 2 + 0) * 64 + lane] = acc[i][2 * jp];
      red[((owner * 7 + i * 2 + jp - 1) * 2 + 1) * 64 + lane] = acc[i][2 * jp + 1];
    }
  f4 rr;
  {
    float sx = __uint_as_float((unsigned)psu), sy = __uint_as_float((unsigned)(psu >> 32));
    sx += __shfl_xor(sx, 16); sy += __shfl_xor(sy, 16);
    sx += __shfl_xor(sx, 32); sy += __shfl_xor(sy, 32);
    float mb, ri;
    ln_consts(sx, sy, mb, ri);
    if (FINAL) rr = f4{ri, ri, ri, ri};
    else {
#pragma unroll
      for (int e = 0; e < 4; ++e) rr[e] = __shfl(ri, 4 * g + e);
    }
  }
  bar();
  float s[4] = {0, 0, 0, 0}, ss[4] = {0, 0, 0, 0};
#pragma unroll
  for (int j = 0; j < 2; ++j) {
    const f4* rp = red + (wid * 14 + j) * 64 + lane;
    f4 v = ((acc[0][j] + rp[0]) + (rp[128] + rp[256])) + ((rp[384] + rp[512]) + (rp[640] + rp[768]));
    v *= rr;
    unsigned m01 = pack_bf(v[0], v[1]), m23 = pack_bf(v[2], v[3]);
    u2 ov;
    if (!FINAL) {
      ov[0] = pack_bf(bflo(res[j][0]) + bflo(m01), bfhi(res[j][0]) + bfhi(m01));
      ov[1] = pack_bf(bflo(res[j][1]) + bflo(m23), bfhi(res[j][1]) + bfhi(m23));
      *(u2*)(out + xoff(db * 64 + (no + j) * 16 + n, tb * 64 + mo * 16 + 4 * g)) = ov;
      float o[4] = {bflo(ov[0]), bfhi(ov[0]), bflo(ov[1]), bfhi(ov[1])};
#pragma unroll
      for (int e = 0; e < 4; ++e) { s[e] += o[e]; ss[e] = fmaf(o[e], o[e], ss[e]); }
    } else {
      ov[0] = pack_bf(bf2f(resf[j][0]) + bflo(m01), bf2f(resf[j][1]) + bfhi(m01));
      ov[1] = pack_bf(bf2f(resf[j][2]) + bflo(m23), bf2f(resf[j][3]) + bfhi(m23));
      *(u2*)(out + (size_t)(tb * 64 + mo * 16 + n) * DM + db * 64 + (no + j) * 16 + 4 * g) = ov;
    }
  }
  if (!FINAL) {
#pragma unroll
    for (int e = 0; e < 4; ++e) { s[e] = row_sum16(s[e]); ss[e] = row_sum16(ss[e]); }
    if (n < 8) {
      int e = n >> 1;
      float v = (n & 1) ? (e == 0 ? ss[0] : e == 1 ? ss[1] : e == 2 ? ss[2] : ss[3]) : (e == 0 ? s[0] : e == 1 ? s[1] : e == 2 ? s[2] : s[3]);
      unsafeAtomicAdd(stats + (tb * 64 + mo * 16 + 4 * g) * 2 + n, (double)v);
    }
  }
}

extern "C" void kernel_launch(void* const* d_in, const int* in_sizes, int n_in,
                              void* d_out, int out_size, void* d_ws, size_t ws_size,
                              hipStream_t stream) {
  const u16* x = (const u16*)d_in[0];
  const u16* logA = (const u16*)d_in[1];
  const u16* Aim = (const u16*)d_in[2];
  const u16* Cre = (const u16*)d_in[3];
  const u16* Cim = (const u16*)d_in[4];
  const u16* logdt = (const u16*)d_in[5];
  const u16* dskip = (const u16*)d_in[6];
  const u16* w1 = (const u16*)d_in[8];
  const u16* w2 = (const u16*)d_in[9];
  char* ws = (char*)d_ws;
  u16* out = (u16*)d_out;
  (void)hipFuncSetAttribute((const void*)ffn_kernel<false>, hipFuncAttributeMaxDynamicSharedMemorySize, G1_SMEM);
  (void)hipFuncSetAttribute((const void*)ffn_kernel<true>, hipFuncAttributeMaxDynamicSharedMemorySize, G1_SMEM);
  (void)hipFuncSetAttribute((const void*)conv_kernel, hipFuncAttributeMaxDynamicSharedMemorySize, CV_SMEM);

  prep_kernel<<<256 + PREP_W1 + PREP_W2 + PREP_TR, 512, 0, stream>>>(x, logA, Aim, Cre, Cim, logdt, w1, w2, ws);
  conv_kernel<<<256, 512, CV_SMEM, stream>>>((const u16*)(ws + WS_XT0), (const double*)(ws + WS_ST0), (const u16*)(ws + WS_KH),
                                             (const u16*)(ws + WS_KL), (const float*)(ws + WS_KSC), dskip, (u16*)(ws + WS_XT1));
  ffn_kernel<false><<<256, 512, G1_SMEM, stream>>>((const u16*)(ws + WS_XT1), (const u16*)(ws + WS_W1F), (const float*)(ws + WS_C1),
                                                   (u16*)(ws + WS_HF), (float*)(ws + WS_PST), (const u16*)(ws + WS_W2F),
                                                   (unsigned*)(ws + WS_CNT), (u16*)(ws + WS_XT2), (double*)(ws + WS_ST2));
  conv_kernel<<<256, 512, CV_SMEM, stream>>>((const u16*)(ws + WS_XT2), (const double*)(ws + WS_ST2), (const u16*)(ws + WS_KH),
                                             (const u16*)(ws + WS_KL), (const float*)(ws + WS_KSC), dskip, (u16*)(ws + WS_XT3));
  ffn_kernel<true><<<256, 512, G1_SMEM, stream>>>((const u16*)(ws + WS_XT3), (const u16*)(ws + WS_W1F), (const float*)(ws + WS_C1),
                                                  (u16*)(ws + WS_HF2), (float*)(ws + WS_PST2), (const u16*)(ws + WS_W2F),
                                                  (unsigned*)(ws + WS_CNT) + 512, out, nullptr);
}
```

```cpp
#include <hip/hip_runtime.h>
#include <hip/hip_bf16.h>

typedef unsigned short u16;
typedef short s4 __attribute__((ext_vector_type(4)));
typedef short s2v __attribute__((ext_vector_type(2)));
typedef short s8 __attribute__((ext_vector_type(8)));
typedef _Float16 h8 __attribute__((ext_vector_type(8)));
typedef _Float16 h2 __attribute__((ext_vector_type(2)));
typedef __bf16 b2 __attribute__((ext_vector_type(2)));
typedef __bf16 b8 __attribute__((ext_vector_type(8)));
typedef float f4 __attribute__((ext_vector_type(4)));
typedef float f2 __attribute__((ext_vector_type(2)));
typedef double d2 __attribute__((ext_vector_type(2)));
typedef __bf16 b4 __attribute__((ext_vector_type(4)));
typedef float f16v __attribute__((ext_vector_type(16)));
typedef unsigned u4 __attribute__((ext_vector_type(4)));
typedef unsigned u2 __attribute__((ext_vector_type(2)));
typedef __attribute__((address_space(3))) s4* lds_s4p;

#define SCHED __builtin_amdgcn_sched_barrier(0)
template <int... Is> struct iseq {};
template <int N, int... Is> struct mk_iseq : mk_iseq<N - 1, N - 1, Is...> {};
template <int... Is> struct mk_iseq<0, Is...> { typedef iseq<Is...> type; };
template <int N> using make_iseq = typename mk_iseq<N>::type;
template <int I> struct ic { static constexpr int value = I; };
template <class F, int... Is> __device__ __forceinline__ void sfor_impl(F&& f, iseq<Is...>) { (f(ic<Is>{}), ...); }
template <int N, class F> __device__ __forceinline__ void sfor(F&& f) { sfor_impl(f, make_iseq<N>{}); }
#define AINL __attribute__((always_inline))

constexpr int SL = 2048;
constexpr int DM = 512;
constexpr int NS = 128;
constexpr int FF = 1792;
#define KTERMS 1

constexpr size_t WS_XT0 = 0;
constexpr size_t WS_XT1 = WS_XT0 + (size_t)SL * DM * 2;
constexpr size_t WS_XT2 = WS_XT1 + (size_t)SL * DM * 2;
constexpr size_t WS_XT3 = WS_XT2 + (size_t)SL * DM * 2;
constexpr size_t WS_KH  = WS_XT3 + (size_t)SL * DM * 2;
constexpr size_t WS_KL  = WS_KH + (size_t)SL * DM * 2;
constexpr size_t WS_W1F = WS_KL + (size_t)SL * DM * 2;
constexpr size_t WS_W2F = WS_W1F + (size_t)DM * FF * 2;
constexpr size_t WS_HF  = WS_W2F + (size_t)DM * FF * 2;
constexpr size_t WS_HF2 = WS_HF + (size_t)SL * FF * 2;
constexpr size_t WS_ST0 = WS_HF2 + (size_t)SL * FF * 2;
constexpr size_t WS_ST2 = WS_ST0 + (size_t)SL * 16;
constexpr size_t WS_C1  = WS_ST2 + (size_t)SL * 16;
constexpr size_t WS_KSC = WS_C1 + (size_t)FF * 4;
constexpr size_t WS_PST = WS_KSC + (size_t)DM * 4;
constexpr size_t WS_PST2 = WS_PST + (size_t)SL * 32;
constexpr size_t WS_CNT = WS_PST2 + (size_t)SL * 32;

__host__ __device__ __forceinline__ int xoff(int d, int t) { return (t >> 5) * 16384 + d * 32 + (t & 31); }
__device__ __forceinline__ float bf2f(unsigned h) { return __uint_as_float(h << 16); }
__device__ __forceinline__ float bflo(unsigned w) { return __uint_as_float(w << 16); }
__device__ __forceinline__ float bfhi(unsigned w) { return __uint_as_float(w & 0xffff0000u); }
__device__ __forceinline__ unsigned pack_bf(float a, float b) {
  b2 v = {(__bf16)a, (__bf16)b};
  return __builtin_bit_cast(unsigned, v);
}
__device__ __forceinline__ float rbf(float f) {
  return __uint_as_float(((unsigned)__builtin_bit_cast(u16, (__bf16)f)) << 16);
}
__device__ __forceinline__ unsigned pack_h(float a, float b) {
  h2 v = {(_Float16)a, (_Float16)b};
  return __builtin_bit_cast(unsigned, v);
}
__device__ __forceinline__ float dot2(unsigned a, unsigned b, float c) {
  return __builtin_amdgcn_fdot2_f32_bf16(__builtin_bit_cast(b2, a), __builtin_bit_cast(b2, b), c, false);
}
__device__ __forceinline__ s4 tr_read(const char* p) {
  return __builtin_amdgcn_ds_read_tr16_b64_v4i16((lds_s4p)p);
}
__device__ __forceinline__ s8 cat8(s4 a, s4 b) { return __builtin_shufflevector(a, b, 0, 1, 2, 3, 4, 5, 6, 7); }
template <int CTRL>
__device__ __forceinline__ float dppf(float v) {
  return __builtin_bit_cast(float, __builtin_amdgcn_update_dpp(0, __builtin_bit_cast(int, v), CTRL, 0xf, 0xf, true));
}
__device__ __forceinline__ float shr1(float v) { return dppf<0x138>(v); }
__device__ __forceinline__ float row_sum16(float v) {
  v += dppf<0x128>(v);
  v += dppf<0x124>(v);
  v += dppf<0x122>(v);
  v += dppf<0x121>(v);
  return v;
}
constexpr unsigned BF_ONES = 0x3f803f80u;
__device__ __forceinline__ void st16(void* p, u4 v) { asm volatile("global_store_dwordx4 %0, %1, off sc1\n\ts_nop 1" :: "v"(p), "v"(v) : "memory"); }
__device__ __forceinline__ void st8(void* p, u2 v) { asm volatile("global_store_dwordx2 %0, %1, off sc1\n\ts_nop 0" :: "v"(p), "v"(v) : "memory"); }

__device__ __forceinline__ void ln_consts(float s, float ss, float& mb, float& rinv) {
  float mean = s * (1.0f / DM);
  float var = fmaxf(ss * (1.0f / DM) - mean * mean, 0.0f);
  mb = rbf(mean);
  float sd = rbf(__builtin_amdgcn_sqrtf(rbf(rbf(var) + 1e-5f)));
  const float rc = __builtin_amdgcn_rcpf(sd);
  asm("s_nop 0\n\tv_mov_b32 %0, %1" : "=v"(rinv) : "v"(rc), "v"(sd));
}

#define CODE_ADDR(var, sym) asm volatile("s_getpc_b64 s[20:21]\n\ts_add_u32 s20, s20, " sym "@rel32@lo+4\n\ts_addc_u32 s21, s21, " sym "@rel32@hi+12\n\ts_mov_b64 %0, s[20:21]" : "=s"(var) :: "s20", "s21", "scc")
#define SYM_PREP "_Z11prep_kernelPKtS0_S0_S0_S0_S0_S0_S0_Pc"
#define SYM_CONV "_Z11conv_kernelPKtPKdS0_S0_PKfS0_Pt"
#define SYM_FFN0 "_Z10ffn_kernelILb0EEvPKtS1_PKfPtPfS1_PjS4_Pd"
#define SYM_FFN1 "_Z10ffn_kernelILb1EEvPKtS1_PKfPtPfS1_PjS4_Pd"
constexpr int CL_PREP = 128, CL_CONV = 76, CL_FFN0 = 200, CL_FFN1 = 192;
__device__ __forceinline__ void touch(const char* p) {
  unsigned t;
  asm volatile("global_load_dword %0, %1, off\n\ts_waitcnt vmcnt(0)" : "=&v"(t) : "v"(p) : "memory");
}

constexpr int PREP_W1 = 56, PREP_W2 = 56, PREP_TR = 64, PREP_PF = 16;
constexpr int PK_PAR = 0, PK_PARC = 2048, PK_RED = 3072, PK_KV = PK_RED + 12288, PK_MX = PK_KV + 3072, PK_SZ = PK_MX + 128;
constexpr int PREP_SMEM = (32768 + 1024 > 2 * PK_SZ) ? 32768 + 1024 : 2 * PK_SZ;

__device__ __forceinline__ int alpha_of(int idx) {
  if (idx < 16) return (idx >> 3) * 128 + (idx & 7);
  if (idx < 24) { int j = idx - 16; return (2 + (j >> 2)) * 128 + (j & 3) * 2; }
  if (idx < 32) { int j = idx - 24; return (4 + (j >> 1)) * 128 + (j & 1) * 4; }
  if (idx < 41) return (8 + idx - 32) * 128;
  return 0;
}
__device__ __forceinline__ float exp_acc(float x) {
  const float L2EH = 1.44269504f, L2EL = (float)(1.4426950408889634 - (double)1.44269504f);
  float t = x * L2EH;
  float e = fmaf(x, L2EH, -t) + x * L2EL;
  float r = __builtin_amdgcn_exp2f(t);
  float o = fmaf(r, e * 0.69314718f, r);
  asm("" : "+v"(o) : "v"(t));
  return o;
}
__device__ __forceinline__ float row_max16(float v) {
  v = fmaxf(v, dppf<0x128>(v));
  v = fmaxf(v, dppf<0x124>(v));
  v = fmaxf(v, dppf<0x122>(v));
  v = fmaxf(v, dppf<0x121>(v));
  return v;
}
__device__ __forceinline__ void split_h(float a, float b, unsigned& hi, unsigned& lo) {
  float ah = __uint_as_float(__float_as_uint(a) & 0xffffe000u), bh = __uint_as_float(__float_as_uint(b) & 0xffffe000u);
  hi = pack_h(ah, bh);
  lo = pack_h(a - ah, b - bh);
}

__global__ __launch_bounds__(512, 4) void prep_kernel(
    const u16* __restrict__ x, const u16* __restrict__ logA, const u16* __restrict__ Aim,
    const u16* __restrict__ Cre, const u16* __restrict__ Cim, const u16* __restrict__ logdt,
    const u16* __restrict__ w1, const u16* __restrict__ w2, char* __restrict__ ws) {
  __shared__ __attribute__((aligned(16))) char smem[PREP_SMEM];
  const int tid = threadIdx.x, lane = tid & 63;
  const int wid = __builtin_amdgcn_readfirstlane(tid >> 6);
  const int b = blockIdx.x;
  const int idx = lane & 15, g = lane >> 4;
  if (b >= 256 && b < 256 + PREP_PF) {
    unsigned long long a0, a1, a2, a3;
    CODE_ADDR(a0, SYM_PREP); CODE_ADDR(a1, SYM_CONV); CODE_ADDR(a2, SYM_FFN0); CODE_ADDR(a3, SYM_FFN1);
    if (b < 256 + 8) {
      const int u = tid < CL_PREP + CL_CONV + CL_FFN0 ? tid : 0;
      touch(u < CL_PREP ? (const char*)a0 + u * 64 : (u < CL_PREP + CL_CONV ? (const char*)a1 + (u - CL_PREP) * 64 : (const char*)a2 + (u - CL_PREP - CL_CONV) * 64));
    } else {
      touch((const char*)a3 + (tid < CL_FFN1 ? tid : 0) * 64);
    }
    return;
  }
  if (b >= 256) {
    const int bj = b - 256 - PREP_PF;
    const int job = bj < PREP_W1 ? 0 : (bj < PREP_W1 + PREP_W2 ? 1 : 2);
    const int jb = job == 0 ? bj : (job == 1 ? bj - PREP_W1 : bj - PREP_W1 - PREP_W2);
    u4 v[4];
    int lofs;
    int ls1, ls2;
    {
      const u16* src;
      int gs1, gs2;
      if (job == 0) {
        src = w1 + (size_t)(tid >> 2) * FF + jb * 32 + (tid & 3) * 8;
        gs1 = 128 * FF; gs2 = 256 * FF; lofs = tid * 16; ls1 = 8192; ls2 = 16384;
      } else if (job == 1) {
        src = w2 + (size_t)((jb >> 2) * 128 + (tid >> 4)) * DM + (jb & 3) * 128 + (tid & 15) * 8;
        gs1 = 32 * DM; gs2 = 64 * DM; lofs = (tid >> 4) * 256 + (((tid & 15) ^ (((tid >> 4) & 7) << 1)) * 16); ls1 = 8192; ls2 = 16384;
      } else {
        const int r0 = (wid >> 2) * 8 + (wid & 3);
        src = x + (size_t)(jb * 32 + r0) * DM + lane * 8;
        gs1 = 4 * DM; gs2 = 16 * DM; lofs = r0 * 1024 + ((lane ^ (wid << 1)) * 16); ls1 = 4096; ls2 = 16384;
      }
      v[0] = *(const u4*)(src); v[1] = *(const u4*)(src + gs1); v[2] = *(const u4*)(src + gs2); v[3] = *(const u4*)(src + gs1 + gs2);
    }
    *(u4*)(smem + lofs) = v[0];
    *(u4*)(smem + lofs + ls1) = v[1];
    *(u4*)(smem + lofs + ls2) = v[2];
    *(u4*)(smem + lofs + ls1 + ls2) = v[3];
    __syncthreads();
    const int q = (lane >> 2) & 3, p = lane & 3;
    float* csr = (float*)(smem + 32768);
    if (job == 0) {
      const int h = lane >> 5, blk = (lane >> 4) & 1;
      const char* rb = smem + (wid * 64 + 8 * h + q) * 64 + blk * 32 + p * 8;
      u16* W1F = (u16*)(ws + WS_W1F);
      float cs = 0.f;
#pragma unroll
      for (int i = 0; i < 4; ++i) {
        s8 o = cat8(tr_read(rb + i * 1024), tr_read(rb + i * 1024 + 256));
        u4 ou = __builtin_bit_cast(u4, o);
#pragma unroll
        for (int j = 0; j < 4; ++j) cs = dot2(ou[j], BF_ONES, cs);
        st16(W1F + ((size_t)(jb * 32 + wid * 4 + i) * 64 + lane) * 8, ou);
      }
      cs += __shfl_xor(cs, 32);
      if (lane < 32) csr[wid * 32 + lane] = cs;
    } else if (job == 1) {
      const int rl = 16 * (g >> 1) + 4 * (g & 1) + q;
      const int sw = rl & 7;
      u16* W2F = (u16*)(ws + WS_W2F);
#pragma unroll
      for (int i = 0; i < 4; ++i) {
        const int nt = (wid & 1) * 4 + i;
        const char* rb = smem + ((wid >> 1) * 32 + rl) * 256 + ((nt ^ sw) * 32) + p * 8;
        s8 o = cat8(tr_read(rb), tr_read(rb + 2048));
        st16(W2F + ((size_t)(((jb & 3) * 8 + nt) * 56 + (jb >> 2) * 4 + (wid >> 1)) * 64 + lane) * 8, __builtin_bit_cast(u4, o));
      }
    } else if (job == 2) {
      const int sw = q | ((g & 1) << 2);
      u16* xT = (u16*)(ws + WS_XT0);
#pragma unroll
      for (int i = 0; i < 4; ++i) {
        int dt_ = wid * 4 + i;
        const char* base = smem + (8 * g + q) * 1024 + ((dt_ ^ sw) * 32) + 8 * p;
        s8 o = cat8(tr_read(base), tr_read(base + 4096));
        *(s8*)(xT + xoff(dt_ * 16 + idx, jb * 32 + 8 * g)) = o;
      }
      float sv[4], ssv[4];
#pragma unroll
      for (int i = 0; i < 4; ++i) {
        float s = 0.f, ss = 0.f;
#pragma unroll
        for (int j = 0; j < 4; ++j) { s = dot2(v[i][j], BF_ONES, s); ss = dot2(v[i][j], v[i][j], ss); }
        sv[i] = row_sum16(s); ssv[i] = row_sum16(ss);
      }
      float val = sv[0];
#pragma unroll
      for (int i = 1; i < 4; ++i) val = (idx == 2 * i) ? sv[i] : val;
#pragma unroll
      for (int i = 0; i < 4; ++i) val = (idx == 2 * i + 1) ? ssv[i] : val;
      val += __shfl_xor(val, 16);
      val += __shfl_xor(val, 32);
      if (lane < 8) {
        const int i = lane >> 1;
        const int r = jb * 32 + (i >> 1) * 16 + (wid >> 2) * 8 + (i & 1) * 4 + (wid & 3);
        ((double*)(ws + WS_ST0))[r * 2 + (lane & 1)] = (double)val;
        ((double*)(ws + WS_ST2))[r * 2 + (lane & 1)] = 0.0;
      }
    }

    if (job == 0) {
      __syncthreads();
      if (tid < 32) {
        float cs = 0.f;
#pragma unroll
        for (int i = 0; i < 8; ++i) cs += csr[i * 32 + tid];
        ((float*)(ws + WS_C1))[jb * 32 + tid] = cs;
      }
    }
    if (bj == 0 && tid >= 64 && tid < 96) ((unsigned*)(ws + WS_CNT))[(tid - 64) * 32] = 0u;
    return;
  }
  const int ch = wid >> 2, w4 = wid & 3, t8 = tid & 255;
  const int d = b * 2 + ch;
  const int pn = d * NS + (t8 & (NS - 1));
  const u16 pl0 = logdt[d], pl1 = logA[pn], pl2 = Aim[pn], pl3 = Cre[pn], pl4 = Cim[pn];
  SCHED;
  int kidx[8];
#pragma unroll
  for (int j = 0; j < 8; ++j) {
    float pf = rbf((float)(t8 * 8 + j));
    int p = (int)pf;
    int e = (int)(__float_as_uint(pf) >> 23) - (127 + 7);
    e = e < 0 ? 0 : (e > 3 ? 3 : e);
    int ai_ = ((((p >> 7) << 3) + (p & 7)) >> e) + 8 * e;
    kidx[j] = ch * PK_SZ + PK_KV + (ai_ * 16 + ((p & 127) >> 3)) * 4;
    asm volatile("" : "+v"(kidx[j]));
  }
  char* ks = smem + ch * PK_SZ;
  f4* par = (f4*)(ks + PK_PAR);
  f2* parc = (f2*)(ks + PK_PARC);
  f4* red = (f4*)(ks + PK_RED);
  float* kv = (float*)(ks + PK_KV);
  float* mc = (float*)(ks + PK_MX);
  SCHED;
  {
    float dt = rbf(exp_acc(bf2f(pl0)));
    float Ar = -rbf(exp_acc(bf2f(pl1)));
    float ar = Ar * dt;
    float ai = bf2f(pl2) * dt;
    const float I2PH = 0.15915494f;
    const float I2PL = (float)(0.15915494309189535 - (double)0.15915494f);
    float fh = ai * I2PH;
    float fl = fmaf(ai, I2PH, -fh) + ai * I2PL;
    fh -= rintf(fh);
    float fhi = __uint_as_float(__float_as_uint(fh) & 0xfffff000u);
    float flo = (fh - fhi) + fl;
    float crv = 2.0f * bf2f(pl3) * dt;
    float civ = 2.0f * bf2f(pl4) * dt;
    float cm = row_sum16(fabsf(crv) + fabsf(civ));
    float p0 = ar * 1.44269504f;
    asm volatile("" : "+v"(p0), "+v"(fhi), "+v"(flo), "+v"(cm));
    if (w4 < 2) {
      par[t8] = f4{p0, fhi, flo, 0.f};
      parc[t8] = f2{crv, civ};
      if (idx == 0) mc[t8 >> 4] = cm;
    }
  }
  __syncthreads();
  int ce;
  {
    f4 m0 = *(const f4*)(mc), m1 = *(const f4*)(mc + 4);
    float m = ((m0[0] + m0[1]) + (m0[2] + m0[3])) + ((m1[0] + m1[1]) + (m1[2] + m1[3]));
    ce = (int)((__float_as_uint(m) >> 23) & 0xff) - 127;
    ce = ce < -100 ? -100 : (ce > 100 ? 100 : ce);
  }
  {
    const f2 pos01 = {(float)(8 * idx), (float)alpha_of(idx)};
    const f2 pos23 = {(float)alpha_of(16 + idx), (float)alpha_of(32 + idx)};
    const f2 eo01 = {(float)(12 - ce), 13.f}, eo23 = {13.f, 13.f};
    f4 acc0 = {0, 0, 0, 0}, acc1 = {0, 0, 0, 0}, acc2 = {0, 0, 0, 0};
#pragma unroll
    for (int k2 = 0; k2 < 2; ++k2) {
      u4 hi[4], lo[4];
#pragma unroll
      for (int e = 0; e < 4; ++e) {
        const int n = (w4 * 2 + k2) * 16 + 4 * g + e;
        const f4 pp = par[n];
        const f2 pc = parc[n];
        f2 t01 = pos01 * pp[1], t23 = pos23 * pp[1];
        f2 r01 = {__builtin_amdgcn_fractf(t01[0]), __builtin_amdgcn_fractf(t01[1])};
        f2 r23 = {__builtin_amdgcn_fractf(t23[0]), __builtin_amdgcn_fractf(t23[1])};
        r01 = __builtin_elementwise_fma(pos01, f2{pp[2], pp[2]}, r01);
        r23 = __builtin_elementwise_fma(pos23, f2{pp[2], pp[2]}, r23);
        f2 a01 = __builtin_elementwise_fma(pos01, f2{pp[0], pp[0]}, eo01);
        f2 a23 = __builtin_elementwise_fma(pos23, f2{pp[0], pp[0]}, eo23);
        float rv[4] = {r01[0], r01[1], r23[0], r23[1]};
        float av[4] = {a01[0], a01[1], a23[0], a23[1]};
#pragma unroll
        for (int qq = 0; qq < 4; ++qq) {
          float ex = __builtin_amdgcn_exp2f(av[qq]);
          float cs = __builtin_amdgcn_cosf(rv[qq]), sn = __builtin_amdgcn_sinf(rv[qq]);
          f2 z;
          if (qq == 0) {
            f2 t = f2{cs, sn} * pc[0];
            t = __builtin_elementwise_fma(f2{-sn, cs}, f2{pc[1], pc[1]}, t);
            z = t * ex;
          } else {
            z = f2{cs, -sn} * ex;
          }
          asm("" : "+v"(z) : "v"(rv[qq]), "v"(av[qq]));
          unsigned hh, ll;
          split_h(z[0], z[1], hh, ll);
          hi[qq][e] = hh; lo[qq][e] = ll;
        }
      }
      const h8 bh = __builtin_bit_cast(h8, hi[0]), bl = __builtin_bit_cast(h8, lo[0]);
      acc0 = __builtin_amdgcn_mfma_f32_16x16x32_f16(__builtin_bit_cast(h8, lo[1]), bh, acc0, 0, 0, 0);
      acc1 = __builtin_amdgcn_mfma_f32_16x16x32_f16(__builtin_bit_cast(h8, lo[2]), bh, acc1, 0, 0, 0);
      acc2 = __builtin_amdgcn_mfma_f32_16x16x32_f16(__builtin_bit_cast(h8, lo[3]), bh, acc2, 0, 0, 0);
      acc0 = __builtin_amdgcn_mfma_f32_16x16x32_f16(__builtin_bit_cast(h8, hi[1]), bl, acc0, 0, 0, 0);
      acc1 = __builtin_amdgcn_mfma_f32_16x16x32_f16(__builtin_bit_cast(h8, hi[2]), bl, acc1, 0, 0, 0);
      acc2 = __builtin_amdgcn_mfma_f32_16x16x32_f16(__builtin_bit_cast(h8, hi[3]), bl, acc2, 0, 0, 0);
      acc0 = __builtin_amdgcn_mfma_f32_16x16x32_f16(__builtin_bit_cast(h8, hi[1]), bh, acc0, 0, 0, 0);
      acc1 = __builtin_amdgcn_mfma_f32_16x16x32_f16(__builtin_bit_cast(h8, hi[2]), bh, acc1, 0, 0, 0);
      acc2 = __builtin_amdgcn_mfma_f32_16x16x32_f16(__builtin_bit_cast(h8, hi[3]), bh, acc2, 0, 0, 0);
    }
    red[(w4 * 3 + 0) * 64 + lane] = acc0;
    red[(w4 * 3 + 1) * 64 + lane] = acc1;
    red[(w4 * 3 + 2) * 64 + lane] = acc2;
  }
  __syncthreads();
  if (w4 < 3) {
    f4 vv = red[(0 * 3 + w4) * 64 + lane] + red[(1 * 3 + w4) * 64 + lane] +
            (red[(2 * 3 + w4) * 64 + lane] + red[(3 * 3 + w4) * 64 + lane]);
#pragma unroll
    for (int r = 0; r < 4; ++r) kv[(16 * w4 + 4 * g + r) * 16 + idx] = vv[r] * (1.0f / 2048.0f);
  }
  if (t8 == 0) ((float*)(ws + WS_KSC))[d] = __uint_as_float((unsigned)(127 + ce - 14) << 23);
  __syncthreads();
  u4 hi, lo;
#pragma unroll
  for (int j = 0; j < 4; ++j) {
    if (KTERMS > 1) {
      unsigned hh, ll;
      split_h(*(const float*)(smem + kidx[2 * j]), *(const float*)(smem + kidx[2 * j + 1]), hh, ll);
      hi[j] = hh; lo[j] = ll;
    } else {
      hi[j] = pack_h(*(const float*)(smem + kidx[2 * j]), *(const float*)(smem + kidx[2 * j + 1]));
    }
  }
  st16(ws + WS_KH + ((size_t)d * SL + t8 * 8) * 2, hi);
  if (KTERMS > 1) st16(ws + WS_KL + ((size_t)d * SL + t8 * 8) * 2, lo);
}

constexpr int CV_U = 2064 * 2;
constexpr int CV_K = 2160 * 2;
constexpr int CV_OFF_K = 2 * CV_U;
constexpr int CV_OFF_Y = CV_OFF_K + 2 * KTERMS * CV_K;
constexpr int CV_Q = (SL + 4) * 4;
constexpr int CV_SMEM = CV_OFF_Y + 2 * 4 * CV_Q;

constexpr int CV_RPF = 2 + 2 * KTERMS;
constexpr int CV_R0 = (KTERMS == 2 ? 6 : 10) * CV_RPF;
constexpr int CV_NM = 68 * KTERMS;
struct CvStep { int a, n, k, first, op, arg, arg2; };
template <int SUB>
struct CvSched {
  CvStep s[CV_NM];
  int wt, wc, slack;
  constexpr CvSched() : s{}, wt(-1), wc(4), slack(1000) {
    const int AMAX = SUB == 0 ? 15 : 14;
    const int NR = (AMAX + 1) * CV_RPF;
    int nr = CV_R0, mi = 0;
    int endat[16] = {};
    for (int a = 0; a < 16; ++a) endat[a] = 1 << 20;
    for (int a = 0; a <= AMAX; ++a) {
      if ((((a + 1) >> 1) & 1) != SUB) continue;
      for (int nn = 0; nn <= a; ++nn) {
        const int n = (a == 0) ? 0 : (nn + 1 < a ? nn + 1 : (nn + 1 == a ? 0 : a));
        for (int k = 0; k < KTERMS; ++k) {
          CvStep st{a, n, k, (nn == 0 && k == 0) ? 1 : 0, 0, 0, 0};
          { const int need = ((n > a - n ? n : a - n) + 1) * CV_RPF; if (nr < NR && nr - need < slack) slack = nr - need; }
          ++mi;
          if (wc == 4) {
            for (int t = wt + 1; t < 16; ++t)
              if (endat[t] < (1 << 20)) { if (endat[t] + 8 <= mi) { wt = t; wc = 0; } break; }
          }
          if (wc < 4) { st.op = 2; st.arg = wt; st.arg2 = wc; ++wc; }
          else if (nr < NR) { st.op = 1; st.arg = nr; ++nr; }
          s[mi - 1] = st;
        }
      }
      endat[a] = mi;
    }
  }
};
template <int R>
__device__ __forceinline__ void cv_rd(s4 (&A)[16][KTERMS][2], s4 (&B)[16][2], const char* ub, const char* kb) {
  constexpr int i = R / CV_RPF, part = R % CV_RPF;
  if constexpr (part < 2) B[i][part] = tr_read(ub + 256 * i + 32 * part);
  else A[i][(part - 2) >> 1][part & 1] = tr_read(kb + ((part - 2) >> 1) * CV_K + 256 * i + 32 * (1 - (part & 1)));
}
template <int SUB, int I>
__device__ __forceinline__ void cv_step(s4 (&A)[16][KTERMS][2], s4 (&B)[16][2], f4 (&acc)[16], const char* ub, const char* kb, char* qb) {
  constexpr CvSched<SUB> S{};
  constexpr CvStep st = S.s[I];
  const s8 av = cat8(A[st.a - st.n][st.k][0], A[st.a - st.n][st.k][1]), bv = cat8(B[st.n][0], B[st.n][1]);
  if constexpr (st.first) asm volatile("v_mfma_f32_16x16x32_f16 %0, %1, %2, 0" : "=&v"(acc[st.a]) : "v"(av), "v"(bv));
  else asm volatile("v_mfma_f32_16x16x32_f16 %0, %1, %2, %0" : "+v"(acc[st.a]) : "v"(av), "v"(bv));
  if constexpr (st.op == 2) *(float*)(qb + st.arg2 * (CV_Q + 4) + st.arg * 512) = acc[st.arg][st.arg2];
  else if constexpr (st.op == 1) cv_rd<st.arg>(A, B, ub, kb);
  SCHED;
}
template <int SUB, int... Rs, int... Is>
__device__ __forceinline__ void conv_mfma_impl(const char* ub, const char* kb, char* qb, iseq<Rs...>, iseq<Is...>) {
  s4 A[16][KTERMS][2];
  s4 B[16][2];
  f4 acc[16];
  (cv_rd<Rs>(A, B, ub, kb), ...);
  SCHED;
  (cv_step<SUB, Is>(A, B, acc, ub, kb, qb), ...);
  asm volatile("s_nop 7");
  SCHED;
  constexpr CvSched<SUB> S{};
  static_assert(S.slack >= 4, "fragment read scheduled too late");
#pragma unroll
  for (int t = 0; t < 16; ++t) {
    if ((((t + 1) >> 1) & 1) != SUB) continue;
#pragma unroll
    for (int r = 0; r < 4; ++r)
      if (t > S.wt || (t == S.wt && r >= S.wc)) *(float*)(qb + r * (CV_Q + 4) + t * 512) = acc[t][r];
  }
}
template <int SUB>
__device__ __forceinline__ void conv_mfma(const char* ub, const char* kb, char* qb) {
  conv_mfma_impl<SUB>(ub, kb, qb, make_iseq<CV_R0>{}, make_iseq<CV_NM>{});
}

__global__ __launch_bounds__(512, 2) void conv_kernel(
    const u16* __restrict__ xT, const double* __restrict__ stats, const u16* __restrict__ Kh,
    const u16* __restrict__ Kl, const float* __restrict__ ksc, const u16* __restrict__ dskip,
    u16* __restrict__ outT) {
  extern __shared__ __attribute__((aligned(16))) char smem[];
  const int tid = threadIdx.x, lane = tid & 63;
  const int wid = __builtin_amdgcn_readfirstlane(tid >> 6);
  const int d0 = blockIdx.x * 2;
  const int t0 = tid * 4;
  u2 xr[2];
  u4 kr[KTERMS];
  d2 sr[4];
#pragma unroll
  for (int i = 0; i < 4; ++i) sr[i] = *(const d2*)(stats + t0 * 2 + i * 2);
#pragma unroll
  for (int c = 0; c < 2; ++c) xr[c] = *(const u2*)(xT + xoff(d0 + c, t0));
  kr[0] = *(const u4*)(Kh + (size_t)d0 * SL + tid * 8);
  if (KTERMS > 1) kr[KTERMS - 1] = *(const u4*)(Kl + (size_t)d0 * SL + tid * 8);
  const float2 isc2 = *(const float2*)(ksc + d0);
  const unsigned ds2 = *(const unsigned*)(dskip + d0);
  if (tid < 14 * 2 * KTERMS) {
    int which = tid / 14, j = tid % 14;
    *(u4*)(smem + CV_OFF_K + which * CV_K + j * 16) = u4{0, 0, 0, 0};
  } else if (tid >= 64 && tid < 70) {
    int c = (tid - 64) / 3, j = (tid - 64) % 3;
    *(u2*)(smem + c * CV_U + j * 8) = u2{0, 0};
  } else if (tid >= 128 && tid < 136) {
    *(f4*)(smem + CV_OFF_Y + (tid - 128) * CV_Q) = f4{0, 0, 0, 0};
  }
  float mb[4], ri[4];
#pragma unroll
  for (int j = 0; j < 4; ++j) ln_consts((float)sr[j][0], (float)sr[j][1], mb[j], ri[j]);
  u2 ub16[2];
#pragma unroll
  for (int c = 0; c < 2; ++c) {
    unsigned d01 = pack_bf(bflo(xr[c][0]) - mb[0], bfhi(xr[c][0]) - mb[1]);
    unsigned d23 = pack_bf(bflo(xr[c][1]) - mb[2], bfhi(xr[c][1]) - mb[3]);
    ub16[c][0] = pack_bf(bflo(d01) * ri[0], bfhi(d01) * ri[1]);
    ub16[c][1] = pack_bf(bflo(d23) * ri[2], bfhi(d23) * ri[3]);
    u2 a = {pack_h(bflo(ub16[c][0]), bfhi(ub16[c][0])), pack_h(bflo(ub16[c][1]), bfhi(ub16[c][1]))};
    *(u2*)(smem + c * CV_U + 24 + tid * 8) = a;
  }
#pragma unroll
  for (int k = 0; k < KTERMS; ++k)
    *(u4*)(smem + CV_OFF_K + ((tid >> 8) * KTERMS + k) * CV_K + 224 + (tid & 255) * 16) = kr[k];
  __syncthreads();
  {
    const int ch = wid >> 2, par = (wid >> 1) & 1;
    const int g = lane >> 4, q = (lane >> 2) & 3, p = lane & 3;
    const char* ub = smem + ch * CV_U + 8 * (p + q + 8 * g);
    const char* kb = smem + CV_OFF_K + ch * KTERMS * CV_K + 8 * (31 + 16 * par + 4 * p - 8 * g - q) - 32;
    char* qb = smem + CV_OFF_Y + ch * 4 * CV_Q + par * 256 + lane * 4;
    if (wid & 1) conv_mfma<1>(ub, kb, qb);
    else conv_mfma<0>(ub, kb, qb);
  }
  __syncthreads();
#pragma unroll
  for (int c = 0; c < 2; ++c) {
    const char* Q = smem + CV_OFF_Y + c * 4 * CV_Q + t0 * 4;
    f4 y0 = (*(const f4*)(Q) + *(const f4*)(Q + CV_Q)) + (*(const f4*)(Q + 2 * CV_Q) + *(const f4*)(Q + 3 * CV_Q));
    float isc = c ? isc2.y : isc2.x;
    float ds = c ? bfhi(ds2) : bflo(ds2);
    unsigned y01 = pack_bf(y0[0] * isc, y0[1] * isc), y23 = pack_bf(y0[2] * isc, y0[3] * isc);
    unsigned s01 = pack_bf(bflo(ub16[c][0]) * ds, bfhi(ub16[c][0]) * ds), s23 = pack_bf(bflo(ub16[c][1]) * ds, bfhi(ub16[c][1]) * ds);
    unsigned a01 = pack_bf(bflo(y01) + bflo(s01), bfhi(y01) + bfhi(s01)), a23 = pack_bf(bflo(y23) + bflo(s23), bfhi(y23) + bfhi(s23));
    u2 ov = {pack_bf(bflo(xr[c][0]) + bflo(a01), bfhi(xr[c][0]) + bfhi(a01)), pack_bf(bflo(xr[c][1]) + bflo(a23), bfhi(xr[c][1]) + bfhi(a23))};
    st8(outT + xoff(d0 + c, t0), ov);
  }
}

constexpr int G1_SMEM = 512 * 256 + 512 * 8;

__device__ __forceinline__ void glds16(const void* gsrc, unsigned lds_dst) {
  unsigned keep;
  asm volatile("s_mov_b32 %0, m0\n\ts_mov_b32 m0, %2\n\ts_nop 0\n\tglobal_load_lds_dwordx4 %1, off\n\ts_mov_b32 m0, %0"
               : "=&s"(keep) : "v"(gsrc), "s"(lds_dst) : "memory");
}
template <int OFF>
__device__ __forceinline__ void gload16(u4& dst, const void* p) {
  asm volatile("global_load_dwordx4 %0, %1, off offset:%2" : "=v"(dst) : "v"(p), "i"(OFF) : "memory");
}
template <int N>
__device__ __forceinline__ void wait_only(u4& a) {
  asm volatile("s_waitcnt vmcnt(%1)" : "+v"(a) : "i"(N) : "memory");
}
template <int N>
__device__ __forceinline__ void wait_vm() { asm volatile("s_waitcnt vmcnt(%0)" :: "i"(N) : "memory"); }
__device__ __forceinline__ void add1(float& a, float b) { asm("v_add_f32 %0, %0, %1" : "+v"(a) : "v"(b)); }
__device__ __forceinline__ void sqa1(float& a, float b) { asm("v_fmac_f32 %0, %1, %1" : "+v"(a) : "v"(b)); }
__device__ __forceinline__ void bar() { asm volatile("s_waitcnt lgkmcnt(0)\n\ts_barrier" ::: "memory"); }
constexpr int G1_ST = 4 * 32768;
constexpr int F_BPRE = 5;
constexpr int G1_XPF = 3;
constexpr int G1_WPF = 8;

template <int OFF>
__device__ __forceinline__ void gload16_sc1(u4& dst, const void* p) {
  asm volatile("global_load_dwordx4 %0, %1, off offset:%2 sc1" : "=v"(dst) : "v"(p), "i"(OFF) : "memory");
}
template <int N>
__device__ __forceinline__ void wait4(u4 (&a)[4]) {
  asm volatile("s_waitcnt vmcnt(%4)" : "+v"(a[0]), "+v"(a[1]), "+v"(a[2]), "+v"(a[3]) : "i"(N) : "memory");
}

template <bool FINAL>
__global__ __launch_bounds__(512, 2) void ffn_kernel(
    const u16* __restrict__ xT, const u16* __restrict__ W1F, const float* __restrict__ c1,
    u16* HF, float* pst, const u16* __restrict__ W2F, unsigned* cnt,
    u16* __restrict__ out, double* __restrict__ stats) {
  extern __shared__ __attribute__((aligned(16))) char smem[];
  const int tid = threadIdx.x, lane = tid & 63;
  const int wid = __builtin_amdgcn_readfirstlane(tid >> 6);
  const int bx = blockIdx.x, xj = bx & 7, r = bx >> 3;
  const int tp = 4 * (xj >> 1) + (r & 3);
  const int fp = 7 * (xj & 1) + (r >> 2);
  if (r < 28) {
  const int h = lane >> 5, blk = (lane >> 4) & 1, q = (lane >> 2) & 3, p = lane & 3;
  const char* rbase = smem + (8 * h + q) * 64 + blk * 32 + 8 * p;
  float* st = (float*)(smem + G1_ST);
  if (wid >= 4) {
    const int hq = wid - 4;
    const char* xsrc = (const char*)xT + (size_t)tp * 131072 + hq * 1024 + lane * 16;
    const unsigned ldst = (unsigned)(size_t)smem + hq * 1024;
    auto issue_c = [&](int c) AINL {
#pragma unroll
      for (int i = 0; i < 3; ++i) glds16(xsrc + c * 4096 + i * 32768, ldst + c * 4096 + i * 32768);
    };
#pragma unroll
    for (int c = 0; c < G1_XPF; ++c) issue_c(c);
#pragma unroll 1
    for (int c = 0; c < 8 - G1_XPF; ++c) {
      wait_vm<3 * (G1_XPF - 1)>(); bar();
      issue_c(c + G1_XPF);
    }
    sfor<G1_XPF>([&](auto I) AINL {
      constexpr int i = decltype(I)::value;
      if constexpr (i == 0) wait_vm<3 * (G1_XPF - 1)>(); else wait_vm<3 * (G1_XPF - 1 - i) + 8>();
      bar();
      if constexpr (i == 0) {
#pragma unroll
        for (int k = 0; k < 8; ++k) glds16(xsrc + 3 * 32768 + k * 4096, ldst + 3 * 32768 + k * 4096);
      }
    });
    wait_vm<0>();
    bar();
#pragma unroll 1
    for (int it = 0; it < 2; ++it) {
      const int u = it == 0 ? 12 + hq : (fp - 1) * 4 + hq;
      if (it == 1 && (fp < 1 || fp > 3)) break;
      const char* rq = rbase + u * 8192;
      s4 fr[8][2];
      sfor<8>([&](auto I) AINL {
        constexpr int i = decltype(I)::value;
        fr[i][0] = tr_read(rq + i * 1024);
        fr[i][1] = tr_read(rq + i * 1024 + 256);
      });
      float sa = 0.f, sb = 0.f, qa = 0.f, qb = 0.f;
      sfor<8>([&](auto I) AINL {
        constexpr int i = decltype(I)::value;
        u4 xv = __builtin_bit_cast(u4, cat8(fr[i][0], fr[i][1]));
#pragma unroll
        for (int j = 0; j < 4; ++j) {
          float lo = bflo(xv[j]), hi = bfhi(xv[j]);
          add1(sa, lo); add1(sb, hi); sqa1(qa, lo); sqa1(qb, hi);
        }
      });
      float s0 = sa + sb, ss0 = qa + qb;
      s0 += __shfl_xor(s0, 32);
      ss0 += __shfl_xor(ss0, 32);
      if (it == 0) {
        if (lane < 32) st[128 + hq * 32 + lane] = s0;
        bar();
      }
      if (lane < 32 && (it == 1 || fp == 0))
        st8(pst + ((tp * 128 + (u >> 2) * 32 + lane) * 4 + (u & 3)) * 2, u2{__float_as_uint(s0), __float_as_uint(ss0)});
    }
  } else {
  const int ft = fp * 4 + wid;
  u4 wf[32];
  u4 cfu[4];
  const char* wsrc = (const char*)W1F + ((size_t)ft * 32 * 64 + lane) * 16;
  auto issue_w = [&](auto S) AINL {
    constexpr int s = decltype(S)::value;
    gload16<(s & 3) * 1024>(wf[s], wsrc + (s >> 2) * 4096);
  };
  sfor<G1_WPF>(issue_w);
  f16v acc[4], asum;
  s4 xa[3][3][2];
  constexpr int XD = 4, XR = XD + 1;
  s4 xc[XR][2];
  u4 ov;
  float nm[4];
  const int w3 = wid == 3 ? 0 : wid;
  int tto[3];
#pragma unroll
  for (int i = 0; i < 3; ++i) { int tt = i + w3; tto[i] = tt >= 3 ? tt - 3 : tt; }
  const char* rb[3];
#pragma unroll
  for (int i = 0; i < 3; ++i) rb[i] = rbase + tto[i] * 32768;
  u16* dst = HF + ((size_t)((tp * 8 + blk) * 56 + ft) * 64 + (lane & 15) + 16 * h) * 8;
  auto epi_pair = [&](const f16v& a, float m, int e) AINL -> unsigned {
    f4 cf = __builtin_bit_cast(f4, cfu[e >> 2]);
    unsigned pu = pack_bf(fmaf(cf[e & 3], m, a[e]), fmaf(cf[(e & 3) + 1], m, a[e + 1]));
    asm("" : "+v"(pu));
    return __builtin_bit_cast(unsigned, __builtin_elementwise_max(__builtin_bit_cast(s2v, pu), s2v{0, 0}));
  };
  f16v z;
#pragma unroll
  for (int j = 0; j < 16; ++j) z[j] = 0.f;
  const s8 ones8 = {0x3f80, 0x3f80, 0x3f80, 0x3f80, 0x3f80, 0x3f80, 0x3f80, 0x3f80};
  SCHED;
  asm volatile("s_barrier" ::: "memory");
#pragma unroll
  for (int k = 0; k < 2; ++k)
#pragma unroll
    for (int i = 0; i < 3; ++i) { xa[k][i][0] = tr_read(rb[i] + k * 1024); xa[k][i][1] = tr_read(rb[i] + k * 1024 + 256); }
  SCHED;
  sfor<32>([&](auto S) AINL {
    constexpr int s = decltype(S)::value;
    constexpr int issued = (G1_WPF + s <= 32) ? G1_WPF + s : 36;
    asum = __builtin_amdgcn_mfma_f32_32x32x16_bf16(__builtin_bit_cast(b8, ones8), __builtin_bit_cast(b8, cat8(xa[s % 3][0][0], xa[s % 3][0][1])), s == 0 ? z : asum, 0, 0, 0);
    wait_only<issued - s - 1>(wf[s]);
    if constexpr ((s & 3) == 2 && s < 30) asm volatile("s_barrier" ::: "memory");
    SCHED;
    sfor<3>([&](auto I) AINL {
      constexpr int i = decltype(I)::value;
      const b8 xv = __builtin_bit_cast(b8, cat8(xa[s % 3][i][0], xa[s % 3][i][1]));
      acc[i] = __builtin_amdgcn_mfma_f32_32x32x16_bf16(__builtin_bit_cast(b8, wf[s]), xv, s == 0 ? z : acc[i], 0, 0, 0);
      if constexpr (s + 2 < 32) {
        xa[(s + 2) % 3][i][0] = tr_read(rb[i] + (s + 2) * 1024);
        xa[(s + 2) % 3][i][1] = tr_read(rb[i] + (s + 2) * 1024 + 256);
      }
      if constexpr (i == 0) {
        if constexpr (G1_WPF + s < 32) issue_w(ic<(G1_WPF + s < 32) ? G1_WPF + s : 0>{});
        if constexpr (G1_WPF + s == 32) {
          gload16<0>(cfu[0], c1 + ft * 32 + 4 * h);
          gload16<32>(cfu[1], c1 + ft * 32 + 4 * h);
          gload16<64>(cfu[2], c1 + ft * 32 + 4 * h);
          gload16<96>(cfu[3], c1 + ft * 32 + 4 * h);
        }
      }
      SCHED;
    });
  });
  if (lane < 32) st[wid * 32 + lane] = -rbf(asum[0] * (1.0f / DM));
  asm volatile("s_waitcnt vmcnt(0) lgkmcnt(0)\n\ts_barrier" : "+v"(cfu[0]), "+v"(cfu[1]), "+v"(cfu[2]), "+v"(cfu[3]) :: "memory");
  {
    unsigned rbo = (unsigned)(rbase - smem) + 3 * 32768;
    asm volatile("" : "+v"(rbo));
    const char* rbb = smem + rbo;
    sfor<XD>([&](auto I) AINL {
      constexpr int i = decltype(I)::value;
      xc[i][0] = tr_read(rbb + i * 1024); xc[i][1] = tr_read(rbb + i * 1024 + 256);
    });
#pragma unroll
    for (int i = 0; i < 3; ++i) nm[i] = st[tto[i] * 32 + (lane & 31)];
    SCHED;
    sfor<32>([&](auto S) AINL {
      constexpr int s = decltype(S)::value;
      const b8 xv = __builtin_bit_cast(b8, cat8(xc[s % XR][0], xc[s % XR][1]));
      acc[3] = __builtin_amdgcn_mfma_f32_32x32x16_bf16(__builtin_bit_cast(b8, wf[s]), xv, s == 0 ? z : acc[3], 0, 0, 0);
      if constexpr (s + XD < 32) {
        xc[(s + XD) % XR][0] = tr_read(rbb + (s + XD) * 1024);
        xc[(s + XD) % XR][1] = tr_read(rbb + (s + XD) * 1024 + 256);
      }
      if constexpr (s < 24) {
        constexpr int i = s >> 3, pr = s & 7;
        ov[pr & 3] = epi_pair(acc[i], nm[i], 2 * pr);
        if constexpr ((pr & 3) == 3) st16(dst + tto[i] * (2 * 56 * 512) + (pr >> 2) * 256, ov);
      }
      SCHED;
    });
  }
  asm volatile("s_barrier" ::: "memory");
  {
    const float* sp = st + 128 + (lane & 31);
    nm[3] = -rbf(((sp[0] + sp[32]) + (sp[64] + sp[96])) * (1.0f / DM));
  }
#pragma unroll
  for (int c = 0; c < 2; ++c) {
#pragma unroll
    for (int j = 0; j < 4; ++j) ov[j] = epi_pair(acc[3], nm[3], 8 * c + 2 * j);
    st16(dst + 3 * (2 * 56 * 512) + c * 256, ov);
  }
  }
  }
  const int tb = 8 * (xj >> 1) + (r & 7), db = 4 * (xj & 1) + (r >> 3);
  const int g = lane >> 4, n = lane & 15;
  const int mo = wid >> 1, no = 2 * (wid & 1);
  u4 bf[7][4], af[7][4];
  {
    const char* bp[4];
#pragma unroll
    for (int j = 0; j < 4; ++j) bp[j] = (const char*)W2F + ((size_t)((db * 4 + ((j + no) & 3)) * 56 + wid * 7) * 64 + lane) * 16;
    auto issue_b = [&](auto KS) AINL {
      constexpr int ks = decltype(KS)::value;
      sfor<4>([&](auto J) AINL {
        constexpr int j = decltype(J)::value;
        gload16<(ks & 3) * 1024>(bf[ks][j], bp[j] + (ks >> 2) * 4096);
      });
    };
    wait_vm<0>();
    bar();
    if (r < 28 && tid == 0) __hip_atomic_fetch_add(cnt + tp * 32, 1u, __ATOMIC_RELAXED, __HIP_MEMORY_SCOPE_AGENT);
    if (wid == 7) {
      const unsigned* cp = cnt + (tb >> 1) * 32;
      for (int spin = 0; spin < (1 << 22); ++spin) {
        if (__hip_atomic_load(cp, __ATOMIC_RELAXED, __HIP_MEMORY_SCOPE_AGENT) >= 14u) break;
        __builtin_amdgcn_s_sleep(2);
      }
    }
    sfor<F_BPRE>(issue_b);
    bar();
    sfor<7 - F_BPRE>([&](auto I) AINL { issue_b(ic<F_BPRE + decltype(I)::value>{}); });
  }
  u2 res[2];
  u16 resf[2][4];
  if (!FINAL) {
#pragma unroll
    for (int j = 0; j < 2; ++j) res[j] = *(const u2*)(xT + xoff(db * 64 + (no + j) * 16 + n, tb * 64 + mo * 16 + 4 * g));
  } else {
#pragma unroll
    for (int j = 0; j < 2; ++j)
#pragma unroll
      for (int e = 0; e < 4; ++e) resf[j][e] = xT[xoff(db * 64 + (no + j) * 16 + 4 * g + e, tb * 64 + mo * 16 + n)];
  }
  const unsigned long long psu = __hip_atomic_load((const unsigned long long*)(pst + ((tb * 64 + mo * 16 + n) * 4 + g) * 2), __ATOMIC_RELAXED, __HIP_MEMORY_SCOPE_AGENT);
  const char* ap[4];
#pragma unroll
  for (int i = 0; i < 4; ++i) ap[i] = (const char*)HF + ((size_t)((tb * 4 + ((i + mo) & 3)) * 56 + wid * 7) * 64 + lane) * 16;
  auto issue_a = [&](auto KS) AINL {
    constexpr int ks = decltype(KS)::value;
    sfor<4>([&](auto I) AINL {
      constexpr int i = decltype(I)::value;
      gload16_sc1<(ks & 3) * 1024>(af[ks][i], ap[i] + (ks >> 2) * 4096);
    });
  };
  sfor<4>(issue_a);
  f4 acc[4][4];
#pragma unroll
  for (int i = 0; i < 4; ++i)
#pragma unroll
    for (int j = 0; j < 4; ++j) acc[i][j] = f4{0, 0, 0, 0};
  SCHED;
  sfor<7>([&](auto KS) AINL {
    constexpr int ks = decltype(KS)::value;
    wait4<(ks < 4 ? 12 : 4 * (6 - ks))>(af[ks]);
    wait4<63>(bf[ks]);
    SCHED;
#pragma unroll
    for (int i = 0; i < 4; ++i)
#pragma unroll
      for (int j = 0; j < 4; ++j) {
        if (!FINAL) acc[i][j] = __builtin_amdgcn_mfma_f32_16x16x32_bf16(__builtin_bit_cast(b8, af[ks][i]), __builtin_bit_cast(b8, bf[ks][j]), acc[i][j], 0, 0, 0);
        else acc[i][j] = __builtin_amdgcn_mfma_f32_16x16x32_bf16(__builtin_bit_cast(b8, bf[ks][j]), __builtin_bit_cast(b8, af[ks][i]), acc[i][j], 0, 0, 0);
      }
    SCHED;
    if constexpr (ks + 4 < 7) issue_a(ic<(ks + 4 < 7) ? ks + 4 : 0>{});
    SCHED;
  });
  f4* red = (f4*)smem;
#pragma unroll
  for (int i = 0; i < 4; ++i)
#pragma unroll
    for (int jp = 0; jp < 2; ++jp) {
      if (i == 0 && jp == 0) continue;
      const int owner = ((i + mo) & 3) * 2 + ((jp + wid) & 1);
      red[((owner * 7 + i * 2 + jp - 1) * 2 + 0) * 64 + lane] = acc[i][2 * jp];
      red[((owner * 7 + i * 2 + jp - 1) * 2 + 1) * 64 + lane] = acc[i][2 * jp + 1];
    }
  f4 rr;
  {
    float sx = __uint_as_float((unsigned)psu), sy = __uint_as_float((unsigned)(psu >> 32));
    sx += __shfl_xor(sx, 16); sy += __shfl_xor(sy, 16);
    sx += __shfl_xor(sx, 32); sy += __shfl_xor(sy, 32);
    float mb, ri;
    ln_consts(sx, sy, mb, ri);
    if (FINAL) rr = f4{ri, ri, ri, ri};
    else {
#pragma unroll
      for (int e = 0; e < 4; ++e) rr[e] = __shfl(ri, 4 * g + e);
    }
  }
  bar();
  float s[4] = {0, 0, 0, 0}, ss[4] = {0, 0, 0, 0};
#pragma unroll
  for (int j = 0; j < 2; ++j) {
    const f4* rp = red + (wid * 14 + j) * 64 + lane;
    f4 v = ((acc[0][j] + rp[0]) + (rp[128] + rp[256])) + ((rp[384] + rp[512]) + (rp[640] + rp[768]));
    v *= rr;
    unsigned m01 = pack_bf(v[0], v[1]), m23 = pack_bf(v[2], v[3]);
    u2 ov;
    if (!FINAL) {
      ov[0] = pack_bf(bflo(res[j][0]) + bflo(m01), bfhi(res[j][0]) + bfhi(m01));
      ov[1] = pack_bf(bflo(res[j][1]) + bflo(m23), bfhi(res[j][1]) + bfhi(m23));
      *(u2*)(out + xoff(db * 64 + (no + j) * 16 + n, tb * 64 + mo * 16 + 4 * g)) = ov;
      float o[4] = {bflo(ov[0]), bfhi(ov[0]), bflo(ov[1]), bfhi(ov[1])};
#pragma unroll
      for (int e = 0; e < 4; ++e) { s[e] += o[e]; ss[e] = fmaf(o[e], o[e], ss[e]); }
    } else {
      ov[0] = pack_bf(bf2f(resf[j][0]) + bflo(m01), bf2f(resf[j][1]) + bfhi(m01));
      ov[1] = pack_bf(bf2f(resf[j][2]) + bflo(m23), bf2f(resf[j][3]) + bfhi(m23));
      *(u2*)(out + (size_t)(tb * 64 + mo * 16 + n) * DM + db * 64 + (no + j) * 16 + 4 * g) = ov;
    }
  }
  if (!FINAL) {
#pragma unroll
    for (int e = 0; e < 4; ++e) { s[e] = row_sum16(s[e]); ss[e] = row_sum16(ss[e]); }
    if (n < 8) {
      int e = n >> 1;
      float v = (n & 1) ? (e == 0 ? ss[0] : e == 1 ? ss[1] : e == 2 ? ss[2] : ss[3]) : (e == 0 ? s[0] : e == 1 ? s[1] : e == 2 ? s[2] : s[3]);
      unsafeAtomicAdd(stats + (tb * 64 + mo * 16 + 4 * g) * 2 + n, (double)v);
    }
  }
}

extern "C" void kernel_launch(void* const* d_in, const int* in_sizes, int n_in,
                              void* d_out, int out_size, void* d_ws, size_t ws_size,
                              hipStream_t stream) {
  const u16* x = (const u16*)d_in[0];
  const u16* logA = (const u16*)d_in[1];
  const u16* Aim = (const u16*)d_in[2];
  const u16* Cre = (const u16*)d_in[3];
  const u16* Cim = (const u16*)d_in[4];
  const u16* logdt = (const u16*)d_in[5];
  const u16* dskip = (const u16*)d_in[6];
  const u16* w1 = (const u16*)d_in[8];
  const u16* w2 = (const u16*)d_in[9];
  char* ws = (char*)d_ws;
  u16* out = (u16*)d_out;
  (void)hipFuncSetAttribute((const void*)ffn_kernel<false>, hipFuncAttributeMaxDynamicSharedMemorySize, G1_SMEM);
  (void)hipFuncSetAttribute((const void*)ffn_kernel<true>, hipFuncAttributeMaxDynamicSharedMemorySize, G1_SMEM);
  (void)hipFuncSetAttribute((const void*)conv_kernel, hipFuncAttributeMaxDynamicSharedMemorySize, CV_SMEM);

  prep_kernel<<<256 + PREP_PF + PREP_W1 + PREP_W2 + PREP_TR, 512, 0, stream>>>(x, logA, Aim, Cre, Cim, logdt, w1, w2, ws);
  conv_kernel<<<256, 512, CV_SMEM, stream>>>((const u16*)(ws + WS_XT0), (const double*)(ws + WS_ST0), (const u16*)(ws + WS_KH),
                                             (const u16*)(ws + WS_KL), (const float*)(ws + WS_KSC), dskip, (u16*)(ws + WS_XT1));
  ffn_kernel<false><<<256, 512, G1_SMEM, stream>>>((const u16*)(ws + WS_XT1), (const u16*)(ws + WS_W1F), (const float*)(ws + WS_C1),
                                                   (u16*)(ws + WS_HF), (float*)(ws + WS_PST), (const u16*)(ws + WS_W2F),
                                                   (unsigned*)(ws + WS_CNT), (u16*)(ws + WS_XT2), (double*)(ws + WS_ST2));
  conv_kernel<<<256, 512, CV_SMEM, stream>>>((const u16*)(ws + WS_XT2), (const double*)(ws + WS_ST2), (const u16*)(ws + WS_KH),
                                             (const u16*)(ws + WS_KL), (const float*)(ws + WS_KSC), dskip, (u16*)(ws + WS_XT3));
  ffn_kernel<true><<<256, 512, G1_SMEM, stream>>>((const u16*)(ws + WS_XT3), (const u16*)(ws + WS_W1F), (const float*)(ws + WS_C1),
                                                  (u16*)(ws + WS_HF2), (float*)(ws + WS_PST2), (const u16*)(ws + WS_W2F),
                                                  (unsigned*)(ws + WS_CNT) + 512, out, nullptr);
}
```

```cpp
#include <hip/hip_runtime.h>
#include <hip/hip_bf16.h>

typedef unsigned short u16;
typedef short s4 __attribute__((ext_vector_type(4)));
typedef short s2v __attribute__((ext_vector_type(2)));
typedef short s8 __attribute__((ext_vector_type(8)));
typedef _Float16 h8 __attribute__((ext_vector_type(8)));
typedef _Float16 h2 __attribute__((ext_vector_type(2)));
typedef __bf16 b2 __attribute__((ext_vector_type(2)));
typedef __bf16 b8 __attribute__((ext_vector_type(8)));
typedef float f4 __attribute__((ext_vector_type(4)));
typedef float f2 __attribute__((ext_vector_type(2)));
typedef double d2 __attribute__((ext_vector_type(2)));
typedef __bf16 b4 __attribute__((ext_vector_type(4)));
typedef float f16v __attribute__((ext_vector_type(16)));
typedef unsigned u4 __attribute__((ext_vector_type(4)));
typedef unsigned u2 __attribute__((ext_vector_type(2)));
typedef __attribute__((address_space(3))) s4* lds_s4p;

#define SCHED __builtin_amdgcn_sched_barrier(0)
template <int... Is> struct iseq {};
template <int N, int... Is> struct mk_iseq : mk_iseq<N - 1, N - 1, Is...> {};
template <int... Is> struct mk_iseq<0, Is...> { typedef iseq<Is...> type; };
template <int N> using make_iseq = typename mk_iseq<N>::type;
template <int I> struct ic { static constexpr int value = I; };
template <class F, int... Is> __device__ __forceinline__ void sfor_impl(F&& f, iseq<Is...>) { (f(ic<Is>{}), ...); }
template <int N, class F> __device__ __forceinline__ void sfor(F&& f) { sfor_impl(f, make_iseq<N>{}); }
#define AINL __attribute__((always_inline))

constexpr int SL = 2048;
constexpr int DM = 512;
constexpr int NS = 128;
constexpr int FF = 1792;
#define KTERMS 1

constexpr size_t WS_XT0 = 0;
constexpr size_t WS_XT1 = WS_XT0 + (size_t)SL * DM * 2;
constexpr size_t WS_XT2 = WS_XT1 + (size_t)SL * DM * 2;
constexpr size_t WS_XT3 = WS_XT2 + (size_t)SL * DM * 2;
constexpr size_t WS_KH  = WS_XT3 + (size_t)SL * DM * 2;
constexpr size_t WS_KL  = WS_KH + (size_t)SL * DM * 2;
constexpr size_t WS_W1F = WS_KL + (size_t)SL * DM * 2;
constexpr size_t WS_W2F = WS_W1F + (size_t)DM * FF * 2;
constexpr size_t WS_HF  = WS_W2F + (size_t)DM * FF * 2;
constexpr size_t WS_HF2 = WS_HF + (size_t)SL * FF * 2;
constexpr size_t WS_ST0 = WS_HF2 + (size_t)SL * FF * 2;
constexpr size_t WS_ST2 = WS_ST0 + (size_t)SL * 16;
constexpr size_t WS_C1  = WS_ST2 + (size_t)SL * 16;
constexpr size_t WS_KSC = WS_C1 + (size_t)FF * 4;
constexpr size_t WS_PST = WS_KSC + (size_t)DM * 4;
constexpr size_t WS_PST2 = WS_PST + (size_t)SL * 32;
constexpr size_t WS_CNT = WS_PST2 + (size_t)SL * 32;

__host__ __device__ __forceinline__ int xoff(int d, int t) { return (t >> 5) * 16384 + d * 32 + (t & 31); }
__device__ __forceinline__ float bf2f(unsigned h) { return __uint_as_float(h << 16); }
__device__ __forceinline__ float bflo(unsigned w) { return __uint_as_float(w << 16); }
__device__ __forceinline__ float bfhi(unsigned w) { return __uint_as_float(w & 0xffff0000u); }
__device__ __forceinline__ unsigned pack_bf(float a, float b) {
  b2 v = {(__bf16)a, (__bf16)b};
  return __builtin_bit_cast(unsigned, v);
}
__device__ __forceinline__ float rbf(float f) {
  return __uint_as_float(((unsigned)__builtin_bit_cast(u16, (__bf16)f)) << 16);
}
__device__ __forceinline__ unsigned pack_h(float a, float b) {
  h2 v = {(_Float16)a, (_Float16)b};
  return __builtin_bit_cast(unsigned, v);
}
__device__ __forceinline__ float dot2(unsigned a, unsigned b, float c) {
  return __builtin_amdgcn_fdot2_f32_bf16(__builtin_bit_cast(b2, a), __builtin_bit_cast(b2, b), c, false);
}
__device__ __forceinline__ s4 tr_read(const char* p) {
  return __builtin_amdgcn_ds_read_tr16_b64_v4i16((lds_s4p)p);
}
__device__ __forceinline__ s8 cat8(s4 a, s4 b) { return __builtin_shufflevector(a, b, 0, 1, 2, 3, 4, 5, 6, 7); }
template <int CTRL>
__device__ __forceinline__ float dppf(float v) {
  return __builtin_bit_cast(float, __builtin_amdgcn_update_dpp(0, __builtin_bit_cast(int, v), CTRL, 0xf, 0xf, true));
}
__device__ __forceinline__ float shr1(float v) { return dppf<0x138>(v); }
__device__ __forceinline__ float row_sum16(float v) {
  v += dppf<0x128>(v);
  v += dppf<0x124>(v);
  v += dppf<0x122>(v);
  v += dppf<0x121>(v);
  return v;
}
constexpr unsigned BF_ONES = 0x3f803f80u;
__device__ __forceinline__ void st16(void* p, u4 v) { asm volatile("global_store_dwordx4 %0, %1, off sc1\n\ts_nop 1" :: "v"(p), "v"(v) : "memory"); }
__device__ __forceinline__ void st8(void* p, u2 v) { asm volatile("global_store_dwordx2 %0, %1, off sc1\n\ts_nop 0" :: "v"(p), "v"(v) : "memory"); }

__device__ __forceinline__ void ln_consts(float s, float ss, float& mb, float& rinv) {
  float mean = s * (1.0f / DM);
  float var = fmaxf(ss * (1.0f / DM) - mean * mean, 0.0f);
  mb = rbf(mean);
  float sd = rbf(__builtin_amdgcn_sqrtf(rbf(rbf(var) + 1e-5f)));
  const float rc = __builtin_amdgcn_rcpf(sd);
  asm("s_nop 0\n\tv_mov_b32 %0, %1" : "=v"(rinv) : "v"(rc), "v"(sd));
}

#define CODE_ADDR(var, sym) asm volatile("s_getpc_b64 s[20:21]\n\ts_add_u32 s20, s20, " sym "@rel32@lo+4\n\ts_addc_u32 s21, s21, " sym "@rel32@hi+12\n\ts_mov_b64 %0, s[20:21]" : "=s"(var) :: "s20", "s21", "scc")
#define SYM_PREP "_Z11prep_kernelPKtS0_S0_S0_S0_S0_S0_S0_Pc"
#define SYM_CONV "_Z11conv_kernelPKtPKdS0_S0_PKfS0_Pt"
#define SYM_FFN0 "_Z10ffn_kernelILb0EEvPKtS1_PKfPtPfS1_PjS4_Pd"
#define SYM_FFN1 "_Z10ffn_kernelILb1EEvPKtS1_PKfPtPfS1_PjS4_Pd"
constexpr int CL_PREP = 128, CL_CONV = 76, CL_FFN0 = 200, CL_FFN1 = 192;
__device__ __forceinline__ void touch(const char* p) {
  unsigned t;
  asm volatile("global_load_dword %0, %1, off\n\ts_waitcnt vmcnt(0)" : "=&v"(t) : "v"(p) : "memory");
}

constexpr int PREP_W1 = 56, PREP_W2 = 56, PREP_TR = 64, PREP_PF = 16;
constexpr int PK_PAR = 0, PK_PARC = 2048, PK_RED = 3072, PK_KV = PK_RED + 12288, PK_MX = PK_KV + 3072, PK_SZ = PK_MX + 128;
constexpr int PREP_SMEM = (32768 + 1024 > 2 * PK_SZ) ? 32768 + 1024 : 2 * PK_SZ;

__device__ __forceinline__ int alpha_of(int idx) {
  if (idx < 16) return (idx >> 3) * 128 + (idx & 7);
  if (idx < 24) { int j = idx - 16; return (2 + (j >> 2)) * 128 + (j & 3) * 2; }
  if (idx < 32) { int j = idx - 24; return (4 + (j >> 1)) * 128 + (j & 1) * 4; }
  if (idx < 41) return (8 + idx - 32) * 128;
  return 0;
}
__device__ __forceinline__ float exp_acc(float x) {
  const float L2EH = 1.44269504f, L2EL = (float)(1.4426950408889634 - (double)1.44269504f);
  float t = x * L2EH;
  float e = fmaf(x, L2EH, -t) + x * L2EL;
  float r = __builtin_amdgcn_exp2f(t);
  float o = fmaf(r, e * 0.69314718f, r);
  asm("" : "+v"(o) : "v"(t));
  return o;
}
__device__ __forceinline__ float row_max16(float v) {
  v = fmaxf(v, dppf<0x128>(v));
  v = fmaxf(v, dppf<0x124>(v));
  v = fmaxf(v, dppf<0x122>(v));
  v = fmaxf(v, dppf<0x121>(v));
  return v;
}
__device__ __forceinline__ void split_h(float a, float b, unsigned& hi, unsigned& lo) {
  float ah = __uint_as_float(__float_as_uint(a) & 0xffffe000u), bh = __uint_as_float(__float_as_uint(b) & 0xffffe000u);
  hi = pack_h(ah, bh);
  lo = pack_h(a - ah, b - bh);
}

__global__ __launch_bounds__(512, 4) void prep_kernel(
    const u16* __restrict__ x, const u16* __restrict__ logA, const u16* __restrict__ Aim,
    const u16* __restrict__ Cre, const u16* __restrict__ Cim, const u16* __restrict__ logdt,
    const u16* __restrict__ w1, const u16* __restrict__ w2, char* __restrict__ ws) {
  __shared__ __attribute__((aligned(16))) char smem[PREP_SMEM];
  const int tid = threadIdx.x, lane = tid & 63;
  const int wid = __builtin_amdgcn_readfirstlane(tid >> 6);
  const int b = blockIdx.x;
  const int idx = lane & 15, g = lane >> 4;
  if (b >= 256 && b < 256 + PREP_PF) {
    unsigned long long a0, a1, a2, a3;
    CODE_ADDR(a0, SYM_PREP); CODE_ADDR(a1, SYM_CONV); CODE_ADDR(a2, SYM_FFN0); CODE_ADDR(a3, SYM_FFN1);
    if (b < 256 + 8) {
      const int u = tid < CL_PREP + CL_CONV + CL_FFN0 ? tid : 0;
      touch(u < CL_PREP ? (const char*)a0 + u * 64 : (u < CL_PREP + CL_CONV ? (const char*)a1 + (u - CL_PREP) * 64 : (const char*)a2 + (u - CL_PREP - CL_CONV) * 64));
    } else {
      touch((const char*)a3 + (tid < CL_FFN1 ? tid : 0) * 64);
    }
    return;
  }
  if (b >= 256) {
    const int bj = b - 256 - PREP_PF;
    const int job = bj < PREP_W1 ? 0 : (bj < PREP_W1 + PREP_W2 ? 1 : 2);
    const int jb = job == 0 ? bj : (job == 1 ? bj - PREP_W1 : bj - PREP_W1 - PREP_W2);
    u4 v[4];
    int lofs;
    int ls1, ls2;
    {
      const u16* src;
      int gs1, gs2;
      if (job == 0) {
        src = w1 + (size_t)(tid >> 2) * FF + jb * 32 + (tid & 3) * 8;
        gs1 = 128 * FF; gs2 = 256 * FF; lofs = tid * 16; ls1 = 8192; ls2 = 16384;
      } else if (job == 1) {
        src = w2 + (size_t)((jb >> 2) * 128 + (tid >> 4)) * DM + (jb & 3) * 128 + (tid & 15) * 8;
        gs1 = 32 * DM; gs2 = 64 * DM; lofs = (tid >> 4) * 256 + (((tid & 15) ^ (((tid >> 4) & 7) << 1)) * 16); ls1 = 8192; ls2 = 16384;
      } else {
        const int r0 = (wid >> 2) * 8 + (wid & 3);
        src = x + (size_t)(jb * 32 + r0) * DM + lane * 8;
        gs1 = 4 * DM; gs2 = 16 * DM; lofs = r0 * 1024 + ((lane ^ (wid << 1)) * 16); ls1 = 4096; ls2 = 16384;
      }
      v[0] = *(const u4*)(src); v[1] = *(const u4*)(src + gs1); v[2] = *(const u4*)(src + gs2); v[3] = *(const u4*)(src + gs1 + gs2);
    }
    *(u4*)(smem + lofs) = v[0];
    *(u4*)(smem + lofs + ls1) = v[1];
    *(u4*)(smem + lofs + ls2) = v[2];
    *(u4*)(smem + lofs + ls1 + ls2) = v[3];
    __syncthreads();
    const int q = (lane >> 2) & 3, p = lane & 3;
    float* csr = (float*)(smem + 32768);
    if (job == 0) {
      const int h = lane >> 5, blk = (lane >> 4) & 1;
      const char* rb = smem + (wid * 64 + 8 * h + q) * 64 + blk * 32 + p * 8;
      u16* W1F = (u16*)(ws + WS_W1F);
      float cs = 0.f;
#pragma unroll
      for (int i = 0; i < 4; ++i) {
        s8 o = cat8(tr_read(rb + i * 1024), tr_read(rb + i * 1024 + 256));
        u4 ou = __builtin_bit_cast(u4, o);
#pragma unroll
        for (int j = 0; j < 4; ++j) cs = dot2(ou[j], BF_ONES, cs);
        st16(W1F + ((size_t)(jb * 32 + wid * 4 + i) * 64 + lane) * 8, ou);
      }
      cs += __shfl_xor(cs, 32);
      if (lane < 32) csr[wid * 32 + lane] = cs;
    } else if (job == 1) {
      const int rl = 16 * (g >> 1) + 4 * (g & 1) + q;
      const int sw = rl & 7;
      u16* W2F = (u16*)(ws + WS_W2F);
#pragma unroll
      for (int i = 0; i < 4; ++i) {
        const int nt = (wid & 1) * 4 + i;
        const char* rb = smem + ((wid >> 1) * 32 + rl) * 256 + ((nt ^ sw) * 32) + p * 8;
        s8 o = cat8(tr_read(rb), tr_read(rb + 2048));
        st16(W2F + ((size_t)(((jb & 3) * 8 + nt) * 56 + (jb >> 2) * 4 + (wid >> 1)) * 64 + lane) * 8, __builtin_bit_cast(u4, o));
      }
    } else if (job == 2) {
      const int sw = q | ((g & 1) << 2);
      u16* xT = (u16*)(ws + WS_XT0);
#pragma unroll
      for (int i = 0; i < 4; ++i) {
        int dt_ = wid * 4 + i;
        const char* base = smem + (8 * g + q) * 1024 + ((dt_ ^ sw) * 32) + 8 * p;
        s8 o = cat8(tr_read(base), tr_read(base + 4096));
        st16(xT + xoff(dt_ * 16 + idx, jb * 32 + 8 * g), __builtin_bit_cast(u4, o));
      }
      float sv[4], ssv[4];
#pragma unroll
      for (int i = 0; i < 4; ++i) {
        float s = 0.f, ss = 0.f;
#pragma unroll
        for (int j = 0; j < 4; ++j) { s = dot2(v[i][j], BF_ONES, s); ss = dot2(v[i][j], v[i][j], ss); }
        sv[i] = row_sum16(s); ssv[i] = row_sum16(ss);
      }
      float val = sv[0];
#pragma unroll
      for (int i = 1; i < 4; ++i) val = (idx == 2 * i) ? sv[i] : val;
#pragma unroll
      for (int i = 0; i < 4; ++i) val = (idx == 2 * i + 1) ? ssv[i] : val;
      val += __shfl_xor(val, 16);
      val += __shfl_xor(val, 32);
      if (lane < 8) {
        const int i = lane >> 1;
        const int r = jb * 32 + (i >> 1) * 16 + (wid >> 2) * 8 + (i & 1) * 4 + (wid & 3);
        ((double*)(ws + WS_ST0))[r * 2 + (lane & 1)] = (double)val;
        ((double*)(ws + WS_ST2))[r * 2 + (lane & 1)] = 0.0;
      }
    }

    if (job == 0) {
      __syncthreads();
      if (tid < 32) {
        float cs = 0.f;
#pragma unroll
        for (int i = 0; i < 8; ++i) cs += csr[i * 32 + tid];
        ((float*)(ws + WS_C1))[jb * 32 + tid] = cs;
      }
    }
    if (bj == 0 && tid >= 64 && tid < 96) ((unsigned*)(ws + WS_CNT))[(tid - 64) * 32] = 0u;
    return;
  }
  const int ch = wid >> 2, w4 = wid & 3, t8 = tid & 255;
  const int d = b * 2 + ch;
  const int pn = d * NS + (t8 & (NS - 1));
  const u16 pl0 = logdt[d], pl1 = logA[pn], pl2 = Aim[pn], pl3 = Cre[pn], pl4 = Cim[pn];
  SCHED;
  int kidx[8];
#pragma unroll
  for (int j = 0; j < 8; ++j) {
    float pf = rbf((float)(t8 * 8 + j));
    int p = (int)pf;
    int e = (int)(__float_as_uint(pf) >> 23) - (127 + 7);
    e = e < 0 ? 0 : (e > 3 ? 3 : e);
    int ai_ = ((((p >> 7) << 3) + (p & 7)) >> e) + 8 * e;
    kidx[j] = ch * PK_SZ + PK_KV + (ai_ * 16 + ((p & 127) >> 3)) * 4;
    asm volatile("" : "+v"(kidx[j]));
  }
  char* ks = smem + ch * PK_SZ;
  f4* par = (f4*)(ks + PK_PAR);
  f2* parc = (f2*)(ks + PK_PARC);
  f4* red = (f4*)(ks + PK_RED);
  float* kv = (float*)(ks + PK_KV);
  float* mc = (float*)(ks + PK_MX);
  SCHED;
  {
    float dt = rbf(exp_acc(bf2f(pl0)));
    float Ar = -rbf(exp_acc(bf2f(pl1)));
    float ar = Ar * dt;
    float ai = bf2f(pl2) * dt;
    const float I2PH = 0.15915494f;
    const float I2PL = (float)(0.15915494309189535 - (double)0.15915494f);
    float fh = ai * I2PH;
    float fl = fmaf(ai, I2PH, -fh) + ai * I2PL;
    fh -= rintf(fh);
    float fhi = __uint_as_float(__float_as_uint(fh) & 0xfffff000u);
    float flo = (fh - fhi) + fl;
    float crv = 2.0f * bf2f(pl3) * dt;
    float civ = 2.0f * bf2f(pl4) * dt;
    float cm = row_sum16(fabsf(crv) + fabsf(civ));
    float p0 = ar * 1.44269504f;
    asm volatile("" : "+v"(p0), "+v"(fhi), "+v"(flo), "+v"(cm));
    if (w4 < 2) {
      par[t8] = f4{p0, fhi, flo, 0.f};
      parc[t8] = f2{crv, civ};
      if (idx == 0) mc[t8 >> 4] = cm;
    }
  }
  __syncthreads();
  int ce;
  {
    f4 m0 = *(const f4*)(mc), m1 = *(const f4*)(mc + 4);
    float m = ((m0[0] + m0[1]) + (m0[2] + m0[3])) + ((m1[0] + m1[1]) + (m1[2] + m1[3]));
    ce = (int)((__float_as_uint(m) >> 23) & 0xff) - 127;
    ce = ce < -100 ? -100 : (ce > 100 ? 100 : ce);
  }
  {
    const f2 pos01 = {(float)(8 * idx), (float)alpha_of(idx)};
    const f2 pos23 = {(float)alpha_of(16 + idx), (float)alpha_of(32 + idx)};
    const f2 eo01 = {(float)(12 - ce), 13.f}, eo23 = {13.f, 13.f};
    f4 acc0 = {0, 0, 0, 0}, acc1 = {0, 0, 0, 0}, acc2 = {0, 0, 0, 0};
#pragma unroll
    for (int k2 = 0; k2 < 2; ++k2) {
      u4 hi[4], lo[4];
#pragma unroll
      for (int e = 0; e < 4; ++e) {
        const int n = (w4 * 2 + k2) * 16 + 4 * g + e;
        const f4 pp = par[n];
        const f2 pc = parc[n];
        f2 t01 = pos01 * pp[1], t23 = pos23 * pp[1];
        f2 r01 = {__builtin_amdgcn_fractf(t01[0]), __builtin_amdgcn_fractf(t01[1])};
        f2 r23 = {__builtin_amdgcn_fractf(t23[0]), __builtin_amdgcn_fractf(t23[1])};
        r01 = __builtin_elementwise_fma(pos01, f2{pp[2], pp[2]}, r01);
        r23 = __builtin_elementwise_fma(pos23, f2{pp[2], pp[2]}, r23);
        f2 a01 = __builtin_elementwise_fma(pos01, f2{pp[0], pp[0]}, eo01);
        f2 a23 = __builtin_elementwise_fma(pos23, f2{pp[0], pp[0]}, eo23);
        float rv[4] = {r01[0], r01[1], r23[0], r23[1]};
        float av[4] = {a01[0], a01[1], a23[0], a23[1]};
#pragma unroll
        for (int qq = 0; qq < 4; ++qq) {
          float ex = __builtin_amdgcn_exp2f(av[qq]);
          float cs = __builtin_amdgcn_cosf(rv[qq]), sn = __builtin_amdgcn_sinf(rv[qq]);
          f2 z;
          if (qq == 0) {
            f2 t = f2{cs, sn} * pc[0];
            t = __builtin_elementwise_fma(f2{-sn, cs}, f2{pc[1], pc[1]}, t);
            z = t * ex;
          } else {
            z = f2{cs, -sn} * ex;
          }
          asm("" : "+v"(z) : "v"(rv[qq]), "v"(av[qq]));
          unsigned hh, ll;
          split_h(z[0], z[1], hh, ll);
          hi[qq][e] = hh; lo[qq][e] = ll;
        }
      }
      const h8 bh = __builtin_bit_cast(h8, hi[0]), bl = __builtin_bit_cast(h8, lo[0]);
      acc0 = __builtin_amdgcn_mfma_f32_16x16x32_f16(__builtin_bit_cast(h8, lo[1]), bh, acc0, 0, 0, 0);
      acc1 = __builtin_amdgcn_mfma_f32_16x16x32_f16(__builtin_bit_cast(h8, lo[2]), bh, acc1, 0, 0, 0);
      acc2 = __builtin_amdgcn_mfma_f32_16x16x32_f16(__builtin_bit_cast(h8, lo[3]), bh, acc2, 0, 0, 0);
      acc0 = __builtin_amdgcn_mfma_f32_16x16x32_f16(__builtin_bit_cast(h8, hi[1]), bl, acc0, 0, 0, 0);
      acc1 = __builtin_amdgcn_mfma_f32_16x16x32_f16(__builtin_bit_cast(h8, hi[2]), bl, acc1, 0, 0, 0);
      acc2 = __builtin_amdgcn_mfma_f32_16x16x32_f16(__builtin_bit_cast(h8, hi[3]), bl, acc2, 0, 0, 0);
      acc0 = __builtin_amdgcn_mfma_f32_16x16x32_f16(__builtin_bit_cast(h8, hi[1]), bh, acc0, 0, 0, 0);
      acc1 = __builtin_amdgcn_mfma_f32_16x16x32_f16(__builtin_bit_cast(h8, hi[2]), bh, acc1, 0, 0, 0);
      acc2 = __builtin_amdgcn_mfma_f32_16x16x32_f16(__builtin_bit_cast(h8, hi[3]), bh, acc2, 0, 0, 0);
    }
    red[(w4 * 3 + 0) * 64 + lane] = acc0;
    red[(w4 * 3 + 1) * 64 + lane] = acc1;
    red[(w4 * 3 + 2) * 64 + lane] = acc2;
  }
  __syncthreads();
  if (w4 < 3) {
    f4 vv = red[(0 * 3 + w4) * 64 + lane] + red[(1 * 3 + w4) * 64 + lane] +
            (red[(2 * 3 + w4) * 64 + lane] + red[(3 * 3 + w4) * 64 + lane]);
#pragma unroll
    for (int r = 0; r < 4; ++r) kv[(16 * w4 + 4 * g + r) * 16 + idx] = vv[r] * (1.0f / 2048.0f);
  }
  if (t8 == 0) ((float*)(ws + WS_KSC))[d] = __uint_as_float((unsigned)(127 + ce - 14) << 23);
  __syncthreads();
  u4 hi, lo;
#pragma unroll
  for (int j = 0; j < 4; ++j) {
    if (KTERMS > 1) {
      unsigned hh, ll;
      split_h(*(const float*)(smem + kidx[2 * j]), *(const float*)(smem + kidx[2 * j + 1]), hh, ll);
      hi[j] = hh; lo[j] = ll;
    } else {
      hi[j] = pack_h(*(const float*)(smem + kidx[2 * j]), *(const float*)(smem + kidx[2 * j + 1]));
    }
  }
  st16(ws + WS_KH + ((size_t)d * SL + t8 * 8) * 2, hi);
  if (KTERMS > 1) st16(ws + WS_KL + ((size_t)d * SL + t8 * 8) * 2, lo);
}

constexpr int CV_U = 2064 * 2;
constexpr int CV_K = 2160 * 2;
constexpr int CV_OFF_K = 2 * CV_U;
constexpr int CV_OFF_Y = CV_OFF_K + 2 * KTERMS * CV_K;
constexpr int CV_Q = (SL + 4) * 4;
constexpr int CV_SMEM = CV_OFF_Y + 2 * 4 * CV_Q;

constexpr int CV_RPF = 2 + 2 * KTERMS;
constexpr int CV_R0 = (KTERMS == 2 ? 6 : 10) * CV_RPF;
constexpr int CV_NM = 68 * KTERMS;
struct CvStep { int a, n, k, first, op, arg, arg2; };
template <int SUB>
struct CvSched {
  CvStep s[CV_NM];
  int wt, wc, slack;
  constexpr CvSched() : s{}, wt(-1), wc(4), slack(1000) {
    const int AMAX = SUB == 0 ? 15 : 14;
    const int NR = (AMAX + 1) * CV_RPF;
    int nr = CV_R0, mi = 0;
    int endat[16] = {};
    for (int a = 0; a < 16; ++a) endat[a] = 1 << 20;
    for (int a = 0; a <= AMAX; ++a) {
      if ((((a + 1) >> 1) & 1) != SUB) continue;
      for (int nn = 0; nn <= a; ++nn) {
        const int n = (a == 0) ? 0 : (nn + 1 < a ? nn + 1 : (nn + 1 == a ? 0 : a));
        for (int k = 0; k < KTERMS; ++k) {
          CvStep st{a, n, k, (nn == 0 && k == 0) ? 1 : 0, 0, 0, 0};
          { const int need = ((n > a - n ? n : a - n) + 1) * CV_RPF; if (nr < NR && nr - need < slack) slack = nr - need; }
          ++mi;
          if (wc == 4) {
            for (int t = wt + 1; t < 16; ++t)
              if (endat[t] < (1 << 20)) { if (endat[t] + 8 <= mi) { wt = t; wc = 0; } break; }
          }
          if (wc < 4) { st.op = 2; st.arg = wt; st.arg2 = wc; ++wc; }
          else if (nr < NR) { st.op = 1; st.arg = nr; ++nr; }
          s[mi - 1] = st;
        }
      }
      endat[a] = mi;
    }
  }
};
template <int R>
__device__ __forceinline__ void cv_rd(s4 (&A)[16][KTERMS][2], s4 (&B)[16][2], const char* ub, const char* kb) {
  constexpr int i = R / CV_RPF, part = R % CV_RPF;
  if constexpr (part < 2) B[i][part] = tr_read(ub + 256 * i + 32 * part);
  else A[i][(part - 2) >> 1][part & 1] = tr_read(kb + ((part - 2) >> 1) * CV_K + 256 * i + 32 * (1 - (part & 1)));
}
template <int SUB, int I>
__device__ __forceinline__ void cv_step(s4 (&A)[16][KTERMS][2], s4 (&B)[16][2], f4 (&acc)[16], const char* ub, const char* kb, char* qb) {
  constexpr CvSched<SUB> S{};
  constexpr CvStep st = S.s[I];
  const s8 av = cat8(A[st.a - st.n][st.k][0], A[st.a - st.n][st.k][1]), bv = cat8(B[st.n][0], B[st.n][1]);
  if constexpr (st.first) asm volatile("v_mfma_f32_16x16x32_f16 %0, %1, %2, 0" : "=&v"(acc[st.a]) : "v"(av), "v"(bv));
  else asm volatile("v_mfma_f32_16x16x32_f16 %0, %1, %2, %0" : "+v"(acc[st.a]) : "v"(av), "v"(bv));
  if constexpr (st.op == 2) *(float*)(qb + st.arg2 * (CV_Q + 4) + st.arg * 512) = acc[st.arg][st.arg2];
  else if constexpr (st.op == 1) cv_rd<st.arg>(A, B, ub, kb);
  SCHED;
}
template <int SUB, int... Rs, int... Is>
__device__ __forceinline__ void conv_mfma_impl(const char* ub, const char* kb, char* qb, iseq<Rs...>, iseq<Is...>) {
  s4 A[16][KTERMS][2];
  s4 B[16][2];
  f4 acc[16];
  (cv_rd<Rs>(A, B, ub, kb), ...);
  SCHED;
  (cv_step<SUB, Is>(A, B, acc, ub, kb, qb), ...);
  asm volatile("s_nop 7");
  SCHED;
  constexpr CvSched<SUB> S{};
  static_assert(S.slack >= 4, "fragment read scheduled too late");
#pragma unroll
  for (int t = 0; t < 16; ++t) {
    if ((((t + 1) >> 1) & 1) != SUB) continue;
#pragma unroll
    for (int r = 0; r < 4; ++r)
      if (t > S.wt || (t == S.wt && r >= S.wc)) *(float*)(qb + r * (CV_Q + 4) + t * 512) = acc[t][r];
  }
}
template <int SUB>
__device__ __forceinline__ void conv_mfma(const char* ub, const char* kb, char* qb) {
  conv_mfma_impl<SUB>(ub, kb, qb, make_iseq<CV_R0>{}, make_iseq<CV_NM>{});
}

__global__ __launch_bounds__(512, 2) void conv_kernel(
    const u16* __restrict__ xT, const double* __restrict__ stats, const u16* __restrict__ Kh,
    const u16* __restrict__ Kl, const float* __restrict__ ksc, const u16* __restrict__ dskip,
    u16* __restrict__ outT) {
  extern __shared__ __attribute__((aligned(16))) char smem[];
  const int tid = threadIdx.x, lane = tid & 63;
  const int wid = __builtin_amdgcn_readfirstlane(tid >> 6);
  const int d0 = blockIdx.x * 2;
  const int t0 = tid * 4;
  u2 xr[2];
  u4 kr[KTERMS];
  d2 sr[4];
#pragma unroll
  for (int i = 0; i < 4; ++i) sr[i] = *(const d2*)(stats + t0 * 2 + i * 2);
#pragma unroll
  for (int c = 0; c < 2; ++c) xr[c] = *(const u2*)(xT + xoff(d0 + c, t0));
  kr[0] = *(const u4*)(Kh + (size_t)d0 * SL + tid * 8);
  if (KTERMS > 1) kr[KTERMS - 1] = *(const u4*)(Kl + (size_t)d0 * SL + tid * 8);
  const float2 isc2 = *(const float2*)(ksc + d0);
  const unsigned ds2 = *(const unsigned*)(dskip + d0);
  if (tid < 14 * 2 * KTERMS) {
    int which = tid / 14, j = tid % 14;
    *(u4*)(smem + CV_OFF_K + which * CV_K + j * 16) = u4{0, 0, 0, 0};
  } else if (tid >= 64 && tid < 70) {
    int c = (tid - 64) / 3, j = (tid - 64) % 3;
    *(u2*)(smem + c * CV_U + j * 8) = u2{0, 0};
  } else if (tid >= 128 && tid < 136) {
    *(f4*)(smem + CV_OFF_Y + (tid - 128) * CV_Q) = f4{0, 0, 0, 0};
  }
  float mb[4], ri[4];
#pragma unroll
  for (int j = 0; j < 4; ++j) ln_consts((float)sr[j][0], (float)sr[j][1], mb[j], ri[j]);
  u2 ub16[2];
#pragma unroll
  for (int c = 0; c < 2; ++c) {
    unsigned d01 = pack_bf(bflo(xr[c][0]) - mb[0], bfhi(xr[c][0]) - mb[1]);
    unsigned d23 = pack_bf(bflo(xr[c][1]) - mb[2], bfhi(xr[c][1]) - mb[3]);
    ub16[c][0] = pack_bf(bflo(d01) * ri[0], bfhi(d01) * ri[1]);
    ub16[c][1] = pack_bf(bflo(d23) * ri[2], bfhi(d23) * ri[3]);
    u2 a = {pack_h(bflo(ub16[c][0]), bfhi(ub16[c][0])), pack_h(bflo(ub16[c][1]), bfhi(ub16[c][1]))};
    *(u2*)(smem + c * CV_U + 24 + tid * 8) = a;
  }
#pragma unroll
  for (int k = 0; k < KTERMS; ++k)
    *(u4*)(smem + CV_OFF_K + ((tid >> 8) * KTERMS + k) * CV_K + 224 + (tid & 255) * 16) = kr[k];
  __syncthreads();
  {
    const int ch = wid >> 2, par = (wid >> 1) & 1;
    const int g = lane >> 4, q = (lane >> 2) & 3, p = lane & 3;
    const char* ub = smem + ch * CV_U + 8 * (p + q + 8 * g);
    const char* kb = smem + CV_OFF_K + ch * KTERMS * CV_K + 8 * (31 + 16 * par + 4 * p - 8 * g - q) - 32;
    char* qb = smem + CV_OFF_Y + ch * 4 * CV_Q + par * 256 + lane * 4;
    if (wid & 1) conv_mfma<1>(ub, kb, qb);
    else conv_mfma<0>(ub, kb, qb);
  }
  __syncthreads();
#pragma unroll
  for (int c = 0; c < 2; ++c) {
    const char* Q = smem + CV_OFF_Y + c * 4 * CV_Q + t0 * 4;
    f4 y0 = (*(const f4*)(Q) + *(const f4*)(Q + CV_Q)) + (*(const f4*)(Q + 2 * CV_Q) + *(const f4*)(Q + 3 * CV_Q));
    float isc = c ? isc2.y : isc2.x;
    float ds = c ? bfhi(ds2) : bflo(ds2);
    unsigned y01 = pack_bf(y0[0] * isc, y0[1] * isc), y23 = pack_bf(y0[2] * isc, y0[3] * isc);
    unsigned s01 = pack_bf(bflo(ub16[c][0]) * ds, bfhi(ub16[c][0]) * ds), s23 = pack_bf(bflo(ub16[c][1]) * ds, bfhi(ub16[c][1]) * ds);
    unsigned a01 = pack_bf(bflo(y01) + bflo(s01), bfhi(y01) + bfhi(s01)), a23 = pack_bf(bflo(y23) + bflo(s23), bfhi(y23) + bfhi(s23));
    u2 ov = {pack_bf(bflo(xr[c][0]) + bflo(a01), bfhi(xr[c][0]) + bfhi(a01)), pack_bf(bflo(xr[c][1]) + bflo(a23), bfhi(xr[c][1]) + bfhi(a23))};
    st8(outT + xoff(d0 + c, t0), ov);
  }
}

constexpr int G1_SMEM = 512 * 256 + 512 * 8;

__device__ __forceinline__ void glds16(const void* gsrc, unsigned lds_dst) {
  unsigned keep;
  asm volatile("s_mov_b32 %0, m0\n\ts_mov_b32 m0, %2\n\ts_nop 0\n\tglobal_load_lds_dwordx4 %1, off\n\ts_mov_b32 m0, %0"
               : "=&s"(keep) : "v"(gsrc), "s"(lds_dst) : "memory");
}
template <int OFF>
__device__ __forceinline__ void gload16(u4& dst, const void* p) {
  asm volatile("global_load_dwordx4 %0, %1, off offset:%2" : "=v"(dst) : "v"(p), "i"(OFF) : "memory");
}
template <int N>
__device__ __forceinline__ void wait_only(u4& a) {
  asm volatile("s_waitcnt vmcnt(%1)" : "+v"(a) : "i"(N) : "memory");
}
template <int N>
__device__ __forceinline__ void wait_vm() { asm volatile("s_waitcnt vmcnt(%0)" :: "i"(N) : "memory"); }
__device__ __forceinline__ void add1(float& a, float b) { asm("v_add_f32 %0, %0, %1" : "+v"(a) : "v"(b)); }
__device__ __forceinline__ void sqa1(float& a, float b) { asm("v_fmac_f32 %0, %1, %1" : "+v"(a) : "v"(b)); }
__device__ __forceinline__ void bar() { asm volatile("s_waitcnt lgkmcnt(0)\n\ts_barrier" ::: "memory"); }
constexpr int G1_ST = 4 * 32768;
constexpr int F_BPRE = 5;
constexpr int G1_XPF = 3;
constexpr int G1_WPF = 8;

template <int OFF>
__device__ __forceinline__ void gload16_sc1(u4& dst, const void* p) {
  asm volatile("global_load_dwordx4 %0, %1, off offset:%2 sc1" : "=v"(dst) : "v"(p), "i"(OFF) : "memory");
}
template <int N>
__device__ __forceinline__ void wait4(u4 (&a)[4]) {
  asm volatile("s_waitcnt vmcnt(%4)" : "+v"(a[0]), "+v"(a[1]), "+v"(a[2]), "+v"(a[3]) : "i"(N) : "memory");
}

template <bool FINAL>
__global__ __launch_bounds__(512, 2) void ffn_kernel(
    const u16* __restrict__ xT, const u16* __restrict__ W1F, const float* __restrict__ c1,
    u16* HF, float* pst, const u16* __restrict__ W2F, unsigned* cnt,
    u16* __restrict__ out, double* __restrict__ stats) {
  extern __shared__ __attribute__((aligned(16))) char smem[];
  const int tid = threadIdx.x, lane = tid & 63;
  const int wid = __builtin_amdgcn_readfirstlane(tid >> 6);
  const int bx = blockIdx.x, xj = bx & 7, r = bx >> 3;
  const int tp = 4 * (xj >> 1) + (r & 3);
  const int fp = 7 * (xj & 1) + (r >> 2);
  if (r < 28) {
  const int h = lane >> 5, blk = (lane >> 4) & 1, q = (lane >> 2) & 3, p = lane & 3;
  const char* rbase = smem + (8 * h + q) * 64 + blk * 32 + 8 * p;
  float* st = (float*)(smem + G1_ST);
  if (wid >= 4) {
    const int hq = wid - 4;
    const char* xsrc = (const char*)xT + (size_t)tp * 131072 + hq * 1024 + lane * 16;
    const unsigned ldst = (unsigned)(size_t)smem + hq * 1024;
    auto issue_c = [&](int c) AINL {
#pragma unroll
      for (int i = 0; i < 3; ++i) glds16(xsrc + c * 4096 + i * 32768, ldst + c * 4096 + i * 32768);
    };
#pragma unroll
    for (int c = 0; c < G1_XPF; ++c) issue_c(c);
#pragma unroll 1
    for (int c = 0; c < 8 - G1_XPF; ++c) {
      wait_vm<3 * (G1_XPF - 1)>(); bar();
      issue_c(c + G1_XPF);
    }
    sfor<G1_XPF>([&](auto I) AINL {
      constexpr int i = decltype(I)::value;
      if constexpr (i == 0) wait_vm<3 * (G1_XPF - 1)>(); else wait_vm<3 * (G1_XPF - 1 - i) + 8>();
      bar();
      if constexpr (i == 0) {
#pragma unroll
        for (int k = 0; k < 8; ++k) glds16(xsrc + 3 * 32768 + k * 4096, ldst + 3 * 32768 + k * 4096);
      }
    });
    wait_vm<0>();
    bar();
#pragma unroll 1
    for (int it = 0; it < 2; ++it) {
      const int u = it == 0 ? 12 + hq : (fp - 1) * 4 + hq;
      if (it == 1 && (fp < 1 || fp > 3)) break;
      const char* rq = rbase + u * 8192;
      s4 fr[8][2];
      sfor<8>([&](auto I) AINL {
        constexpr int i = decltype(I)::value;
        fr[i][0] = tr_read(rq + i * 1024);
        fr[i][1] = tr_read(rq + i * 1024 + 256);
      });
      float sa = 0.f, sb = 0.f, qa = 0.f, qb = 0.f;
      sfor<8>([&](auto I) AINL {
        constexpr int i = decltype(I)::value;
        u4 xv = __builtin_bit_cast(u4, cat8(fr[i][0], fr[i][1]));
#pragma unroll
        for (int j = 0; j < 4; ++j) {
          float lo = bflo(xv[j]), hi = bfhi(xv[j]);
          add1(sa, lo); add1(sb, hi); sqa1(qa, lo); sqa1(qb, hi);
        }
      });
      float s0 = sa + sb, ss0 = qa + qb;
      s0 += __shfl_xor(s0, 32);
      ss0 += __shfl_xor(ss0, 32);
      if (it == 0) {
        if (lane < 32) st[128 + hq * 32 + lane] = s0;
        bar();
      }
      if (lane < 32 && (it == 1 || fp == 0))
        st8(pst + ((tp * 128 + (u >> 2) * 32 + lane) * 4 + (u & 3)) * 2, u2{__float_as_uint(s0), __float_as_uint(ss0)});
    }
  } else {
  const int ft = fp * 4 + wid;
  u4 wf[32];
  u4 cfu[4];
  const char* wsrc = (const char*)W1F + ((size_t)ft * 32 * 64 + lane) * 16;
  auto issue_w = [&](auto S) AINL {
    constexpr int s = decltype(S)::value;
    gload16<(s & 3) * 1024>(wf[s], wsrc + (s >> 2) * 4096);
  };
  sfor<G1_WPF>(issue_w);
  f16v acc[4], asum;
  s4 xa[3][3][2];
  constexpr int XD = 4, XR = XD + 1;
  s4 xc[XR][2];
  u4 ov;
  float nm[4];
  const int w3 = wid == 3 ? 0 : wid;
  int tto[3];
#pragma unroll
  for (int i = 0; i < 3; ++i) { int tt = i + w3; tto[i] = tt >= 3 ? tt - 3 : tt; }
  const char* rb[3];
#pragma unroll
  for (int i = 0; i < 3; ++i) rb[i] = rbase + tto[i] * 32768;
  u16* dst = HF + ((size_t)((tp * 8 + blk) * 56 + ft) * 64 + (lane & 15) + 16 * h) * 8;
  auto epi_pair = [&](const f16v& a, float m, int e) AINL -> unsigned {
    f4 cf = __builtin_bit_cast(f4, cfu[e >> 2]);
    unsigned pu = pack_bf(fmaf(cf[e & 3], m, a[e]), fmaf(cf[(e & 3) + 1], m, a[e + 1]));
    asm("" : "+v"(pu));
    return __builtin_bit_cast(unsigned, __builtin_elementwise_max(__builtin_bit_cast(s2v, pu), s2v{0, 0}));
  };
  f16v z;
#pragma unroll
  for (int j = 0; j < 16; ++j) z[j] = 0.f;
  const s8 ones8 = {0x3f80, 0x3f80, 0x3f80, 0x3f80, 0x3f80, 0x3f80, 0x3f80, 0x3f80};
  SCHED;
  asm volatile("s_barrier" ::: "memory");
#pragma unroll
  for (int k = 0; k < 2; ++k)
#pragma unroll
    for (int i = 0; i < 3; ++i) { xa[k][i][0] = tr_read(rb[i] + k * 1024); xa[k][i][1] = tr_read(rb[i] + k * 1024 + 256); }
  SCHED;
  sfor<32>([&](auto S) AINL {
    constexpr int s = decltype(S)::value;
    constexpr int issued = (G1_WPF + s <= 32) ? G1_WPF + s : 36;
    asum = __builtin_amdgcn_mfma_f32_32x32x16_bf16(__builtin_bit_cast(b8, ones8), __builtin_bit_cast(b8, cat8(xa[s % 3][0][0], xa[s % 3][0][1])), s == 0 ? z : asum, 0, 0, 0);
    wait_only<issued - s - 1>(wf[s]);
    if constexpr ((s & 3) == 2 && s < 30) asm volatile("s_barrier" ::: "memory");
    SCHED;
    sfor<3>([&](auto I) AINL {
      constexpr int i = decltype(I)::value;
      const b8 xv = __builtin_bit_cast(b8, cat8(xa[s % 3][i][0], xa[s % 3][i][1]));
      acc[i] = __builtin_amdgcn_mfma_f32_32x32x16_bf16(__builtin_bit_cast(b8, wf[s]), xv, s == 0 ? z : acc[i], 0, 0, 0);
      if constexpr (s + 2 < 32) {
        xa[(s + 2) % 3][i][0] = tr_read(rb[i] + (s + 2) * 1024);
        xa[(s + 2) % 3][i][1] = tr_read(rb[i] + (s + 2) * 1024 + 256);
      }
      if constexpr (i == 0) {
        if constexpr (G1_WPF + s < 32) issue_w(ic<(G1_WPF + s < 32) ? G1_WPF + s : 0>{});
        if constexpr (G1_WPF + s == 32) {
          gload16<0>(cfu[0], c1 + ft * 32 + 4 * h);
          gload16<32>(cfu[1], c1 + ft * 32 + 4 * h);
          gload16<64>(cfu[2], c1 + ft * 32 + 4 * h);
          gload16<96>(cfu[3], c1 + ft * 32 + 4 * h);
        }
      }
      SCHED;
    });
  });
  if (lane < 32) st[wid * 32 + lane] = -rbf(asum[0] * (1.0f / DM));
  asm volatile("s_waitcnt vmcnt(0) lgkmcnt(0)\n\ts_barrier" : "+v"(cfu[0]), "+v"(cfu[1]), "+v"(cfu[2]), "+v"(cfu[3]) :: "memory");
  {
    unsigned rbo = (unsigned)(rbase - smem) + 3 * 32768;
    asm volatile("" : "+v"(rbo));
    const char* rbb = smem + rbo;
    sfor<XD>([&](auto I) AINL {
      constexpr int i = decltype(I)::value;
      xc[i][0] = tr_read(rbb + i * 1024); xc[i][1] = tr_read(rbb + i * 1024 + 256);
    });
#pragma unroll
    for (int i = 0; i < 3; ++i) nm[i] = st[tto[i] * 32 + (lane & 31)];
    SCHED;
    sfor<32>([&](auto S) AINL {
      constexpr int s = decltype(S)::value;
      const b8 xv = __builtin_bit_cast(b8, cat8(xc[s % XR][0], xc[s % XR][1]));
      acc[3] = __builtin_amdgcn_mfma_f32_32x32x16_bf16(__builtin_bit_cast(b8, wf[s]), xv, s == 0 ? z : acc[3], 0, 0, 0);
      if constexpr (s + XD < 32) {
        xc[(s + XD) % XR][0] = tr_read(rbb + (s + XD) * 1024);
        xc[(s + XD) % XR][1] = tr_read(rbb + (s + XD) * 1024 + 256);
      }
      if constexpr (s < 24) {
        constexpr int i = s >> 3, pr = s & 7;
        ov[pr & 3] = epi_pair(acc[i], nm[i], 2 * pr);
        if constexpr ((pr & 3) == 3) st16(dst + tto[i] * (2 * 56 * 512) + (pr >> 2) * 256, ov);
      }
      SCHED;
    });
  }
  asm volatile("s_barrier" ::: "memory");
  {
    const float* sp = st + 128 + (lane & 31);
    nm[3] = -rbf(((sp[0] + sp[32]) + (sp[64] + sp[96])) * (1.0f / DM));
  }
#pragma unroll
  for (int c = 0; c < 2; ++c) {
#pragma unroll
    for (int j = 0; j < 4; ++j) ov[j] = epi_pair(acc[3], nm[3], 8 * c + 2 * j);
    st16(dst + 3 * (2 * 56 * 512) + c * 256, ov);
  }
  }
  }
  const int tb = 8 * (xj >> 1) + (r & 7), db = 4 * (xj & 1) + (r >> 3);
  const int g = lane >> 4, n = lane & 15;
  const int mo = wid >> 1, no = 2 * (wid & 1);
  u4 bf[7][4], af[7][4];
  {
    const char* bp[4];
#pragma unroll
    for (int j = 0; j < 4; ++j) bp[j] = (const char*)W2F + ((size_t)((db * 4 + ((j + no) & 3)) * 56 + wid * 7) * 64 + lane) * 16;
    auto issue_b = [&](auto KS) AINL {
      constexpr int ks = decltype(KS)::value;
      sfor<4>([&](auto J) AINL {
        constexpr int j = decltype(J)::value;
        gload16<(ks & 3) * 1024>(bf[ks][j], bp[j] + (ks >> 2) * 4096);
      });
    };
    wait_vm<0>();
    bar();
    if (r < 28 && tid == 0) __hip_atomic_fetch_add(cnt + tp * 32, 1u, __ATOMIC_RELAXED, __HIP_MEMORY_SCOPE_AGENT);
    if (wid == 7) {
      const unsigned* cp = cnt + (tb >> 1) * 32;
      for (int spin = 0; spin < (1 << 22); ++spin) {
        if (__hip_atomic_load(cp, __ATOMIC_RELAXED, __HIP_MEMORY_SCOPE_AGENT) >= 14u) break;
        __builtin_amdgcn_s_sleep(2);
      }
    }
    sfor<F_BPRE>(issue_b);
    bar();
    sfor<7 - F_BPRE>([&](auto I) AINL { issue_b(ic<F_BPRE + decltype(I)::value>{}); });
  }
  u2 res[2];
  u16 resf[2][4];
  if (!FINAL) {
#pragma unroll
    for (int j = 0; j < 2; ++j) res[j] = *(const u2*)(xT + xoff(db * 64 + (no + j) * 16 + n, tb * 64 + mo * 16 + 4 * g));
  } else {
#pragma unroll
    for (int j = 0; j < 2; ++j)
#pragma unroll
      for (int e = 0; e < 4; ++e) resf[j][e] = xT[xoff(db * 64 + (no + j) * 16 + 4 * g + e, tb * 64 + mo * 16 + n)];
  }
  const unsigned long long psu = __hip_atomic_load((const unsigned long long*)(pst + ((tb * 64 + mo * 16 + n) * 4 + g) * 2), __ATOMIC_RELAXED, __HIP_MEMORY_SCOPE_AGENT);
  const char* ap[4];
#pragma unroll
  for (int i = 0; i < 4; ++i) ap[i] = (const char*)HF + ((size_t)((tb * 4 + ((i + mo) & 3)) * 56 + wid * 7) * 64 + lane) * 16;
  auto issue_a = [&](auto KS) AINL {
    constexpr int ks = decltype(KS)::value;
    sfor<4>([&](auto I) AINL {
      constexpr int i = decltype(I)::value;
      gload16_sc1<(ks & 3) * 1024>(af[ks][i], ap[i] + (ks >> 2) * 4096);
    });
  };
  sfor<4>(issue_a);
  f4 acc[4][4];
#pragma unroll
  for (int i = 0; i < 4; ++i)
#pragma unroll
    for (int j = 0; j < 4; ++j) acc[i][j] = f4{0, 0, 0, 0};
  SCHED;
  sfor<7>([&](auto KS) AINL {
    constexpr int ks = decltype(KS)::value;
    wait4<(ks < 4 ? 12 : 4 * (6 - ks))>(af[ks]);
    wait4<63>(bf[ks]);
    SCHED;
#pragma unroll
    for (int i = 0; i < 4; ++i)
#pragma unroll
      for (int j = 0; j < 4; ++j) {
        if (!FINAL) acc[i][j] = __builtin_amdgcn_mfma_f32_16x16x32_bf16(__builtin_bit_cast(b8, af[ks][i]), __builtin_bit_cast(b8, bf[ks][j]), acc[i][j], 0, 0, 0);
        else acc[i][j] = __builtin_amdgcn_mfma_f32_16x16x32_bf16(__builtin_bit_cast(b8, bf[ks][j]), __builtin_bit_cast(b8, af[ks][i]), acc[i][j], 0, 0, 0);
      }
    SCHED;
    if constexpr (ks + 4 < 7) issue_a(ic<(ks + 4 < 7) ? ks + 4 : 0>{});
    SCHED;
  });
  f4* red = (f4*)smem;
#pragma unroll
  for (int i = 0; i < 4; ++i)
#pragma unroll
    for (int jp = 0; jp < 2; ++jp) {
      if (i == 0 && jp == 0) continue;
      const int owner = ((i + mo) & 3) * 2 + ((jp + wid) & 1);
      red[((owner * 7 + i * 2 + jp - 1) * 2 + 0) * 64 + lane] = acc[i][2 * jp];
      red[((owner * 7 + i * 2 + jp - 1) * 2 + 1) * 64 + lane] = acc[i][2 * jp + 1];
    }
  f4 rr;
  {
    float sx = __uint_as_float((unsigned)psu), sy = __uint_as_float((unsigned)(psu >> 32));
    sx += __shfl_xor(sx, 16); sy += __shfl_xor(sy, 16);
    sx += __shfl_xor(sx, 32); sy += __shfl_xor(sy, 32);
    float mb, ri;
    ln_consts(sx, sy, mb, ri);
    if (FINAL) rr = f4{ri, ri, ri, ri};
    else {
#pragma unroll
      for (int e = 0; e < 4; ++e) rr[e] = __shfl(ri, 4 * g + e);
    }
  }
  bar();
  char* ot = smem + 114688;
  float s[4] = {0, 0, 0, 0}, ss[4] = {0, 0, 0, 0};
#pragma unroll
  for (int j = 0; j < 2; ++j) {
    const f4* rp = red + (wid * 14 + j) * 64 + lane;
    f4 v = ((acc[0][j] + rp[0]) + (rp[128] + rp[256])) + ((rp[384] + rp[512]) + (rp[640] + rp[768]));
    v *= rr;
    unsigned m01 = pack_bf(v[0], v[1]), m23 = pack_bf(v[2], v[3]);
    u2 ov;
    if (!FINAL) {
      ov[0] = pack_bf(bflo(res[j][0]) + bflo(m01), bfhi(res[j][0]) + bfhi(m01));
      ov[1] = pack_bf(bflo(res[j][1]) + bflo(m23), bfhi(res[j][1]) + bfhi(m23));
      *(u2*)(ot + ((mo >> 1) * 64 + (no + j) * 16 + n) * 72 + ((mo & 1) * 16 + 4 * g) * 2) = ov;
      float o[4] = {bflo(ov[0]), bfhi(ov[0]), bflo(ov[1]), bfhi(ov[1])};
#pragma unroll
      for (int e = 0; e < 4; ++e) { s[e] += o[e]; ss[e] = fmaf(o[e], o[e], ss[e]); }
    } else {
      ov[0] = pack_bf(bf2f(resf[j][0]) + bflo(m01), bf2f(resf[j][1]) + bfhi(m01));
      ov[1] = pack_bf(bf2f(resf[j][2]) + bflo(m23), bf2f(resf[j][3]) + bfhi(m23));
      *(u2*)(ot + (mo * 16 + n) * 144 + ((no + j) * 16 + 4 * g) * 2) = ov;
    }
  }
  if (!FINAL) {
#pragma unroll
    for (int e = 0; e < 4; ++e) { s[e] = row_sum16(s[e]); ss[e] = row_sum16(ss[e]); }
    if (n < 8) {
      int e = n >> 1;
      float v = (n & 1) ? (e == 0 ? ss[0] : e == 1 ? ss[1] : e == 2 ? ss[2] : ss[3]) : (e == 0 ? s[0] : e == 1 ? s[1] : e == 2 ? s[2] : s[3]);
      unsafeAtomicAdd(stats + (tb * 64 + mo * 16 + 4 * g) * 2 + n, (double)v);
    }
  }
  bar();
  if (!FINAL) {
    const int row = tid >> 2, c = tid & 3;
    const u2 lo = *(const u2*)(ot + row * 72 + c * 16), hi = *(const u2*)(ot + row * 72 + c * 16 + 8);
    st16(out + xoff(db * 64 + (row & 63), tb * 64 + (row >> 6) * 32 + c * 8), u4{lo[0], lo[1], hi[0], hi[1]});
  } else {
    const int row = tid >> 3, c = tid & 7;
    st16(out + (size_t)(tb * 64 + row) * DM + db * 64 + c * 8, *(const u4*)(ot + row * 144 + c * 16));
  }
}

extern "C" void kernel_launch(void* const* d_in, const int* in_sizes, int n_in,
                              void* d_out, int out_size, void* d_ws, size_t ws_size,
                              hipStream_t stream) {
  const u16* x = (const u16*)d_in[0];
  const u16* logA = (const u16*)d_in[1];
  const u16* Aim = (const u16*)d_in[2];
  const u16* Cre = (const u16*)d_in[3];
  const u16* Cim = (const u16*)d_in[4];
  const u16* logdt = (const u16*)d_in[5];
  const u16* dskip = (const u16*)d_in[6];
  const u16* w1 = (const u16*)d_in[8];
  const u16* w2 = (const u16*)d_in[9];
  char* ws = (char*)d_ws;
  u16* out = (u16*)d_out;
  (void)hipFuncSetAttribute((const void*)ffn_kernel<false>, hipFuncAttributeMaxDynamicSharedMemorySize, G1_SMEM);
  (void)hipFuncSetAttribute((const void*)ffn_kernel<true>, hipFuncAttributeMaxDynamicSharedMemorySize, G1_SMEM);
  (void)hipFuncSetAttribute((const void*)conv_kernel, hipFuncAttributeMaxDynamicSharedMemorySize, CV_SMEM);

  prep_kernel<<<256 + PREP_PF + PREP_W1 + PREP_W2 + PREP_TR, 512, 0, stream>>>(x, logA, Aim, Cre, Cim, logdt, w1, w2, ws);
  conv_kernel<<<256, 512, CV_SMEM, stream>>>((const u16*)(ws + WS_XT0), (const double*)(ws + WS_ST0), (const u16*)(ws + WS_KH),
                                             (const u16*)(ws + WS_KL), (const float*)(ws + WS_KSC), dskip, (u16*)(ws + WS_XT1));
  ffn_kernel<false><<<256, 512, G1_SMEM, stream>>>((const u16*)(ws + WS_XT1), (const u16*)(ws + WS_W1F), (const float*)(ws + WS_C1),
                                                   (u16*)(ws + WS_HF), (float*)(ws + WS_PST), (const u16*)(ws + WS_W2F),
                                                   (unsigned*)(ws + WS_CNT), (u16*)(ws + WS_XT2), (double*)(ws + WS_ST2));
  conv_kernel<<<256, 512, CV_SMEM, stream>>>((const u16*)(ws + WS_XT2), (const double*)(ws + WS_ST2), (const u16*)(ws + WS_KH),
                                             (const u16*)(ws + WS_KL), (const float*)(ws + WS_KSC), dskip, (u16*)(ws + WS_XT3));
  ffn_kernel<true><<<256, 512, G1_SMEM, stream>>>((const u16*)(ws + WS_XT3), (const u16*)(ws + WS_W1F), (const float*)(ws + WS_C1),
                                                  (u16*)(ws + WS_HF2), (float*)(ws + WS_PST2), (const u16*)(ws + WS_W2F),
                                                  (unsigned*)(ws + WS_CNT) + 512, out, nullptr);
}
```

```cpp
#include <hip/hip_runtime.h>
#include <hip/hip_bf16.h>

typedef unsigned short u16;
typedef short s4 __attribute__((ext_vector_type(4)));
typedef short s2v __attribute__((ext_vector_type(2)));
typedef short s8 __attribute__((ext_vector_type(8)));
typedef _Float16 h8 __attribute__((ext_vector_type(8)));
typedef _Float16 h2 __attribute__((ext_vector_type(2)));
typedef __bf16 b2 __attribute__((ext_vector_type(2)));
typedef __bf16 b8 __attribute__((ext_vector_type(8)));
typedef float f4 __attribute__((ext_vector_type(4)));
typedef float f2 __attribute__((ext_vector_type(2)));
typedef double d2 __attribute__((ext_vector_type(2)));
typedef __bf16 b4 __attribute__((ext_vector_type(4)));
typedef float f16v __attribute__((ext_vector_type(16)));
typedef unsigned u4 __attribute__((ext_vector_type(4)));
typedef unsigned u2 __attribute__((ext_vector_type(2)));
typedef __attribute__((address_space(3))) s4* lds_s4p;

#define SCHED __builtin_amdgcn_sched_barrier(0)
template <int... Is> struct iseq {};
template <int N, int... Is> struct mk_iseq : mk_iseq<N - 1, N - 1, Is...> {};
template <int... Is> struct mk_iseq<0, Is...> { typedef iseq<Is...> type; };
template <int N> using make_iseq = typename mk_iseq<N>::type;
template <int I> struct ic { static constexpr int value = I; };
template <class F, int... Is> __device__ __forceinline__ void sfor_impl(F&& f, iseq<Is...>) { (f(ic<Is>{}), ...); }
template <int N, class F> __device__ __forceinline__ void sfor(F&& f) { sfor_impl(f, make_iseq<N>{}); }
#define AINL __attribute__((always_inline))

constexpr int SL = 2048;
constexpr int DM = 512;
constexpr int NS = 128;
constexpr int FF = 1792;
#define KTERMS 1

constexpr size_t WS_XT0 = 0;
constexpr size_t WS_XT1 = WS_XT0 + (size_t)SL * DM * 2;
constexpr size_t WS_XT2 = WS_XT1 + (size_t)SL * DM * 2;
constexpr size_t WS_XT3 = WS_XT2 + (size_t)SL * DM * 2;
constexpr size_t WS_KH  = WS_XT3 + (size_t)SL * DM * 2;
constexpr size_t WS_KL  = WS_KH + (size_t)SL * DM * 2;
constexpr size_t WS_W1F = WS_KL + (size_t)SL * DM * 2;
constexpr size_t WS_W2F = WS_W1F + (size_t)DM * FF * 2;
constexpr size_t WS_HF  = WS_W2F + (size_t)DM * FF * 2;
constexpr size_t WS_HF2 = WS_HF + (size_t)SL * FF * 2;
constexpr size_t WS_ST0 = WS_HF2 + (size_t)SL * FF * 2;
constexpr size_t WS_ST2 = WS_ST0 + (size_t)SL * 16;
constexpr size_t WS_C1  = WS_ST2 + (size_t)SL * 16;
constexpr size_t WS_KSC = WS_C1 + (size_t)FF * 4;
constexpr size_t WS_PST = WS_KSC + (size_t)DM * 4;
constexpr size_t WS_PST2 = WS_PST + (size_t)SL * 32;
constexpr size_t WS_CNT = WS_PST2 + (size_t)SL * 32;

__host__ __device__ __forceinline__ int xoff(int d, int t) { return (t >> 5) * 16384 + d * 32 + (t & 31); }
__device__ __forceinline__ float bf2f(unsigned h) { return __uint_as_float(h << 16); }
__device__ __forceinline__ float bflo(unsigned w) { return __uint_as_float(w << 16); }
__device__ __forceinline__ float bfhi(unsigned w) { return __uint_as_float(w & 0xffff0000u); }
__device__ __forceinline__ unsigned pack_bf(float a, float b) {
  b2 v = {(__bf16)a, (__bf16)b};
  return __builtin_bit_cast(unsigned, v);
}
__device__ __forceinline__ float rbf(float f) {
  return __uint_as_float(((unsigned)__builtin_bit_cast(u16, (__bf16)f)) << 16);
}
__device__ __forceinline__ unsigned pack_h(float a, float b) {
  h2 v = {(_Float16)a, (_Float16)b};
  return __builtin_bit_cast(unsigned, v);
}
__device__ __forceinline__ float dot2(unsigned a, unsigned b, float c) {
  return __builtin_amdgcn_fdot2_f32_bf16(__builtin_bit_cast(b2, a), __builtin_bit_cast(b2, b), c, false);
}
__device__ __forceinline__ s4 tr_read(const char* p) {
  return __builtin_amdgcn_ds_read_tr16_b64_v4i16((lds_s4p)p);
}
__device__ __forceinline__ s8 cat8(s4 a, s4 b) { return __builtin_shufflevector(a, b, 0, 1, 2, 3, 4, 5, 6, 7); }
template <int CTRL>
__device__ __forceinline__ float dppf(float v) {
  return __builtin_bit_cast(float, __builtin_amdgcn_update_dpp(0, __builtin_bit_cast(int, v), CTRL, 0xf, 0xf, true));
}
__device__ __forceinline__ float shr1(float v) { return dppf<0x138>(v); }
__device__ __forceinline__ float row_sum16(float v) {
  v += dppf<0x128>(v);
  v += dppf<0x124>(v);
  v += dppf<0x122>(v);
  v += dppf<0x121>(v);
  return v;
}
constexpr unsigned BF_ONES = 0x3f803f80u;
__device__ __forceinline__ void st16(void* p, u4 v) { asm volatile("global_store_dwordx4 %0, %1, off sc1\n\ts_nop 1" :: "v"(p), "v"(v) : "memory"); }
__device__ __forceinline__ void st8(void* p, u2 v) { asm volatile("global_store_dwordx2 %0, %1, off sc1\n\ts_nop 0" :: "v"(p), "v"(v) : "memory"); }

__device__ __forceinline__ void ln_consts(float s, float ss, float& mb, float& rinv) {
  float mean = s * (1.0f / DM);
  float var = fmaxf(ss * (1.0f / DM) - mean * mean, 0.0f);
  mb = rbf(mean);
  float sd = rbf(__builtin_amdgcn_sqrtf(rbf(rbf(var) + 1e-5f)));
  const float rc = __builtin_amdgcn_rcpf(sd);
  asm("s_nop 0\n\tv_mov_b32 %0, %1" : "=v"(rinv) : "v"(rc), "v"(sd));
}

#define CODE_ADDR(var, sym) asm volatile("s_getpc_b64 s[20:21]\n\ts_add_u32 s20, s20, " sym "@rel32@lo+4\n\ts_addc_u32 s21, s21, " sym "@rel32@hi+12\n\ts_mov_b64 %0, s[20:21]" : "=s"(var) :: "s20", "s21", "scc")
#define SYM_PREP "_Z11prep_kernelPKtS0_S0_S0_S0_S0_S0_S0_Pc"
#define SYM_CONV "_Z11conv_kernelPKtPKdS0_S0_PKfS0_Pt"
#define SYM_FFN0 "_Z10ffn_kernelILb0EEvPKtS1_PKfPtPfS1_PjS4_Pd"
#define SYM_FFN1 "_Z10ffn_kernelILb1EEvPKtS1_PKfPtPfS1_PjS4_Pd"
constexpr int CL_PREP = 128, CL_CONV = 76, CL_FFN0 = 210, CL_FFN1 = 202;
__device__ __forceinline__ void touch(const char* p) {
  unsigned t;
  asm volatile("global_load_dword %0, %1, off\n\ts_waitcnt vmcnt(0)" : "=&v"(t) : "v"(p) : "memory");
}

constexpr int PREP_W1 = 56, PREP_W2 = 56, PREP_TR = 64, PREP_PF = 16;
constexpr int PK_PAR = 0, PK_PARC = 2048, PK_RED = 3072, PK_KV = PK_RED + 12288, PK_MX = PK_KV + 3072, PK_SZ = PK_MX + 128;
constexpr int PREP_SMEM = (32768 + 1024 > 2 * PK_SZ) ? 32768 + 1024 : 2 * PK_SZ;

__device__ __forceinline__ int alpha_of(int idx) {
  if (idx < 16) return (idx >> 3) * 128 + (idx & 7);
  if (idx < 24) { int j = idx - 16; return (2 + (j >> 2)) * 128 + (j & 3) * 2; }
  if (idx < 32) { int j = idx - 24; return (4 + (j >> 1)) * 128 + (j & 1) * 4; }
  if (idx < 41) return (8 + idx - 32) * 128;
  return 0;
}
__device__ __forceinline__ float exp_acc(float x) {
  const float L2EH = 1.44269504f, L2EL = (float)(1.4426950408889634 - (double)1.44269504f);
  float t = x * L2EH;
  float e = fmaf(x, L2EH, -t) + x * L2EL;
  float r = __builtin_amdgcn_exp2f(t);
  float o = fmaf(r, e * 0.69314718f, r);
  asm("" : "+v"(o) : "v"(t));
  return o;
}
__device__ __forceinline__ float row_max16(float v) {
  v = fmaxf(v, dppf<0x128>(v));
  v = fmaxf(v, dppf<0x124>(v));
  v = fmaxf(v, dppf<0x122>(v));
  v = fmaxf(v, dppf<0x121>(v));
  return v;
}
__device__ __forceinline__ void split_h(float a, float b, unsigned& hi, unsigned& lo) {
  float ah = __uint_as_float(__float_as_uint(a) & 0xffffe000u), bh = __uint_as_float(__float_as_uint(b) & 0xffffe000u);
  hi = pack_h(ah, bh);
  lo = pack_h(a - ah, b - bh);
}

__global__ __launch_bounds__(512, 4) void prep_kernel(
    const u16* __restrict__ x, const u16* __restrict__ logA, const u16* __restrict__ Aim,
    const u16* __restrict__ Cre, const u16* __restrict__ Cim, const u16* __restrict__ logdt,
    const u16* __restrict__ w1, const u16* __restrict__ w2, char* __restrict__ ws) {
  __shared__ __attribute__((aligned(16))) char smem[PREP_SMEM];
  const int tid = threadIdx.x, lane = tid & 63;
  const int wid = __builtin_amdgcn_readfirstlane(tid >> 6);
  const int b = blockIdx.x;
  const int idx = lane & 15, g = lane >> 4;
  if (b >= 256 && b < 256 + PREP_PF) {
    unsigned long long a0, a1, a2, a3;
    CODE_ADDR(a0, SYM_PREP); CODE_ADDR(a1, SYM_CONV); CODE_ADDR(a2, SYM_FFN0); CODE_ADDR(a3, SYM_FFN1);
    if (b < 256 + 8) {
      const int u = tid < CL_PREP + CL_CONV + CL_FFN0 ? tid : 0;
      touch(u < CL_PREP ? (const char*)a0 + u * 64 : (u < CL_PREP + CL_CONV ? (const char*)a1 + (u - CL_PREP) * 64 : (const char*)a2 + (u - CL_PREP - CL_CONV) * 64));
    } else {
      touch((const char*)a3 + (tid < CL_FFN1 ? tid : 0) * 64);
    }
    return;
  }
  if (b >= 256) {
    const int bj = b - 256 - PREP_PF;
    const int job = bj < PREP_W1 ? 0 : (bj < PREP_W1 + PREP_W2 ? 1 : 2);
    const int jb = job == 0 ? bj : (job == 1 ? bj - PREP_W1 : bj - PREP_W1 - PREP_W2);
    u4 v[4];
    int lofs;
    int ls1, ls2;
    {
      const u16* src;
      int gs1, gs2;
      if (job == 0) {
        src = w1 + (size_t)(tid >> 2) * FF + jb * 32 + (tid & 3) * 8;
        gs1 = 128 * FF; gs2 = 256 * FF; lofs = tid * 16; ls1 = 8192; ls2 = 16384;
      } else if (job == 1) {
        src = w2 + (size_t)((jb >> 2) * 128 + (tid >> 4)) * DM + (jb & 3) * 128 + (tid & 15) * 8;
        gs1 = 32 * DM; gs2 = 64 * DM; lofs = (tid >> 4) * 256 + (((tid & 15) ^ (((tid >> 4) & 7) << 1)) * 16); ls1 = 8192; ls2 = 16384;
      } else {
        const int r0 = (wid >> 2) * 8 + (wid & 3);
        src = x + (size_t)(jb * 32 + r0) * DM + lane * 8;
        gs1 = 4 * DM; gs2 = 16 * DM; lofs = r0 * 1024 + ((lane ^ (wid << 1)) * 16); ls1 = 4096; ls2 = 16384;
      }
      v[0] = *(const u4*)(src); v[1] = *(const u4*)(src + gs1); v[2] = *(const u4*)(src + gs2); v[3] = *(const u4*)(src + gs1 + gs2);
    }
    *(u4*)(smem + lofs) = v[0];
    *(u4*)(smem + lofs + ls1) = v[1];
    *(u4*)(smem + lofs + ls2) = v[2];
    *(u4*)(smem + lofs + ls1 + ls2) = v[3];
    __syncthreads();
    const int q = (lane >> 2) & 3, p = lane & 3;
    float* csr = (float*)(smem + 32768);
    if (job == 0) {
      const int h = lane >> 5, blk = (lane >> 4) & 1;
      const char* rb = smem + (wid * 64 + 8 * h + q) * 64 + blk * 32 + p * 8;
      u16* W1F = (u16*)(ws + WS_W1F);
      float cs = 0.f;
#pragma unroll
      for (int i = 0; i < 4; ++i) {
        s8 o = cat8(tr_read(rb + i * 1024), tr_read(rb + i * 1024 + 256));
        u4 ou = __builtin_bit_cast(u4, o);
#pragma unroll
        for (int j = 0; j < 4; ++j) cs = dot2(ou[j], BF_ONES, cs);
        st16(W1F + ((size_t)(jb * 32 + wid * 4 + i) * 64 + lane) * 8, ou);
      }
      cs += __shfl_xor(cs, 32);
      if (lane < 32) csr[wid * 32 + lane] = cs;
    } else if (job == 1) {
      const int rl = 16 * (g >> 1) + 4 * (g & 1) + q;
      const int sw = rl & 7;
      u16* W2F = (u16*)(ws + WS_W2F);
#pragma unroll
      for (int i = 0; i < 4; ++i) {
        const int nt = (wid & 1) * 4 + i;
        const char* rb = smem + ((wid >> 1) * 32 + rl) * 256 + ((nt ^ sw) * 32) + p * 8;
        s8 o = cat8(tr_read(rb), tr_read(rb + 2048));
        st16(W2F + ((size_t)(((jb & 3) * 8 + nt) * 56 + (jb >> 2) * 4 + (wid >> 1)) * 64 + lane) * 8, __builtin_bit_cast(u4, o));
      }
    } else if (job == 2) {
      const int sw = q | ((g & 1) << 2);
      u16* xT = (u16*)(ws + WS_XT0);
#pragma unroll
      for (int i = 0; i < 4; ++i) {
        int dt_ = wid * 4 + i;
        const char* base = smem + (8 * g + q) * 1024 + ((dt_ ^ sw) * 32) + 8 * p;
        s8 o = cat8(tr_read(base), tr_read(base + 4096));
        st16(xT + xoff(dt_ * 16 + idx, jb * 32 + 8 * g), __builtin_bit_cast(u4, o));
      }
      float sv[4], ssv[4];
#pragma unroll
      for (int i = 0; i < 4; ++i) {
        float s = 0.f, ss = 0.f;
#pragma unroll
        for (int j = 0; j < 4; ++j) { s = dot2(v[i][j], BF_ONES, s); ss = dot2(v[i][j], v[i][j], ss); }
        sv[i] = row_sum16(s); ssv[i] = row_sum16(ss);
      }
      float val = sv[0];
#pragma unroll
      for (int i = 1; i < 4; ++i) val = (idx == 2 * i) ? sv[i] : val;
#pragma unroll
      for (int i = 0; i < 4; ++i) val = (idx == 2 * i + 1) ? ssv[i] : val;
      val += __shfl_xor(val, 16);
      val += __shfl_xor(val, 32);
      if (lane < 8) {
        const int i = lane >> 1;
        const int r = jb * 32 + (i >> 1) * 16 + (wid >> 2) * 8 + (i & 1) * 4 + (wid & 3);
        ((double*)(ws + WS_ST0))[r * 2 + (lane & 1)] = (double)val;
        ((double*)(ws + WS_ST2))[r * 2 + (lane & 1)] = 0.0;
      }
    }

    if (job == 0) {
      __syncthreads();
      if (tid < 32) {
        float cs = 0.f;
#pragma unroll
        for (int i = 0; i < 8; ++i) cs += csr[i * 32 + tid];
        ((float*)(ws + WS_C1))[jb * 32 + tid] = cs;
      }
    }
    if (bj == 0 && tid >= 64 && tid < 128) ((unsigned*)(ws + WS_CNT))[(tid - 64) * 32] = 0u;
    return;
  }
  const int ch = wid >> 2, w4 = wid & 3, t8 = tid & 255;
  const int d = b * 2 + ch;
  const int pn = d * NS + (t8 & (NS - 1));
  const u16 pl0 = logdt[d], pl1 = logA[pn], pl2 = Aim[pn], pl3 = Cre[pn], pl4 = Cim[pn];
  SCHED;
  int kidx[8];
#pragma unroll
  for (int j = 0; j < 8; ++j) {
    float pf = rbf((float)(t8 * 8 + j));
    int p = (int)pf;
    int e = (int)(__float_as_uint(pf) >> 23) - (127 + 7);
    e = e < 0 ? 0 : (e > 3 ? 3 : e);
    int ai_ = ((((p >> 7) << 3) + (p & 7)) >> e) + 8 * e;
    kidx[j] = ch * PK_SZ + PK_KV + (ai_ * 16 + ((p & 127) >> 3)) * 4;
    asm volatile("" : "+v"(kidx[j]));
  }
  char* ks = smem + ch * PK_SZ;
  f4* par = (f4*)(ks + PK_PAR);
  f2* parc = (f2*)(ks + PK_PARC);
  f4* red = (f4*)(ks + PK_RED);
  float* kv = (float*)(ks + PK_KV);
  float* mc = (float*)(ks + PK_MX);
  SCHED;
  {
    float dt = rbf(exp_acc(bf2f(pl0)));
    float Ar = -rbf(exp_acc(bf2f(pl1)));
    float ar = Ar * dt;
    float ai = bf2f(pl2) * dt;
    const float I2PH = 0.15915494f;
    const float I2PL = (float)(0.15915494309189535 - (double)0.15915494f);
    float fh = ai * I2PH;
    float fl = fmaf(ai, I2PH, -fh) + ai * I2PL;
    fh -= rintf(fh);
    float fhi = __uint_as_float(__float_as_uint(fh) & 0xfffff000u);
    float flo = (fh - fhi) + fl;
    float crv = 2.0f * bf2f(pl3) * dt;
    float civ = 2.0f * bf2f(pl4) * dt;
    float cm = row_sum16(fabsf(crv) + fabsf(civ));
    float p0 = ar * 1.44269504f;
    asm volatile("" : "+v"(p0), "+v"(fhi), "+v"(flo), "+v"(cm));
    if (w4 < 2) {
      par[t8] = f4{p0, fhi, flo, 0.f};
      parc[t8] = f2{crv, civ};
      if (idx == 0) mc[t8 >> 4] = cm;
    }
  }
  __syncthreads();
  int ce;
  {
    f4 m0 = *(const f4*)(mc), m1 = *(const f4*)(mc + 4);
    float m = ((m0[0] + m0[1]) + (m0[2] + m0[3])) + ((m1[0] + m1[1]) + (m1[2] + m1[3]));
    ce = (int)((__float_as_uint(m) >> 23) & 0xff) - 127;
    ce = ce < -100 ? -100 : (ce > 100 ? 100 : ce);
  }
  {
    const f2 pos01 = {(float)(8 * idx), (float)alpha_of(idx)};
    const f2 pos23 = {(float)alpha_of(16 + idx), (float)alpha_of(32 + idx)};
    const f2 eo01 = {(float)(12 - ce), 13.f}, eo23 = {13.f, 13.f};
    f4 acc0 = {0, 0, 0, 0}, acc1 = {0, 0, 0, 0}, acc2 = {0, 0, 0, 0};
#pragma unroll
    for (int k2 = 0; k2 < 2; ++k2) {
      u4 hi[4], lo[4];
#pragma unroll
      for (int e = 0; e < 4; ++e) {
        const int n = (w4 * 2 + k2) * 16 + 4 * g + e;
        const f4 pp = par[n];
        const f2 pc = parc[n];
        f2 t01 = pos01 * pp[1], t23 = pos23 * pp[1];
        f2 r01 = {__builtin_amdgcn_fractf(t01[0]), __builtin_amdgcn_fractf(t01[1])};
        f2 r23 = {__builtin_amdgcn_fractf(t23[0]), __builtin_amdgcn_fractf(t23[1])};
        r01 = __builtin_elementwise_fma(pos01, f2{pp[2], pp[2]}, r01);
        r23 = __builtin_elementwise_fma(pos23, f2{pp[2], pp[2]}, r23);
        f2 a01 = __builtin_elementwise_fma(pos01, f2{pp[0], pp[0]}, eo01);
        f2 a23 = __builtin_elementwise_fma(pos23, f2{pp[0], pp[0]}, eo23);
        float rv[4] = {r01[0], r01[1], r23[0], r23[1]};
        float av[4] = {a01[0], a01[1], a23[0], a23[1]};
#pragma unroll
        for (int qq = 0; qq < 4; ++qq) {
          float ex = __builtin_amdgcn_exp2f(av[qq]);
          float cs = __builtin_amdgcn_cosf(rv[qq]), sn = __builtin_amdgcn_sinf(rv[qq]);
          f2 z;
          if (qq == 0) {
            f2 t = f2{cs, sn} * pc[0];
            t = __builtin_elementwise_fma(f2{-sn, cs}, f2{pc[1], pc[1]}, t);
            z = t * ex;
          } else {
            z = f2{cs, -sn} * ex;
          }
          asm("" : "+v"(z) : "v"(rv[qq]), "v"(av[qq]));
          unsigned hh, ll;
          split_h(z[0], z[1], hh, ll);
          hi[qq][e] = hh; lo[qq][e] = ll;
        }
      }
      const h8 bh = __builtin_bit_cast(h8, hi[0]), bl = __builtin_bit_cast(h8, lo[0]);
      acc0 = __builtin_amdgcn_mfma_f32_16x16x32_f16(__builtin_bit_cast(h8, lo[1]), bh, acc0, 0, 0, 0);
      acc1 = __builtin_amdgcn_mfma_f32_16x16x32_f16(__builtin_bit_cast(h8, lo[2]), bh, acc1, 0, 0, 0);
      acc2 = __builtin_amdgcn_mfma_f32_16x16x32_f16(__builtin_bit_cast(h8, lo[3]), bh, acc2, 0, 0, 0);
      acc0 = __builtin_amdgcn_mfma_f32_16x16x32_f16(__builtin_bit_cast(h8, hi[1]), bl, acc0, 0, 0, 0);
      acc1 = __builtin_amdgcn_mfma_f32_16x16x32_f16(__builtin_bit_cast(h8, hi[2]), bl, acc1, 0, 0, 0);
      acc2 = __builtin_amdgcn_mfma_f32_16x16x32_f16(__builtin_bit_cast(h8, hi[3]), bl, acc2, 0, 0, 0);
      acc0 = __builtin_amdgcn_mfma_f32_16x16x32_f16(__builtin_bit_cast(h8, hi[1]), bh, acc0, 0, 0, 0);
      acc1 = __builtin_amdgcn_mfma_f32_16x16x32_f16(__builtin_bit_cast(h8, hi[2]), bh, acc1, 0, 0, 0);
      acc2 = __builtin_amdgcn_mfma_f32_16x16x32_f16(__builtin_bit_cast(h8, hi[3]), bh, acc2, 0, 0, 0);
    }
    red[(w4 * 3 + 0) * 64 + lane] = acc0;
    red[(w4 * 3 + 1) * 64 + lane] = acc1;
    red[(w4 * 3 + 2) * 64 + lane] = acc2;
  }
  __syncthreads();
  if (w4 < 3) {
    f4 vv = red[(0 * 3 + w4) * 64 + lane] + red[(1 * 3 + w4) * 64 + lane] +
            (red[(2 * 3 + w4) * 64 + lane] + red[(3 * 3 + w4) * 64 + lane]);
#pragma unroll
    for (int r = 0; r < 4; ++r) kv[(16 * w4 + 4 * g + r) * 16 + idx] = vv[r] * (1.0f / 2048.0f);
  }
  if (t8 == 0) ((float*)(ws + WS_KSC))[d] = __uint_as_float((unsigned)(127 + ce - 14) << 23);
  __syncthreads();
  u4 hi, lo;
#pragma unroll
  for (int j = 0; j < 4; ++j) {
    if (KTERMS > 1) {
      unsigned hh, ll;
      split_h(*(const float*)(smem + kidx[2 * j]), *(const float*)(smem + kidx[2 * j + 1]), hh, ll);
      hi[j] = hh; lo[j] = ll;
    } else {
      hi[j] = pack_h(*(const float*)(smem + kidx[2 * j]), *(const float*)(smem + kidx[2 * j + 1]));
    }
  }
  st16(ws + WS_KH + ((size_t)d * SL + t8 * 8) * 2, hi);
  if (KTERMS > 1) st16(ws + WS_KL + ((size_t)d * SL + t8 * 8) * 2, lo);
}

constexpr int CV_U = 2064 * 2;
constexpr int CV_K = 2160 * 2;
constexpr int CV_OFF_K = 2 * CV_U;
constexpr int CV_OFF_Y = CV_OFF_K + 2 * KTERMS * CV_K;
constexpr int CV_Q = (SL + 4) * 4;
constexpr int CV_SMEM = CV_OFF_Y + 2 * 4 * CV_Q;

constexpr int CV_RPF = 2 + 2 * KTERMS;
constexpr int CV_R0 = (KTERMS == 2 ? 6 : 10) * CV_RPF;
constexpr int CV_NM = 68 * KTERMS;
struct CvStep { int a, n, k, first, op, arg, arg2; };
template <int SUB>
struct CvSched {
  CvStep s[CV_NM];
  int wt, wc, slack;
  constexpr CvSched() : s{}, wt(-1), wc(4), slack(1000) {
    const int AMAX = SUB == 0 ? 15 : 14;
    const int NR = (AMAX + 1) * CV_RPF;
    int nr = CV_R0, mi = 0;
    int endat[16] = {};
    for (int a = 0; a < 16; ++a) endat[a] = 1 << 20;
    for (int a = 0; a <= AMAX; ++a) {
      if ((((a + 1) >> 1) & 1) != SUB) continue;
      for (int nn = 0; nn <= a; ++nn) {
        const int n = (a == 0) ? 0 : (nn + 1 < a ? nn + 1 : (nn + 1 == a ? 0 : a));
        for (int k = 0; k < KTERMS; ++k) {
          CvStep st{a, n, k, (nn == 0 && k == 0) ? 1 : 0, 0, 0, 0};
          { const int need = ((n > a - n ? n : a - n) + 1) * CV_RPF; if (nr < NR && nr - need < slack) slack = nr - need; }
          ++mi;
          if (wc == 4) {
            for (int t = wt + 1; t < 16; ++t)
              if (endat[t] < (1 << 20)) { if (endat[t] + 8 <= mi) { wt = t; wc = 0; } break; }
          }
          if (wc < 4) { st.op = 2; st.arg = wt; st.arg2 = wc; ++wc; }
          else if (nr < NR) { st.op = 1; st.arg = nr; ++nr; }
          s[mi - 1] = st;
        }
      }
      endat[a] = mi;
    }
  }
};
template <int R>
__device__ __forceinline__ void cv_rd(s4 (&A)[16][KTERMS][2], s4 (&B)[16][2], const char* ub, const char* kb) {
  constexpr int i = R / CV_RPF, part = R % CV_RPF;
  if constexpr (part < 2) B[i][part] = tr_read(ub + 256 * i + 32 * part);
  else A[i][(part - 2) >> 1][part & 1] = tr_read(kb + ((part - 2) >> 1) * CV_K + 256 * i + 32 * (1 - (part & 1)));
}
template <int SUB, int I>
__device__ __forceinline__ void cv_step(s4 (&A)[16][KTERMS][2], s4 (&B)[16][2], f4 (&acc)[16], const char* ub, const char* kb, char* qb) {
  constexpr CvSched<SUB> S{};
  constexpr CvStep st = S.s[I];
  const s8 av = cat8(A[st.a - st.n][st.k][0], A[st.a - st.n][st.k][1]), bv = cat8(B[st.n][0], B[st.n][1]);
  if constexpr (st.first) asm volatile("v_mfma_f32_16x16x32_f16 %0, %1, %2, 0" : "=&v"(acc[st.a]) : "v"(av), "v"(bv));
  else asm volatile("v_mfma_f32_16x16x32_f16 %0, %1, %2, %0" : "+v"(acc[st.a]) : "v"(av), "v"(bv));
  if constexpr (st.op == 2) *(float*)(qb + st.arg2 * (CV_Q + 4) + st.arg * 512) = acc[st.arg][st.arg2];
  else if constexpr (st.op == 1) cv_rd<st.arg>(A, B, ub, kb);
  SCHED;
}
template <int SUB, int... Rs, int... Is>
__device__ __forceinline__ void conv_mfma_impl(const char* ub, const char* kb, char* qb, iseq<Rs...>, iseq<Is...>) {
  s4 A[16][KTERMS][2];
  s4 B[16][2];
  f4 acc[16];
  (cv_rd<Rs>(A, B, ub, kb), ...);
  SCHED;
  (cv_step<SUB, Is>(A, B, acc, ub, kb, qb), ...);
  asm volatile("s_nop 7");
  SCHED;
  constexpr CvSched<SUB> S{};
  static_assert(S.slack >= 4, "fragment read scheduled too late");
#pragma unroll
  for (int t = 0; t < 16; ++t) {
    if ((((t + 1) >> 1) & 1) != SUB) continue;
#pragma unroll
    for (int r = 0; r < 4; ++r)
      if (t > S.wt || (t == S.wt && r >= S.wc)) *(float*)(qb + r * (CV_Q + 4) + t * 512) = acc[t][r];
  }
}
template <int SUB>
__device__ __forceinline__ void conv_mfma(const char* ub, const char* kb, char* qb) {
  conv_mfma_impl<SUB>(ub, kb, qb, make_iseq<CV_R0>{}, make_iseq<CV_NM>{});
}

__global__ __launch_bounds__(512, 2) void conv_kernel(
    const u16* __restrict__ xT, const double* __restrict__ stats, const u16* __restrict__ Kh,
    const u16* __restrict__ Kl, const float* __restrict__ ksc, const u16* __restrict__ dskip,
    u16* __restrict__ outT) {
  extern __shared__ __attribute__((aligned(16))) char smem[];
  const int tid = threadIdx.x, lane = tid & 63;
  const int wid = __builtin_amdgcn_readfirstlane(tid >> 6);
  const int d0 = blockIdx.x * 2;
  const int t0 = tid * 4;
  u2 xr[2];
  u4 kr[KTERMS];
  d2 sr[4];
#pragma unroll
  for (int i = 0; i < 4; ++i) sr[i] = *(const d2*)(stats + t0 * 2 + i * 2);
#pragma unroll
  for (int c = 0; c < 2; ++c) xr[c] = *(const u2*)(xT + xoff(d0 + c, t0));
  kr[0] = *(const u4*)(Kh + (size_t)d0 * SL + tid * 8);
  if (KTERMS > 1) kr[KTERMS - 1] = *(const u4*)(Kl + (size_t)d0 * SL + tid * 8);
  const float2 isc2 = *(const float2*)(ksc + d0);
  const unsigned ds2 = *(const unsigned*)(dskip + d0);
  if (tid < 14 * 2 * KTERMS) {
    int which = tid / 14, j = tid % 14;
    *(u4*)(smem + CV_OFF_K + which * CV_K + j * 16) = u4{0, 0, 0, 0};
  } else if (tid >= 64 && tid < 70) {
    int c = (tid - 64) / 3, j = (tid - 64) % 3;
    *(u2*)(smem + c * CV_U + j * 8) = u2{0, 0};
  } else if (tid >= 128 && tid < 136) {
    *(f4*)(smem + CV_OFF_Y + (tid - 128) * CV_Q) = f4{0, 0, 0, 0};
  }
  float mb[4], ri[4];
#pragma unroll
  for (int j = 0; j < 4; ++j) ln_consts((float)sr[j][0], (float)sr[j][1], mb[j], ri[j]);
  u2 ub16[2];
#pragma unroll
  for (int c = 0; c < 2; ++c) {
    unsigned d01 = pack_bf(bflo(xr[c][0]) - mb[0], bfhi(xr[c][0]) - mb[1]);
    unsigned d23 = pack_bf(bflo(xr[c][1]) - mb[2], bfhi(xr[c][1]) - mb[3]);
    ub16[c][0] = pack_bf(bflo(d01) * ri[0], bfhi(d01) * ri[1]);
    ub16[c][1] = pack_bf(bflo(d23) * ri[2], bfhi(d23) * ri[3]);
    u2 a = {pack_h(bflo(ub16[c][0]), bfhi(ub16[c][0])), pack_h(bflo(ub16[c][1]), bfhi(ub16[c][1]))};
    *(u2*)(smem + c * CV_U + 24 + tid * 8) = a;
  }
#pragma unroll
  for (int k = 0; k < KTERMS; ++k)
    *(u4*)(smem + CV_OFF_K + ((tid >> 8) * KTERMS + k) * CV_K + 224 + (tid & 255) * 16) = kr[k];
  __syncthreads();
  {
    const int ch = wid >> 2, par = (wid >> 1) & 1;
    const int g = lane >> 4, q = (lane >> 2) & 3, p = lane & 3;
    const char* ub = smem + ch * CV_U + 8 * (p + q + 8 * g);
    const char* kb = smem + CV_OFF_K + ch * KTERMS * CV_K + 8 * (31 + 16 * par + 4 * p - 8 * g - q) - 32;
    char* qb = smem + CV_OFF_Y + ch * 4 * CV_Q + par * 256 + lane * 4;
    if (wid & 1) conv_mfma<1>(ub, kb, qb);
    else conv_mfma<0>(ub, kb, qb);
  }
  __syncthreads();
#pragma unroll
  for (int c = 0; c < 2; ++c) {
    const char* Q = smem + CV_OFF_Y + c * 4 * CV_Q + t0 * 4;
    f4 y0 = (*(const f4*)(Q) + *(const f4*)(Q + CV_Q)) + (*(const f4*)(Q + 2 * CV_Q) + *(const f4*)(Q + 3 * CV_Q));
    float isc = c ? isc2.y : isc2.x;
    float ds = c ? bfhi(ds2) : bflo(ds2);
    unsigned y01 = pack_bf(y0[0] * isc, y0[1] * isc), y23 = pack_bf(y0[2] * isc, y0[3] * isc);
    unsigned s01 = pack_bf(bflo(ub16[c][0]) * ds, bfhi(ub16[c][0]) * ds), s23 = pack_bf(bflo(ub16[c][1]) * ds, bfhi(ub16[c][1]) * ds);
    unsigned a01 = pack_bf(bflo(y01) + bflo(s01), bfhi(y01) + bfhi(s01)), a23 = pack_bf(bflo(y23) + bflo(s23), bfhi(y23) + bfhi(s23));
    u2 ov = {pack_bf(bflo(xr[c][0]) + bflo(a01), bfhi(xr[c][0]) + bfhi(a01)), pack_bf(bflo(xr[c][1]) + bflo(a23), bfhi(xr[c][1]) + bfhi(a23))};
    st8(outT + xoff(d0 + c, t0), ov);
  }
}

constexpr int G1_SMEM = 512 * 256 + 512 * 8;

__device__ __forceinline__ void glds16(const void* gsrc, unsigned lds_dst) {
  unsigned keep;
  asm volatile("s_mov_b32 %0, m0\n\ts_mov_b32 m0, %2\n\ts_nop 0\n\tglobal_load_lds_dwordx4 %1, off\n\ts_mov_b32 m0, %0"
               : "=&s"(keep) : "v"(gsrc), "s"(lds_dst) : "memory");
}
template <int OFF>
__device__ __forceinline__ void gload16(u4& dst, const void* p) {
  asm volatile("global_load_dwordx4 %0, %1, off offset:%2" : "=v"(dst) : "v"(p), "i"(OFF) : "memory");
}
template <int N>
__device__ __forceinline__ void wait_only(u4& a) {
  asm volatile("s_waitcnt vmcnt(%1)" : "+v"(a) : "i"(N) : "memory");
}
template <int N>
__device__ __forceinline__ void wait_vm() { asm volatile("s_waitcnt vmcnt(%0)" :: "i"(N) : "memory"); }
__device__ __forceinline__ void bar() { asm volatile("s_waitcnt lgkmcnt(0)\n\ts_barrier" ::: "memory"); }
constexpr int G1_ST = 4 * 32768;
constexpr int F_BPRE = 5;
constexpr int G1_WPF = 8;

template <int OFF>
__device__ __forceinline__ void gload16_sc1(u4& dst, const void* p) {
  asm volatile("global_load_dwordx4 %0, %1, off offset:%2 sc1" : "=v"(dst) : "v"(p), "i"(OFF) : "memory");
}
template <int N>
__device__ __forceinline__ void wait4(u4 (&a)[4]) {
  asm volatile("s_waitcnt vmcnt(%4)" : "+v"(a[0]), "+v"(a[1]), "+v"(a[2]), "+v"(a[3]) : "i"(N) : "memory");
}

template <bool FINAL>
__global__ __launch_bounds__(512, 2) void ffn_kernel(
    const u16* __restrict__ xT, const u16* __restrict__ W1F, const float* __restrict__ c1,
    u16* HF, float* pst, const u16* __restrict__ W2F, unsigned* cnt,
    u16* __restrict__ out, double* __restrict__ stats) {
  extern __shared__ __attribute__((aligned(16))) char smem[];
  const int tid = threadIdx.x, lane = tid & 63;
  const int wid = __builtin_amdgcn_readfirstlane(tid >> 6);
  const int bx = blockIdx.x, xj = bx & 7, r = bx >> 3;
  const int tp = 4 * (xj >> 1) + (r & 3);
  const int fp = 7 * (xj & 1) + (r >> 2);
  const int h = lane >> 5, blk = (lane >> 4) & 1, q = (lane >> 2) & 3, p = lane & 3;
  const char* rbase = smem + (8 * h + q) * 64 + blk * 32 + 8 * p;
  float* st = (float*)(smem + G1_ST);
  unsigned* cnt2 = cnt + 512;
  if (r >= 28) {
    const int id = (xj & 1) * 4 + (r - 28);
    const int sp = 4 * (xj >> 1) + (id >> 1), u = (id & 1) * 8 + wid;
    const char* xsrc = (const char*)xT + (size_t)sp * 131072 + u * 8192 + lane * 16;
    const unsigned ldst = (unsigned)(size_t)smem + wid * 8192;
#pragma unroll
    for (int k = 0; k < 8; ++k) glds16(xsrc + k * 1024, ldst + k * 1024);
    wait_vm<0>();
    const char* rq = rbase + wid * 8192;
    s4 fr[8][2];
    sfor<8>([&](auto I) AINL {
      constexpr int i = decltype(I)::value;
      fr[i][0] = tr_read(rq + i * 1024);
      fr[i][1] = tr_read(rq + i * 1024 + 256);
    });
    float sa = 0.f, qa = 0.f;
    sfor<8>([&](auto I) AINL {
      constexpr int i = decltype(I)::value;
      u4 xv = __builtin_bit_cast(u4, cat8(fr[i][0], fr[i][1]));
#pragma unroll
      for (int j = 0; j < 4; ++j) { sa = dot2(xv[j], BF_ONES, sa); qa = dot2(xv[j], xv[j], qa); }
    });
    sa += __shfl_xor(sa, 32);
    qa += __shfl_xor(qa, 32);
    if (lane < 32)
      st8(pst + ((sp * 128 + (u >> 2) * 32 + lane) * 4 + (u & 3)) * 2, u2{__float_as_uint(sa), __float_as_uint(qa)});
    wait_vm<0>();
    bar();
    if (tid == 0) __hip_atomic_fetch_add(cnt2 + sp * 32, 1u, __ATOMIC_RELAXED, __HIP_MEMORY_SCOPE_AGENT);
  } else {
  if (wid >= 4) {
    const int hq = wid - 4;
    const char* xsrc = (const char*)xT + (size_t)tp * 131072 + hq * 1024 + lane * 16;
    const unsigned ldst = (unsigned)(size_t)smem + hq * 1024;
    auto issue_c = [&](int c) AINL {
#pragma unroll
      for (int i = 0; i < 3; ++i) glds16(xsrc + c * 4096 + i * 32768, ldst + c * 4096 + i * 32768);
    };
    const unsigned* fp2 = cnt2 + tp * 32;
    const float* pp = pst + ((tp * 128 + hq * 32) * 4) * 2 + lane * 4;
    unsigned fl;
    u4 ps;
    issue_c(0); issue_c(1); issue_c(2);
    wait_vm<6>(); bar(); issue_c(3);
    wait_vm<6>(); bar(); issue_c(4);
    wait_vm<6>(); bar();
    asm volatile("global_load_dword %0, %1, off sc1" : "=v"(fl) : "v"(fp2) : "memory");
    issue_c(5);
    wait_vm<6>(); bar(); issue_c(6);
    asm volatile("s_waitcnt vmcnt(6)" : "+v"(fl) :: "memory");
    bar();
    const bool early = __builtin_amdgcn_readfirstlane(fl) >= 2u;
    gload16_sc1<0>(ps, pp);
    issue_c(7);
    wait_vm<6>(); bar();
#pragma unroll
    for (int k = 0; k < 8; ++k) glds16(xsrc + 3 * 32768 + k * 4096, ldst + 3 * 32768 + k * 4096);
    wait_vm<11>(); bar();
    wait_vm<8>(); bar();
    wait_only<0>(ps);
    if (!early) {
      for (int spin = 0; spin < (1 << 22); ++spin) {
        if (__hip_atomic_load(fp2, __ATOMIC_RELAXED, __HIP_MEMORY_SCOPE_AGENT) >= 2u) break;
        __builtin_amdgcn_s_sleep(2);
      }
      u4 p2;
      gload16_sc1<0>(p2, pp);
      wait_only<0>(p2);
      ps = p2;
    }
    float s0 = __uint_as_float(ps[0]) + __uint_as_float(ps[2]);
    s0 += __shfl_xor(s0, 1);
    if ((lane & 1) == 0) st[hq * 32 + (lane >> 1)] = -rbf(s0 * (1.0f / DM));
    bar();
  } else {
  const int ft = fp * 4 + wid;
  u4 wf[32];
  u4 cfu[4];
  const char* wsrc = (const char*)W1F + ((size_t)ft * 32 * 64 + lane) * 16;
  auto issue_w = [&](auto S) AINL {
    constexpr int s = decltype(S)::value;
    gload16<(s & 3) * 1024>(wf[s], wsrc + (s >> 2) * 4096);
  };
  sfor<G1_WPF>(issue_w);
  f16v acc[4];
  s4 xa[3][3][2];
  constexpr int XD = 4, XR = XD + 1;
  s4 xc[XR][2];
  u4 ov;
  float nm[4];
  const int w3 = wid == 3 ? 0 : wid;
  int tto[3];
#pragma unroll
  for (int i = 0; i < 3; ++i) { int tt = i + w3; tto[i] = tt >= 3 ? tt - 3 : tt; }
  const char* rb[3];
#pragma unroll
  for (int i = 0; i < 3; ++i) rb[i] = rbase + tto[i] * 32768;
  u16* dst = HF + ((size_t)((tp * 8 + blk) * 56 + ft) * 64 + (lane & 15) + 16 * h) * 8;
  auto epi_pair = [&](const f16v& a, float m, int e) AINL -> unsigned {
    f4 cf = __builtin_bit_cast(f4, cfu[e >> 2]);
    unsigned pu = pack_bf(fmaf(cf[e & 3], m, a[e]), fmaf(cf[(e & 3) + 1], m, a[e + 1]));
    asm("" : "+v"(pu));
    return __builtin_bit_cast(unsigned, __builtin_elementwise_max(__builtin_bit_cast(s2v, pu), s2v{0, 0}));
  };
  f16v z;
#pragma unroll
  for (int j = 0; j < 16; ++j) z[j] = 0.f;
  SCHED;
  asm volatile("s_barrier" ::: "memory");
#pragma unroll
  for (int k = 0; k < 2; ++k)
#pragma unroll
    for (int i = 0; i < 3; ++i) { xa[k][i][0] = tr_read(rb[i] + k * 1024); xa[k][i][1] = tr_read(rb[i] + k * 1024 + 256); }
  SCHED;
  sfor<32>([&](auto S) AINL {
    constexpr int s = decltype(S)::value;
    constexpr int issued = (G1_WPF + s <= 32) ? G1_WPF + s : 36;
    wait_only<issued - s - 1>(wf[s]);
    if constexpr ((s & 3) == 2 && s < 30) asm volatile("s_barrier" ::: "memory");
    SCHED;
    sfor<3>([&](auto I) AINL {
      constexpr int i = decltype(I)::value;
      const b8 xv = __builtin_bit_cast(b8, cat8(xa[s % 3][i][0], xa[s % 3][i][1]));
      acc[i] = __builtin_amdgcn_mfma_f32_32x32x16_bf16(__builtin_bit_cast(b8, wf[s]), xv, s == 0 ? z : acc[i], 0, 0, 0);
      if constexpr (s + 2 < 32) {
        xa[(s + 2) % 3][i][0] = tr_read(rb[i] + (s + 2) * 1024);
        xa[(s + 2) % 3][i][1] = tr_read(rb[i] + (s + 2) * 1024 + 256);
      }
      if constexpr (i == 0) {
        if constexpr (G1_WPF + s < 32) issue_w(ic<(G1_WPF + s < 32) ? G1_WPF + s : 0>{});
        if constexpr (G1_WPF + s == 32) {
          gload16<0>(cfu[0], c1 + ft * 32 + 4 * h);
          gload16<32>(cfu[1], c1 + ft * 32 + 4 * h);
          gload16<64>(cfu[2], c1 + ft * 32 + 4 * h);
          gload16<96>(cfu[3], c1 + ft * 32 + 4 * h);
        }
      }
      SCHED;
    });
  });
  asm volatile("" : "+v"(acc[0]), "+v"(acc[1]), "+v"(acc[2]));
  asm volatile("s_waitcnt vmcnt(0) lgkmcnt(0)\n\ts_barrier" : "+v"(cfu[0]), "+v"(cfu[1]), "+v"(cfu[2]), "+v"(cfu[3]) :: "memory");
  {
    unsigned rbo = (unsigned)(rbase - smem) + 3 * 32768;
    asm volatile("" : "+v"(rbo));
    const char* rbb = smem + rbo;
    sfor<XD>([&](auto I) AINL {
      constexpr int i = decltype(I)::value;
      xc[i][0] = tr_read(rbb + i * 1024); xc[i][1] = tr_read(rbb + i * 1024 + 256);
    });
#pragma unroll
    for (int i = 0; i < 3; ++i) nm[i] = st[tto[i] * 32 + (lane & 31)];
    nm[3] = st[96 + (lane & 31)];
    SCHED;
    sfor<32>([&](auto S) AINL {
      constexpr int s = decltype(S)::value;
      const b8 xv = __builtin_bit_cast(b8, cat8(xc[s % XR][0], xc[s % XR][1]));
      acc[3] = __builtin_amdgcn_mfma_f32_32x32x16_bf16(__builtin_bit_cast(b8, wf[s]), xv, s == 0 ? z : acc[3], 0, 0, 0);
      if constexpr (s + XD < 32) {
        xc[(s + XD) % XR][0] = tr_read(rbb + (s + XD) * 1024);
        xc[(s + XD) % XR][1] = tr_read(rbb + (s + XD) * 1024 + 256);
      }
      if constexpr (s < 24) {
        constexpr int i = s >> 3, pr = s & 7;
        ov[pr & 3] = epi_pair(acc[i], nm[i], 2 * pr);
        if constexpr ((pr & 3) == 3) st16(dst + tto[i] * (2 * 56 * 512) + (pr >> 2) * 256, ov);
      }
      SCHED;
    });
  }
#pragma unroll
  for (int c = 0; c < 2; ++c) {
#pragma unroll
    for (int j = 0; j < 4; ++j) ov[j] = epi_pair(acc[3], nm[3], 8 * c + 2 * j);
    st16(dst + 3 * (2 * 56 * 512) + c * 256, ov);
  }
  }
  }
  const int tb = 8 * (xj >> 1) + (r & 7), db = 4 * (xj & 1) + (r >> 3);
  const int g = lane >> 4, n = lane & 15;
  const int mo = wid >> 1, no = 2 * (wid & 1);
  u4 bf[7][4], af[7][4];
  {
    const char* bp[4];
#pragma unroll
    for (int j = 0; j < 4; ++j) bp[j] = (const char*)W2F + ((size_t)((db * 4 + ((j + no) & 3)) * 56 + wid * 7) * 64 + lane) * 16;
    auto issue_b = [&](auto KS) AINL {
      constexpr int ks = decltype(KS)::value;
      sfor<4>([&](auto J) AINL {
        constexpr int j = decltype(J)::value;
        gload16<(ks & 3) * 1024>(bf[ks][j], bp[j] + (ks >> 2) * 4096);
      });
    };
    wait_vm<0>();
    bar();
    if (r < 28 && tid == 0) __hip_atomic_fetch_add(cnt + tp * 32, 1u, __ATOMIC_RELAXED, __HIP_MEMORY_SCOPE_AGENT);
    if (wid == 7) {
      const unsigned* cp = cnt + (tb >> 1) * 32;
      for (int spin = 0; spin < (1 << 22); ++spin) {
        if (__hip_atomic_load(cp, __ATOMIC_RELAXED, __HIP_MEMORY_SCOPE_AGENT) >= 14u) break;
        __builtin_amdgcn_s_sleep(2);
      }
    }
    sfor<F_BPRE>(issue_b);
    bar();
    sfor<7 - F_BPRE>([&](auto I) AINL { issue_b(ic<F_BPRE + decltype(I)::value>{}); });
  }
  u2 res[2];
  u16 resf[2][4];
  if (!FINAL) {
#pragma unroll
    for (int j = 0; j < 2; ++j) res[j] = *(const u2*)(xT + xoff(db * 64 + (no + j) * 16 + n, tb * 64 + mo * 16 + 4 * g));
  } else {
#pragma unroll
    for (int j = 0; j < 2; ++j)
#pragma unroll
      for (int e = 0; e < 4; ++e) resf[j][e] = xT[xoff(db * 64 + (no + j) * 16 + 4 * g + e, tb * 64 + mo * 16 + n)];
  }
  const unsigned long long psu = __hip_atomic_load((const unsigned long long*)(pst + ((tb * 64 + mo * 16 + n) * 4 + g) * 2), __ATOMIC_RELAXED, __HIP_MEMORY_SCOPE_AGENT);
  const char* ap[4];
#pragma unroll
  for (int i = 0; i < 4; ++i) ap[i] = (const char*)HF + ((size_t)((tb * 4 + ((i + mo) & 3)) * 56 + wid * 7) * 64 + lane) * 16;
  auto issue_a = [&](auto KS) AINL {
    constexpr int ks = decltype(KS)::value;
    sfor<4>([&](auto I) AINL {
      constexpr int i = decltype(I)::value;
      gload16_sc1<(ks & 3) * 1024>(af[ks][i], ap[i] + (ks >> 2) * 4096);
    });
  };
  sfor<4>(issue_a);
  f4 acc[4][4];
#pragma unroll
  for (int i = 0; i < 4; ++i)
#pragma unroll
    for (int j = 0; j < 4; ++j) acc[i][j] = f4{0, 0, 0, 0};
  SCHED;
  sfor<7>([&](auto KS) AINL {
    constexpr int ks = decltype(KS)::value;
    wait4<(ks < 4 ? 12 : 4 * (6 - ks))>(af[ks]);
    wait4<63>(bf[ks]);
    SCHED;
#pragma unroll
    for (int i = 0; i < 4; ++i)
#pragma unroll
      for (int j = 0; j < 4; ++j) {
        if (!FINAL) acc[i][j] = __builtin_amdgcn_mfma_f32_16x16x32_bf16(__builtin_bit_cast(b8, af[ks][i]), __builtin_bit_cast(b8, bf[ks][j]), acc[i][j], 0, 0, 0);
        else acc[i][j] = __builtin_amdgcn_mfma_f32_16x16x32_bf16(__builtin_bit_cast(b8, bf[ks][j]), __builtin_bit_cast(b8, af[ks][i]), acc[i][j], 0, 0, 0);
      }
    SCHED;
    if constexpr (ks + 4 < 7) issue_a(ic<(ks + 4 < 7) ? ks + 4 : 0>{});
    SCHED;
  });
  f4* red = (f4*)smem;
#pragma unroll
  for (int i = 0; i < 4; ++i)
#pragma unroll
    for (int jp = 0; jp < 2; ++jp) {
      if (i == 0 && jp == 0) continue;
      const int owner = ((i + mo) & 3) * 2 + ((jp + wid) & 1);
      red[((owner * 7 + i * 2 + jp - 1) * 2 + 0) * 64 + lane] = acc[i][2 * jp];
      red[((owner * 7 + i * 2 + jp - 1) * 2 + 1) * 64 + lane] = acc[i][2 * jp + 1];
    }
  f4 rr;
  {
    float sx = __uint_as_float((unsigned)psu), sy = __uint_as_float((unsigned)(psu >> 32));
    sx += __shfl_xor(sx, 16); sy += __shfl_xor(sy, 16);
    sx += __shfl_xor(sx, 32); sy += __shfl_xor(sy, 32);
    float mb, ri;
    ln_consts(sx, sy, mb, ri);
    if (FINAL) rr = f4{ri, ri, ri, ri};
    else {
#pragma unroll
      for (int e = 0; e < 4; ++e) rr[e] = __shfl(ri, 4 * g + e);
    }
  }
  bar();
  char* ot = smem + 114688;
  float s[4] = {0, 0, 0, 0}, ss[4] = {0, 0, 0, 0};
#pragma unroll
  for (int j = 0; j < 2; ++j) {
    const f4* rp = red + (wid * 14 + j) * 64 + lane;
    f4 v = ((acc[0][j] + rp[0]) + (rp[128] + rp[256])) + ((rp[384] + rp[512]) + (rp[640] + rp[768]));
    v *= rr;
    unsigned m01 = pack_bf(v[0], v[1]), m23 = pack_bf(v[2], v[3]);
    u2 ov;
    if (!FINAL) {
      ov[0] = pack_bf(bflo(res[j][0]) + bflo(m01), bfhi(res[j][0]) + bfhi(m01));
      ov[1] = pack_bf(bflo(res[j][1]) + bflo(m23), bfhi(res[j][1]) + bfhi(m23));
      *(u2*)(ot + ((mo >> 1) * 64 + (no + j) * 16 + n) * 72 + ((mo & 1) * 16 + 4 * g) * 2) = ov;
      float o[4] = {bflo(ov[0]), bfhi(ov[0]), bflo(ov[1]), bfhi(ov[1])};
#pragma unroll
      for (int e = 0; e < 4; ++e) { s[e] += o[e]; ss[e] = fmaf(o[e], o[e], ss[e]); }
    } else {
      ov[0] = pack_bf(bf2f(resf[j][0]) + bflo(m01), bf2f(resf[j][1]) + bfhi(m01));
      ov[1] = pack_bf(bf2f(resf[j][2]) + bflo(m23), bf2f(resf[j][3]) + bfhi(m23));
      *(u2*)(ot + (mo * 16 + n) * 144 + ((no + j) * 16 + 4 * g) * 2) = ov;
    }
  }
  if (!FINAL) {
#pragma unroll
    for (int e = 0; e < 4; ++e) { s[e] = row_sum16(s[e]); ss[e] = row_sum16(ss[e]); }
    if (n < 8) {
      int e = n >> 1;
      float v = (n & 1) ? (e == 0 ? ss[0] : e == 1 ? ss[1] : e == 2 ? ss[2] : ss[3]) : (e == 0 ? s[0] : e == 1 ? s[1] : e == 2 ? s[2] : s[3]);
      unsafeAtomicAdd(stats + (tb * 64 + mo * 16 + 4 * g) * 2 + n, (double)v);
    }
  }
  bar();
  if (!FINAL) {
    const int row = tid >> 2, c = tid & 3;
    const u2 lo = *(const u2*)(ot + row * 72 + c * 16), hi = *(const u2*)(ot + row * 72 + c * 16 + 8);
    st16(out + xoff(db * 64 + (row & 63), tb * 64 + (row >> 6) * 32 + c * 8), u4{lo[0], lo[1], hi[0], hi[1]});
  } else {
    const int row = tid >> 3, c = tid & 7;
    st16(out + (size_t)(tb * 64 + row) * DM + db * 64 + c * 8, *(const u4*)(ot + row * 144 + c * 16));
  }
}

extern "C" void kernel_launch(void* const* d_in, const int* in_sizes, int n_in,
                              void* d_out, int out_size, void* d_ws, size_t ws_size,
                              hipStream_t stream) {
  const u16* x = (const u16*)d_in[0];
  const u16* logA = (const u16*)d_in[1];
  const u16* Aim = (const u16*)d_in[2];
  const u16* Cre = (const u16*)d_in[3];
  const u16* Cim = (const u16*)d_in[4];
  const u16* logdt = (const u16*)d_in[5];
  const u16* dskip = (const u16*)d_in[6];
  const u16* w1 = (const u16*)d_in[8];
  const u16* w2 = (const u16*)d_in[9];
  char* ws = (char*)d_ws;
  u16* out = (u16*)d_out;
  (void)hipFuncSetAttribute((const void*)ffn_kernel<false>, hipFuncAttributeMaxDynamicSharedMemorySize, G1_SMEM);
  (void)hipFuncSetAttribute((const void*)ffn_kernel<true>, hipFuncAttributeMaxDynamicSharedMemorySize, G1_SMEM);
  (void)hipFuncSetAttribute((const void*)conv_kernel, hipFuncAttributeMaxDynamicSharedMemorySize, CV_SMEM);

  prep_kernel<<<256 + PREP_PF + PREP_W1 + PREP_W2 + PREP_TR, 512, 0, stream>>>(x, logA, Aim, Cre, Cim, logdt, w1, w2, ws);
  conv_kernel<<<256, 512, CV_SMEM, stream>>>((const u16*)(ws + WS_XT0), (const double*)(ws + WS_ST0), (const u16*)(ws + WS_KH),
                                             (const u16*)(ws + WS_KL), (const float*)(ws + WS_KSC), dskip, (u16*)(ws + WS_XT1));
  ffn_kernel<false><<<256, 512, G1_SMEM, stream>>>((const u16*)(ws + WS_XT1), (const u16*)(ws + WS_W1F), (const float*)(ws + WS_C1),
                                                   (u16*)(ws + WS_HF), (float*)(ws + WS_PST), (const u16*)(ws + WS_W2F),
                                                   (unsigned*)(ws + WS_CNT), (u16*)(ws + WS_XT2), (double*)(ws + WS_ST2));
  conv_kernel<<<256, 512, CV_SMEM, stream>>>((const u16*)(ws + WS_XT2), (const double*)(ws + WS_ST2), (const u16*)(ws + WS_KH),
                                             (const u16*)(ws + WS_KL), (const float*)(ws + WS_KSC), dskip, (u16*)(ws + WS_XT3));
  ffn_kernel<true><<<256, 512, G1_SMEM, stream>>>((const u16*)(ws + WS_XT3), (const u16*)(ws + WS_W1F), (const float*)(ws + WS_C1),
                                                  (u16*)(ws + WS_HF2), (float*)(ws + WS_PST2), (const u16*)(ws + WS_W2F),
                                                  (unsigned*)(ws + WS_CNT) + 1024, out, nullptr);
}
```
